```python
import torch

def _softmax(x, axis=-1):
    return torch.softmax(x, dim=axis)
def _gelu(x):
    return torch.nn.functional.gelu(x, approximate="tanh")
def _silu(x):
    return torch.nn.functional.silu(x)
def _sigmoid(x):
    return torch.sigmoid(x)
def _layernorm(x, axis=-1, eps=1e-5):
    m = x.mean(dim=axis, keepdim=True)
    v = x.var(dim=axis, keepdim=True, unbiased=False)
    return (x - m) / torch.sqrt(v + eps)
def _scatter_add(tgt, idx, src, axis=0):
    return tgt.clone().index_add_(axis, idx, src)
def _segment_sum(src, seg, n):
    out = torch.zeros((n,) + src.shape[1:], dtype=src.dtype)
    return out.index_add_(0, seg, src)
def _pad(x, pw):
    flat = tuple(v for pair in reversed(pw) for v in pair)
    return torch.nn.functional.pad(x, flat)

kernel_name = 'parallel_sum__mha_block_cached__geglu_ffn__decode'

def setup_inputs():

    _w = (lambda a: (lambda t: t.to(torch.bfloat16) if t.is_floating_point() else t)(torch.as_tensor(a)))
    def _A_setup():
        B, Sq, D = 1, 1, 1024
        H, Sk, Dh = 32, 32768, 128
        return {
            "x": _w((lambda *s: torch.randn(*s, dtype=torch.bfloat16) * 0.05)(B, Sq, D)),
            "k_cache": _w((lambda *s: torch.randn(*s, dtype=torch.bfloat16) * 3.2)(B, H, Sk, Dh)),
            "v_cache": _w((lambda *s: torch.randn(*s, dtype=torch.bfloat16) * 1.6)(B, H, Sk, Dh)),
            "wq": _w((lambda *s: torch.randn(*s, dtype=torch.bfloat16) * 3.2)(D, H * Dh) * (D ** -0.5)),
            "wo": _w((lambda *s: torch.randn(*s, dtype=torch.bfloat16) * 1.6)(H * Dh, D) * ((H * Dh) ** -0.5)),
        }
    _A_in = _A_setup()

    def _B_setup():
        B, S, D, F = 1, 1, 1024, 3584
        return {
            "x": _w((lambda *s: torch.randn(*s, dtype=torch.bfloat16) * 0.05)(B, S, D)),
            "w_gate": _w((lambda *s: torch.randn(*s, dtype=torch.bfloat16) * 0.05)(D, F)),
            "w_up": _w((lambda *s: torch.randn(*s, dtype=torch.bfloat16) * 0.05)(D, F)),
            "w_down": _w((lambda *s: torch.randn(*s, dtype=torch.bfloat16) * 0.05)(F, D)),
        }
    _B_in = _B_setup()

    return {
        "A_x": _A_in["x"],
        "A_k_cache": _A_in["k_cache"],
        "A_v_cache": _A_in["v_cache"],
        "A_wq": _A_in["wq"],
        "A_wo": _A_in["wo"],
        "B_x": _B_in["x"],
        "B_w_gate": _B_in["w_gate"],
        "B_w_up": _B_in["w_up"],
        "B_w_down": _B_in["w_down"],
    }

def reference(A_x, A_k_cache, A_v_cache, A_wq, A_wo, B_x, B_w_gate, B_w_up, B_w_down):
    def _A_ref(x, k_cache, v_cache, wq, wo):
        B, Sq, D = x.shape
        H, Dh = 32, 128
        q = (lambda x, *d: x.permute(*d))((x @ wq).reshape(B, Sq, H, Dh), 0, 2, 1, 3)
        scores = torch.einsum("bhqd,bhkd->bhqk", q, k_cache) * (Dh ** -0.5)
        p = _softmax(scores, axis=-1)
        o = torch.einsum("bhqk,bhkd->bhqd", p, v_cache)
        return (lambda x, *d: x.permute(*d))(o, 0, 2, 1, 3).reshape(B, Sq, H * Dh) @ wo

    def _B_ref(x, w_gate, w_up, w_down):
        return (_gelu(x @ w_gate) * (x @ w_up)) @ w_down

    x = A_x
    xn = _layernorm(x, axis=-1)
    return x + _A_ref(xn, A_k_cache, A_v_cache, A_wq, A_wo) + _B_ref(xn, B_w_gate, B_w_up, B_w_down)
```

```cpp
#include <hip/hip_runtime.h>
#include <hip/hip_bf16.h>
#include <stdint.h>

#define MIMIC 1

typedef unsigned short u16;
typedef unsigned u32x4 __attribute__((ext_vector_type(4)));
typedef __attribute__((ext_vector_type(2))) __bf16 bf16x2;

constexpr int D = 1024, H = 32, SK = 32768, DH = 128, F = 3584;
constexpr int NSPLIT = 8;

__device__ __forceinline__ float bf2f(u16 v) { return __uint_as_float((unsigned)v << 16); }
__device__ __forceinline__ float rbf(float x) {
  __hip_bfloat16 b = __float2bfloat16(x);
  return __bfloat162float(b);
}
__device__ __forceinline__ float lo(unsigned u) { return __uint_as_float(u << 16); }
__device__ __forceinline__ float hi(unsigned u) { return __uint_as_float(u & 0xffff0000u); }

__device__ void layernorm(const u16* x, float* xn, float* red) {
  int t = threadIdx.x;
  float v[4];
  float s = 0;
  for (int i = 0; i < 4; i++) { v[i] = bf2f(x[t * 4 + i]); s += v[i]; }
  for (int m = 32; m >= 1; m >>= 1) s += __shfl_xor(s, m);
  if ((t & 63) == 0) red[t >> 6] = s;
  __syncthreads();
  float mean = (red[0] + red[1] + red[2] + red[3]) * (1.0f / D);
  __syncthreads();
  float q = 0;
  for (int i = 0; i < 4; i++) { float d = v[i] - mean; q += d * d; }
  for (int m = 32; m >= 1; m >>= 1) q += __shfl_xor(q, m);
  if ((t & 63) == 0) red[t >> 6] = q;
  __syncthreads();
  float var = (red[0] + red[1] + red[2] + red[3]) * (1.0f / D);
  float mb = rbf(mean), vb = rbf(var);
  float den = rbf(sqrtf(rbf(vb + 1e-5f)));
  for (int i = 0; i < 4; i++) xn[t * 4 + i] = rbf(rbf(v[i] - mb) / den);
  __syncthreads();
}

__global__ void __launch_bounds__(256) k1(const u16* x, const u16* wq, const u16* wg, const u16* wu, float* q, float* h) {
  __shared__ float xn[D];
  __shared__ float red[4];
  __shared__ float part[2][32][64];
  layernorm(x, xn, red);
  int b = blockIdx.x, t = threadIdx.x;
  int c = t & 7, r = t >> 3;
  if (b < 64) {
    float acc[8] = {0};
    for (int k = r; k < D; k += 32) {
      u32x4 w = *(const u32x4*)(wq + (size_t)k * 4096 + b * 64 + c * 8);
      float xv = xn[k];
      for (int j = 0; j < 4; j++) { acc[2 * j] += xv * lo(w[j]); acc[2 * j + 1] += xv * hi(w[j]); }
    }
    for (int j = 0; j < 8; j++) part[0][r][c * 8 + j] = acc[j];
    __syncthreads();
    if (t < 64) { float s = 0; for (int i = 0; i < 32; i++) s += part[0][i][t];
      s = rbf(s);
      q[b * 64 + t] = s; }
  } else {
    int bb = b - 64;
    float ag[8] = {0}, au[8] = {0};
    for (int k = r; k < D; k += 32) {
      u32x4 w = *(const u32x4*)(wg + (size_t)k * F + bb * 64 + c * 8);
      u32x4 w2 = *(const u32x4*)(wu + (size_t)k * F + bb * 64 + c * 8);
      float xv = xn[k];
      for (int j = 0; j < 4; j++) { ag[2 * j] += xv * lo(w[j]); ag[2 * j + 1] += xv * hi(w[j]); au[2 * j] += xv * lo(w2[j]); au[2 * j + 1] += xv * hi(w2[j]); }
    }
    for (int j = 0; j < 8; j++) { part[0][r][c * 8 + j] = ag[j]; part[1][r][c * 8 + j] = au[j]; }
    __syncthreads();
    if (t < 64) {
      float g = 0, u = 0;
      for (int i = 0; i < 32; i++) { g += part[0][i][t]; u += part[1][i][t]; }
      g = rbf(g); u = rbf(u);
      float inner = 0.7978845608028654f * (g + 0.044715f * g * g * g);
      float ge = 0.5f * g * (1.f + tanhf(inner));
      ge = rbf(ge);
      h[bb * 64 + t] = rbf(ge * u);
    }
  }
}

__global__ void __launch_bounds__(256) k2(const u16* kc, const u16* vc, const float* q, float* pm, float* pl, float* po) {
  __shared__ float sm[4][4], sl[4][4], so[4][4][DH];
  int b = blockIdx.x, h = b / NSPLIT, s = b % NSPLIT;
  int t = threadIdx.x, w = t >> 6, lane = t & 63, r = lane >> 4, c = lane & 15;
  float qv[8];
  for (int j = 0; j < 8; j++) qv[j] = q[h * DH + c * 8 + j];
  const int KPS = SK / NSPLIT;
  const u16* kb = kc + ((size_t)h * SK + s * KPS) * DH;
  const u16* vb = vc + ((size_t)h * SK + s * KPS) * DH;
  float m = -INFINITY, l = 0, acc[8] = {0};
  for (int k0 = w * 4 + r; k0 < KPS; k0 += 16) {
    u32x4 kk = __builtin_nontemporal_load((const u32x4*)(kb + (size_t)k0 * DH + c * 8));
    u32x4 vv = __builtin_nontemporal_load((const u32x4*)(vb + (size_t)k0 * DH + c * 8));
    float d = 0;
    for (int j = 0; j < 4; j++) { d += qv[2 * j] * lo(kk[j]); d += qv[2 * j + 1] * hi(kk[j]); }
    for (int mm = 8; mm >= 1; mm >>= 1) d += __shfl_xor(d, mm);
    d = rbf(rbf(d) * 0.08838834764831845f);
    float mn = fmaxf(m, d);
    float a = __expf(m - mn), p = __expf(d - mn);
    l = l * a + p;
    for (int j = 0; j < 4; j++) { acc[2 * j] = acc[2 * j] * a + p * lo(vv[j]); acc[2 * j + 1] = acc[2 * j + 1] * a + p * hi(vv[j]); }
    m = mn;
  }
  if (c == 0) { sm[w][r] = m; sl[w][r] = l; }
  for (int j = 0; j < 8; j++) so[w][r][c * 8 + j] = acc[j];
  __syncthreads();
  if (t < DH) {
    float M = -INFINITY;
    for (int i = 0; i < 16; i++) M = fmaxf(M, sm[i >> 2][i & 3]);
    float L = 0, o = 0;
    for (int i = 0; i < 16; i++) { float wt = __expf(sm[i >> 2][i & 3] - M); L += wt * sl[i >> 2][i & 3]; o += wt * so[i >> 2][i & 3][t]; }
    po[b * DH + t] = o;
    if (t == 0) { pm[b] = M; pl[b] = L; }
  }
}

__global__ void __launch_bounds__(256) k3(const u16* x, const u16* wo, const u16* wd, const float* pm, const float* pl, const float* po, const float* h, __hip_bfloat16* out) {
  __shared__ float o[H * DH];
  __shared__ float hs[F];
  __shared__ float part[2][32][64];
  int t = threadIdx.x, b = blockIdx.x;
  for (int i = t; i < H * DH; i += 256) {
    int hh = i / DH, d = i % DH;
    float M = -INFINITY;
    for (int s = 0; s < NSPLIT; s++) M = fmaxf(M, pm[hh * NSPLIT + s]);
    float L = 0, a = 0;
    for (int s = 0; s < NSPLIT; s++) { float wt = __expf(pm[hh * NSPLIT + s] - M); L += wt * pl[hh * NSPLIT + s]; a += wt * po[(hh * NSPLIT + s) * DH + d]; }
    o[i] = rbf(a / L);
  }
  for (int i = t; i < F; i += 256) hs[i] = h[i];
  __syncthreads();
  int c = t & 7, r = t >> 3;
  float acc[8] = {0}, acc2[8] = {0};
  for (int k = r; k < H * DH; k += 32) {
    u32x4 w = *(const u32x4*)(wo + (size_t)k * D + b * 64 + c * 8);
    float xv = o[k];
    for (int j = 0; j < 4; j++) { acc[2 * j] += xv * lo(w[j]); acc[2 * j + 1] += xv * hi(w[j]); }
  }
  for (int k = r; k < F; k += 32) {
    u32x4 w = *(const u32x4*)(wd + (size_t)k * D + b * 64 + c * 8);
    float xv = hs[k];
    for (int j = 0; j < 4; j++) { acc2[2 * j] += xv * lo(w[j]); acc2[2 * j + 1] += xv * hi(w[j]); }
  }
  for (int j = 0; j < 8; j++) { part[0][r][c * 8 + j] = acc[j]; part[1][r][c * 8 + j] = acc2[j]; }
  __syncthreads();
  if (t < 64) {
    float a = 0, f = 0;
    for (int i = 0; i < 32; i++) { a += part[0][i][t]; f += part[1][i][t]; }
    float xv = bf2f(x[b * 64 + t]);
    float res = rbf(xv + rbf(a)) + rbf(f);
    out[b * 64 + t] = __float2bfloat16(res);
  }
}

extern "C" void kernel_launch(void* const* d_in, const int* in_sizes, int n_in, void* d_out, int out_size, void* d_ws, size_t ws_size,
                              hipStream_t stream) {
  const u16* x = (const u16*)d_in[0];
  const u16* kc = (const u16*)d_in[1];
  const u16* vc = (const u16*)d_in[2];
  const u16* wq = (const u16*)d_in[3];
  const u16* wo = (const u16*)d_in[4];
  const u16* wg = (const u16*)d_in[6];
  const u16* wu = (const u16*)d_in[7];
  const u16* wd = (const u16*)d_in[8];
  float* ws = (float*)d_ws;
  float* q = ws;
  float* h = ws + 4096;
  float* pm = ws + 8192;
  float* pl = ws + 8192 + 256;
  float* po = ws + 16384;
  k1<<<64 + 56, 256, 0, stream>>>(x, wq, wg, wu, q, h);
  k2<<<H * NSPLIT, 256, 0, stream>>>(kc, vc, q, pm, pl, po);
  k3<<<16, 256, 0, stream>>>(x, wo, wd, pm, pl, po, h, (__hip_bfloat16*)d_out);
}
```

```cpp
#include <hip/hip_runtime.h>
#include <stdint.h>

namespace {

typedef unsigned u32;
typedef unsigned long long u64;
typedef unsigned short u16;
typedef u32 u32x4 __attribute__((ext_vector_type(4)));
typedef int i32x4 __attribute__((ext_vector_type(4)));
typedef float f32x2 __attribute__((ext_vector_type(2)));
typedef __attribute__((ext_vector_type(2))) __bf16 bf16x2;
#define GAS __attribute__((address_space(1)))
typedef GAS u64 gu64;
typedef GAS u32 gu32;

constexpr int D = 1024, NH = 32, SK = 32768, DH = 128, FF = 3584;
constexpr int NTILE = SK / 16;
#define SPW_ 48
constexpr int SPW = SPW_;
static_assert(NTILE == 2048 && SPW % 2 == 0 && SPW >= 4 && SPW <= 64, "");
constexpr int DYN0 = SPW * 32;
constexpr u32 HEAD_BYTES = (u32)SK * DH * 2;

constexpr size_t WS_EPOCH = 0;
constexpr size_t WS_CTR = 4096;
constexpr size_t WS_QREC = 16384;
constexpr size_t WS_S1 = WS_QREC + 256 * 144 * 8;
constexpr size_t WS_S2 = WS_S1 + 256 * 144 * 8;
constexpr size_t WS_GU = WS_S2 + 256 * 128 * 8;
constexpr size_t WS_FR = WS_GU + 224 * 256 * 8;
constexpr size_t WS_STAMP = WS_FR + 224 * 128 * 8;
constexpr u32 SPIN_CAP = 1u << 16;

__device__ __forceinline__ float bf_lo(u32 v) { return __uint_as_float(v << 16); }
__device__ __forceinline__ float bf_hi(u32 v) { return __uint_as_float(v & 0xffff0000u); }
__device__ __forceinline__ u32 cvt_pk(float lo, float hi) {
  bf16x2 r = {(__bf16)lo, (__bf16)hi};
  return __builtin_bit_cast(u32, r);
}
__device__ __forceinline__ float rbf(float x) { return bf_lo((u32)__builtin_bit_cast(u16, (__bf16)x)); }
__device__ __forceinline__ float dot2(u32 a, u32 b, float c) {
  return __builtin_amdgcn_fdot2_f32_bf16(__builtin_bit_cast(bf16x2, a), __builtin_bit_cast(bf16x2, b), c, false);
}
template <int CTRL> __device__ __forceinline__ float dpp(float x) {
  return __builtin_bit_cast(float, __builtin_amdgcn_mov_dpp(__builtin_bit_cast(int, x), CTRL, 0xf, 0xf, true));
}
constexpr int XOR1 = 0xB1, XOR2 = 0x4E, HMIRROR = 0x141, ROR8 = 0x128, ROR4 = 0x124, BCAST = 0x150;
__device__ __forceinline__ float swap32_add(float a, float b) {
  auto s = __builtin_amdgcn_permlane32_swap(__float_as_uint(a), __float_as_uint(b), false, false);
  return __uint_as_float(s[0]) + __uint_as_float(s[1]);
}
__device__ __forceinline__ float swap16_add(float a, float b) {
  auto s = __builtin_amdgcn_permlane16_swap(__float_as_uint(a), __float_as_uint(b), false, false);
  return __uint_as_float(s[0]) + __uint_as_float(s[1]);
}
__device__ __forceinline__ float xrow_sum(float x) { return swap32_add(swap16_add(x, x), swap16_add(x, x)); }
__device__ __forceinline__ float xrow_max(float x) {
  auto s = __builtin_amdgcn_permlane16_swap(__float_as_uint(x), __float_as_uint(x), false, false);
  x = fmaxf(__uint_as_float(s[0]), __uint_as_float(s[1]));
  auto t = __builtin_amdgcn_permlane32_swap(__float_as_uint(x), __float_as_uint(x), false, false);
  return fmaxf(__uint_as_float(t[0]), __uint_as_float(t[1]));
}
__device__ __forceinline__ float row_sum(float x) { x += dpp<XOR1>(x); x += dpp<XOR2>(x); x += dpp<ROR4>(x); x += dpp<ROR8>(x); return x; }
__device__ __forceinline__ float wave_sum(float x) { return xrow_sum(row_sum(x)); }
__device__ __forceinline__ f32x2 fold_rows(const float (&a)[8]) {
  float s0 = swap32_add(a[0], a[4]), s1 = swap32_add(a[1], a[5]), s2 = swap32_add(a[2], a[6]), s3 = swap32_add(a[3], a[7]);
  return f32x2{swap16_add(s0, s2), swap16_add(s1, s3)};
}

__device__ __forceinline__ i32x4 mkr(const void* p, u32 n) {
  u64 a = (u64)p;
  i32x4 r; r.x = (int)(u32)a; r.y = (int)((u32)(a >> 32) & 0xffffu); r.z = (int)n; r.w = 0x00020000; return r;
}
template <int IMM> __device__ __forceinline__ void ld_nt(u32x4& d, u32 voff, i32x4 r, u32 soff) {
  asm volatile("buffer_load_dwordx4 %0, %1, %2, %3 offen offset:%4 nt" : "+v"(d) : "v"(voff), "s"(r), "s"(soff), "i"(IMM));
}
template <int IMM> __device__ __forceinline__ void ld_nt_o(u32x4& d, u32 voff, i32x4 r, u32 soff) {
  asm volatile("buffer_load_dwordx4 %0, %1, %2, %3 offen offset:%4 nt" : "=v"(d) : "v"(voff), "s"(r), "s"(soff), "i"(IMM));
}
__device__ __forceinline__ void issue4(u32x4 (&R)[4], u32 voff, i32x4 r, u32 soff) {
  ld_nt<0>(R[0], voff, r, soff); ld_nt<1024>(R[1], voff, r, soff); ld_nt<2048>(R[2], voff, r, soff); ld_nt<3072>(R[3], voff, r, soff);
}
__device__ __forceinline__ void issue4_o(u32x4 (&R)[4], u32 voff, i32x4 r, u32 soff) {
  ld_nt_o<0>(R[0], voff, r, soff); ld_nt_o<1024>(R[1], voff, r, soff); ld_nt_o<2048>(R[2], voff, r, soff); ld_nt_o<3072>(R[3], voff, r, soff);
}

__device__ __forceinline__ void issue4_or(u32x4 (&R)[4], u32 voff, i32x4 r, u32 soff, u32 alt, u32x4& W0, u32x4& W1, u32x4& W2, u32x4& W3,
                                          u32 wvo, i32x4 rw, u32 w0, u32 w1, u32 w2, u32 w3) {
  asm volatile(
      "s_cmp_lg_u32 %11, 0\n\t"
      "s_cbranch_scc1 1f\n\t"
      "buffer_load_dwordx4 %0, %8, %9, %10 offen nt\n\t"
      "buffer_load_dwordx4 %1, %8, %9, %10 offen offset:1024 nt\n\t"
      "buffer_load_dwordx4 %2, %8, %9, %10 offen offset:2048 nt\n\t"
      "buffer_load_dwordx4 %3, %8, %9, %10 offen offset:3072 nt\n\t"
      "s_branch 2f\n"
      "1:\n\t"
      "buffer_load_dwordx4 %4, %12, %13, %14 offen nt\n\t"
      "buffer_load_dwordx4 %5, %12, %13, %15 offen nt\n\t"
      "buffer_load_dwordx4 %6, %12, %13, %16 offen nt\n\t"
      "buffer_load_dwordx4 %7, %12, %13, %17 offen nt\n"
      "2:"
      : "+v"(R[0]), "+v"(R[1]), "+v"(R[2]), "+v"(R[3]), "+v"(W0), "+v"(W1), "+v"(W2), "+v"(W3)
      : "v"(voff), "s"(r), "s"(soff), "s"(alt), "v"(wvo), "s"(rw), "s"(w0), "s"(w1), "s"(w2), "s"(w3)
      : "scc");
}
template <int N> __device__ __forceinline__ void wait4(u32x4 (&R)[4]) {
  asm volatile("s_waitcnt vmcnt(%4)" : "+v"(R[0]), "+v"(R[1]), "+v"(R[2]), "+v"(R[3]) : "i"(N));
}
template <int N> __device__ __forceinline__ void wait8(u32x4 (&R)[8]) {
  asm volatile("s_waitcnt vmcnt(%8)" : "+v"(R[0]), "+v"(R[1]), "+v"(R[2]), "+v"(R[3]), "+v"(R[4]), "+v"(R[5]), "+v"(R[6]), "+v"(R[7]) : "i"(N));
}
template <int N> __device__ __forceinline__ void wait1(u32& t) { asm volatile("s_waitcnt vmcnt(%1)" : "+v"(t) : "i"(N)); }
__device__ __forceinline__ void ticket(u32& t, u32 aoff, i32x4 r) {
  asm volatile("v_mov_b32 %0, 1\n\tbuffer_atomic_add %0, %1, %2, 0 offen sc0" : "+v"(t) : "v"(aoff), "s"(r));
}
__device__ __forceinline__ void wg_barrier() { asm volatile("s_waitcnt lgkmcnt(0)\n\ts_barrier" ::: "memory"); }

__device__ __forceinline__ void put(gu64* g, u32 tag, float v) {
  __hip_atomic_store(g, ((u64)tag << 32) | (u64)__float_as_uint(v), __ATOMIC_RELAXED, __HIP_MEMORY_SCOPE_AGENT);
}
__device__ __forceinline__ u64 get(const gu64* g) { return __hip_atomic_load(g, __ATOMIC_RELAXED, __HIP_MEMORY_SCOPE_AGENT); }
__device__ __forceinline__ bool tag_ok(u64 x, u32 tag) { return (u32)(x >> 32) == tag; }
__device__ __forceinline__ float val(u64 x) { return __uint_as_float((u32)x); }

template <int LN> __device__ __forceinline__ void gemv_step(float (&acc)[8], float xv, u32x4 w) {
  const float xb = dpp<BCAST + LN>(xv);
#pragma unroll
  for (int k = 0; k < 4; k++) { acc[2 * k] = fmaf(xb, bf_lo(w[k]), acc[2 * k]); acc[2 * k + 1] = fmaf(xb, bf_hi(w[k]), acc[2 * k + 1]); }
}

struct Lds {
  float part[4][256];
  float qp[4][128];
  float ap[4][132];
  u32 q[64];
  u32 ctl[4];
};

#define STAMP(i) do {} while (0)

__global__ void __launch_bounds__(320, 2)
fused_decode(const u16* __restrict__ x, const u16* __restrict__ kc, const u16* __restrict__ vc, const u16* __restrict__ wq,
             const u16* __restrict__ wo, const u16* __restrict__ wg, const u16* __restrict__ wu, const u16* __restrict__ wd,
             u16* __restrict__ out, unsigned char* __restrict__ ws) {
  __shared__ Lds L;
  const int b = blockIdx.x, head = b >> 3, j = b & 7;
  const int lane = threadIdx.x & 63, wave = __builtin_amdgcn_readfirstlane(threadIdx.x >> 6);
  const int r = lane >> 4, c = lane & 15;
  const bool has_ffn = j != (head & 7);
  const int unit = has_ffn ? 7 * head + j - (j > (head & 7) ? 1 : 0) : 0;
  const int ug = unit >> 3, us = unit & 7;
  STAMP(0);

  if (wave < 4) {
    const int w = wave;
    u32x4 X0, X1; u32 xs;
    u32x4 WQ[8], WG[8], WU[8], AK[4], AV[4], BK[4], BV[4];
    u32 tkA = 0, tkB = 0;
    const i32x4 rx = mkr(x, D * 2);
    {
      const u32 xo = lane * 16;
      const int k7 = c & 7;
      const u32 so = (u32)(((c < 8 ? j : us) * 128 + 32 * w + 4 * k7 + r) * 2);
      asm volatile("buffer_load_dwordx4 %0, %1, %2, 0 offen" : "=v"(X0) : "v"(xo), "s"(rx));
      asm volatile("buffer_load_dwordx4 %0, %1, %2, 0 offen offset:1024" : "=v"(X1) : "v"(xo), "s"(rx));
      asm volatile("buffer_load_ushort %0, %1, %2, 0 offen" : "=v"(xs) : "v"(so), "s"(rx));
    }
    {
      const i32x4 rq = mkr(wq, (u32)D * 4096 * 2);
      const u32 vo = r * 8192 + c * 16, s0 = (u32)(j * 128 + 32 * w) * 8192 + head * 256;
#pragma unroll
      for (int i = 0; i < 8; i++) ld_nt_o<0>(WQ[i], vo, rq, s0 + i * 32768);
      const u32 n = has_ffn ? (u32)D * FF * 2 : 0u;
      const i32x4 rg = mkr(wg, n), ru = mkr(wu, n);
      const u32 vg = r * (FF * 2) + c * 16, g0 = (u32)(us * 128 + 32 * w) * (FF * 2) + ug * 256;
#pragma unroll
      for (int i = 0; i < 8; i++) ld_nt_o<0>(WG[i], vg, rg, g0 + i * (4 * FF * 2));
#pragma unroll
      for (int i = 0; i < 8; i++) ld_nt_o<0>(WU[i], vg, ru, g0 + i * (4 * FF * 2));
    }
    const char* kb = (const char*)kc + (size_t)head * HEAD_BYTES;
    const char* vb = (const char*)vc + (size_t)head * HEAD_BYTES;
    const u32 voff = lane * 16;
    const u32 aoff = lane == 0 ? 0u : 0x7ffffff0u;
    const unsigned char* ctr = ws + WS_CTR + head * 256;
    const int st0 = j * SPW * 4 + w;
    ticket(tkB, aoff, mkr(ctr, 0));
    issue4_o(AK, voff, mkr(kb, HEAD_BYTES), (u32)st0 * 4096); issue4_o(AV, voff, mkr(vb, HEAD_BYTES), (u32)st0 * 4096);
    ticket(tkA, aoff, mkr(ctr, 0));
    issue4_o(BK, voff, mkr(kb, HEAD_BYTES), (u32)(st0 + 4) * 4096); issue4_o(BV, voff, mkr(vb, HEAD_BYTES), (u32)(st0 + 4) * 4096);
    asm volatile("s_waitcnt vmcnt(42)" : "+v"(X0), "+v"(X1), "+v"(xs));
    float xn;
    {
      float v[16];
#pragma unroll
      for (int k = 0; k < 4; k++) { v[2 * k] = bf_lo(X0[k]); v[2 * k + 1] = bf_hi(X0[k]); v[8 + 2 * k] = bf_lo(X1[k]); v[9 + 2 * k] = bf_hi(X1[k]); }
      float s = 0.f;
#pragma unroll
      for (int k = 0; k < 16; k++) s += v[k];
      const float mean = wave_sum(s) * (1.0f / D);
      float q = 0.f;
#pragma unroll
      for (int k = 0; k < 16; k++) { const float d = v[k] - mean; q = fmaf(d, d, q); }
      const float var = wave_sum(q) * (1.0f / D);
      const float den = rbf(sqrtf(rbf(rbf(var) + 1e-5f)));
      xn = rbf(rbf(bf_lo(xs) - rbf(mean)) / den);
    }
    STAMP(1);
    {
      float a[8] = {0.f, 0.f, 0.f, 0.f, 0.f, 0.f, 0.f, 0.f};
      wait8<34>(WQ);
      gemv_step<0>(a, xn, WQ[0]); gemv_step<1>(a, xn, WQ[1]); gemv_step<2>(a, xn, WQ[2]); gemv_step<3>(a, xn, WQ[3]);
      gemv_step<4>(a, xn, WQ[4]); gemv_step<5>(a, xn, WQ[5]); gemv_step<6>(a, xn, WQ[6]); gemv_step<7>(a, xn, WQ[7]);
      *(f32x2*)&L.qp[w][8 * c + 2 * r] = fold_rows(a);
    }
    wg_barrier();
    STAMP(2);
    {
      float a[8] = {0.f, 0.f, 0.f, 0.f, 0.f, 0.f, 0.f, 0.f};
      wait8<26>(WG);
      gemv_step<8>(a, xn, WG[0]); gemv_step<9>(a, xn, WG[1]); gemv_step<10>(a, xn, WG[2]); gemv_step<11>(a, xn, WG[3]);
      gemv_step<12>(a, xn, WG[4]); gemv_step<13>(a, xn, WG[5]); gemv_step<14>(a, xn, WG[6]); gemv_step<15>(a, xn, WG[7]);
      *(f32x2*)&L.part[w][8 * c + 2 * r] = fold_rows(a);
    }
    {
      float a[8] = {0.f, 0.f, 0.f, 0.f, 0.f, 0.f, 0.f, 0.f};
      wait8<18>(WU);
      gemv_step<8>(a, xn, WU[0]); gemv_step<9>(a, xn, WU[1]); gemv_step<10>(a, xn, WU[2]); gemv_step<11>(a, xn, WU[3]);
      gemv_step<12>(a, xn, WU[4]); gemv_step<13>(a, xn, WU[5]); gemv_step<14>(a, xn, WU[6]); gemv_step<15>(a, xn, WU[7]);
      *(f32x2*)&L.part[w][128 + 8 * c + 2 * r] = fold_rows(a);
    }
    STAMP(3);
    wg_barrier();
    STAMP(4);
    const u32x4 q = *(const u32x4*)&L.q[4 * c];
    const u32 tag = __builtin_amdgcn_readfirstlane(L.ctl[0]);
    const u32 base = __builtin_amdgcn_readfirstlane(L.ctl[1]);

    u32x4 W[8];
#pragma unroll
    for (int i = 0; i < 8; i++) asm volatile("" : "=v"(W[i]));
    const i32x4 rwo = mkr(wo, (u32)4096 * D * 2);
    const u32 wvo = (16 * (r >> 1) + (r & 1)) * 2048 + c * 16, wso = (u32)(head * 128 + 32 * w) * 2048 + j * 256;
    float m = -INFINITY, l = 0.f, acc[8] = {0.f, 0.f, 0.f, 0.f, 0.f, 0.f, 0.f, 0.f};
    u32 done = 0;

    auto half_step = [&](u32x4 (&K)[4], u32x4 (&V)[4], u32& tk_use, u32& tk_new, int i, u32 invalid) -> u32 {
      ticket(tk_new, aoff, mkr(ctr, (((u32)(SPW - 2 - i) >> 31) & ~done) << 2));
      wait4<14>(K);
      float d[4];
#pragma unroll
      for (int t = 0; t < 4; t++) d[t] = dot2(K[t][3], q[3], dot2(K[t][2], q[2], dot2(K[t][1], q[1], dot2(K[t][0], q[0], 0.f))));
      asm volatile("" : "+v"(d[0]), "+v"(d[1]), "+v"(d[2]), "+v"(d[3]));
      wait1<9>(tk_use);
      const u32 dyn = min((u32)DYN0 + (__builtin_amdgcn_readfirstlane(tk_use) - base), (u32)NTILE) | (done << 11);
      const u32 t = i < SPW ? (u32)(st0 + 4 * i) : dyn;
      const u32 inv = t >> 11;
      const u32 first_end = inv & ~done;
      const u32 nr = (inv - 1u) & HEAD_BYTES;
      done = inv;
      issue4_or(K, voff, mkr(kb, nr), (u32)t * 4096, first_end, W[0], W[1], W[2], W[3], wvo, rwo, wso, wso + 4096, wso + 8192, wso + 12288);
      asm volatile("" : "+v"(d[0]), "+v"(d[1]), "+v"(d[2]), "+v"(d[3]));
      float p = 0.f;
      if (!invalid) {
        const bool b8 = (c & 8) != 0, b4 = (c & 4) != 0;
        const float e0 = (b8 ? d[2] : d[0]) + dpp<ROR8>(b8 ? d[0] : d[2]);
        const float e1 = (b8 ? d[3] : d[1]) + dpp<ROR8>(b8 ? d[1] : d[3]);
        float f = (b4 ? e1 : e0) + dpp<HMIRROR>(b4 ? e0 : e1);
        f += dpp<XOR1>(f); f += dpp<XOR2>(f);
        const float y = rbf(rbf(f) * 0.08838834764831845f) * 1.4426950408889634f;
        float ym = fmaxf(y, dpp<ROR4>(y)); ym = fmaxf(ym, dpp<ROR8>(ym));
        if (__any(ym > m)) {
          const float mn = fmaxf(m, ym), al = __builtin_amdgcn_exp2f(m - mn);
          l *= al;
#pragma unroll
          for (int k = 0; k < 8; k++) acc[k] *= al;
          m = mn;
        }
        p = __builtin_amdgcn_exp2f(y - m);
        l += p;
      }
      wait4<14>(V);
      if (!invalid) {
        gemv_step<0>(acc, p, V[0]); gemv_step<4>(acc, p, V[1]); gemv_step<8>(acc, p, V[2]); gemv_step<12>(acc, p, V[3]);
      }
      asm volatile("" : "+v"(acc[0]), "+v"(acc[1]), "+v"(acc[2]), "+v"(acc[3]), "+v"(acc[4]), "+v"(acc[5]), "+v"(acc[6]), "+v"(acc[7]));
      issue4_or(V, voff, mkr(vb, nr), (u32)t * 4096, first_end, W[4], W[5], W[6], W[7], wvo, rwo, wso + 16384, wso + 20480, wso + 24576, wso + 28672);
      return inv;
    };
    wait1<17>(tkB);
    int i = 2; u32 iA, iB = 0;
    do {
      iA = half_step(AK, AV, tkA, tkB, i, 0u);
      iB = half_step(BK, BV, tkB, tkA, i + 1, iB);
      i += 2;
    } while (!iA);
    STAMP(5);
    {
      const float M = xrow_max(m), sc = __builtin_amdgcn_exp2f(m - M);
#pragma unroll
      for (int k = 0; k < 8; k++) acc[k] *= sc;
      const float ls = wave_sum(l * sc) * 0.25f;
      *(f32x2*)&L.ap[w][8 * c + 2 * r] = fold_rows(acc);
      if (lane == 0) *(f32x2*)&L.ap[w][128] = f32x2{M, ls};
    }
    wg_barrier();
    STAMP(6);
    {
      const gu64* s1 = (const gu64*)(ws + WS_S1) + (size_t)(head * 8 + r) * 144;
      u64 g[8];
      for (u32 spin = 0;; spin++) {
        g[0] = get(s1 + 32 * w + 2 * c); g[1] = get(s1 + 32 * w + 2 * c + 1); g[2] = get(s1 + 128); g[3] = get(s1 + 129);
        g[4] = get(s1 + 4 * 144 + 32 * w + 2 * c); g[5] = get(s1 + 4 * 144 + 32 * w + 2 * c + 1); g[6] = get(s1 + 4 * 144 + 128); g[7] = get(s1 + 4 * 144 + 129);
        bool ok = true;
#pragma unroll
        for (int k = 0; k < 8; k++) ok &= tag_ok(g[k], tag);
        if (__all(ok) || spin > SPIN_CAP) break;
        __builtin_amdgcn_s_sleep(1);
      }
      STAMP(7);
      const float M = xrow_max(fmaxf(val(g[2]), val(g[6])));
      const float w0 = __builtin_amdgcn_exp2f(val(g[2]) - M), w1 = __builtin_amdgcn_exp2f(val(g[6]) - M);
      const float Lt = xrow_sum(fmaf(w0, val(g[3]), w1 * val(g[7])));
      const float o0 = xrow_sum(fmaf(w0, val(g[0]), w1 * val(g[4]))), o1 = xrow_sum(fmaf(w0, val(g[1]), w1 * val(g[5])));
      float o = rbf(((r & 1) ? o1 : o0) / Lt);
      const float orot = dpp<ROR8>(o);
      o = (r & 2) ? orot : o;
      float a[8] = {0.f, 0.f, 0.f, 0.f, 0.f, 0.f, 0.f, 0.f};
      wait8<0>(W);
      wait4<0>(AK); wait4<0>(AV); wait4<0>(BK); wait4<0>(BV); wait1<0>(tkA); wait1<0>(tkB);
      gemv_step<0>(a, o, W[0]); gemv_step<1>(a, o, W[1]); gemv_step<2>(a, o, W[2]); gemv_step<3>(a, o, W[3]);
      gemv_step<4>(a, o, W[4]); gemv_step<5>(a, o, W[5]); gemv_step<6>(a, o, W[6]); gemv_step<7>(a, o, W[7]);
      *(f32x2*)&L.qp[w][8 * c + 2 * r] = fold_rows(a);
    }
    wg_barrier();
    STAMP(8);
  } else {
    const u32 tag = __builtin_amdgcn_readfirstlane(__hip_atomic_load((gu32*)(ws + WS_EPOCH), __ATOMIC_RELAXED, __HIP_MEMORY_SCOPE_AGENT)) + 1u;
    gu64* qrec = (gu64*)(ws + WS_QREC);
    if (j == 0 && lane == 0) {
      const u32 g = __hip_atomic_fetch_add((gu32*)(ws + WS_CTR + head * 256), 0u, __ATOMIC_RELAXED, __HIP_MEMORY_SCOPE_AGENT);
      put(qrec + (size_t)b * 144 + 128, tag, __uint_as_float(g));
    }
    const u32 xfin = ((const GAS u32*)x)[2 * b + (lane & 1)];
    STAMP(1);
    wg_barrier();
    {
      f32x2 s = *(const f32x2*)&L.qp[0][2 * lane];
#pragma unroll
      for (int k = 1; k < 4; k++) s += *(const f32x2*)&L.qp[k][2 * lane];
      put(qrec + (size_t)b * 144 + 2 * lane, tag, s.x); put(qrec + (size_t)b * 144 + 2 * lane + 1, tag, s.y);
      STAMP(2);
      const gu64* qr = qrec + (size_t)head * 8 * 144;
      u64 g[17];
      for (u32 spin = 0;; spin++) {
#pragma unroll
        for (int k = 0; k < 8; k++) { g[2 * k] = get(qr + k * 144 + 2 * lane); g[2 * k + 1] = get(qr + k * 144 + 2 * lane + 1); }
        g[16] = get(qr + 128);
        bool ok = true;
#pragma unroll
        for (int k = 0; k < 17; k++) ok &= tag_ok(g[k], tag);
        if (__all(ok) || spin > SPIN_CAP) break;
        __builtin_amdgcn_s_sleep(1);
      }
      float q0 = 0.f, q1 = 0.f;
#pragma unroll
      for (int k = 0; k < 8; k++) { q0 += val(g[2 * k]); q1 += val(g[2 * k + 1]); }
      L.q[lane] = cvt_pk(q0, q1);
      if (lane == 0) { L.ctl[0] = tag; L.ctl[1] = (u32)g[16]; }
    }
    STAMP(3);
    wg_barrier();
    STAMP(4);
    if (has_ffn) {
      gu64* gu = (gu64*)(ws + WS_GU);
      f32x2 sg = *(const f32x2*)&L.part[0][2 * lane], su = *(const f32x2*)&L.part[0][128 + 2 * lane];
#pragma unroll
      for (int k = 1; k < 4; k++) { sg += *(const f32x2*)&L.part[k][2 * lane]; su += *(const f32x2*)&L.part[k][128 + 2 * lane]; }
      gu64* mine = gu + (size_t)unit * 256;
      put(mine + 2 * lane, tag, sg.x); put(mine + 2 * lane + 1, tag, sg.y);
      put(mine + 128 + 2 * lane, tag, su.x); put(mine + 128 + 2 * lane + 1, tag, su.y);
      const GAS u32x4* wp = (const GAS u32x4*)(wd + (size_t)(128 * ug + 32 * r) * D + 128 * us + 8 * c);
      u32x4 WD[32];
#pragma unroll
      for (int i = 0; i < 32; i++) WD[i] = __builtin_nontemporal_load(wp + i * (D / 8));
      const gu64* gr = gu + (size_t)ug * 8 * 256;
      float g0, g1, u0, u1;
      for (u32 spin = 0;; spin++) {
        bool ok = true;
        g0 = g1 = u0 = u1 = 0.f;
#pragma unroll
        for (int k = 0; k < 8; k++) {
          const u64 a0 = get(gr + k * 256 + 2 * lane), a1 = get(gr + k * 256 + 2 * lane + 1), b0 = get(gr + k * 256 + 128 + 2 * lane), b1 = get(gr + k * 256 + 129 + 2 * lane);
          ok &= tag_ok(a0, tag) & tag_ok(a1, tag) & tag_ok(b0, tag) & tag_ok(b1, tag);
          g0 += val(a0); g1 += val(a1); u0 += val(b0); u1 += val(b1);
        }
        if (__all(ok) || spin > SPIN_CAP) break;
        __builtin_amdgcn_s_sleep(8);
      }
      auto act = [](float g, float u) {
        g = rbf(g); u = rbf(u);
        const float inner = 0.7978845608028654f * (g + 0.044715f * g * g * g);
        return rbf(rbf(0.5f * g * (1.f + tanhf(inner))) * u);
      };
      const float h0 = act(g0, u0), h1 = act(g1, u1);
      float a[8] = {0.f, 0.f, 0.f, 0.f, 0.f, 0.f, 0.f, 0.f};
#define WD2(k) gemv_step<k>(a, h0, WD[2 * k]); gemv_step<k>(a, h1, WD[2 * k + 1]);
      WD2(0) WD2(1) WD2(2) WD2(3) WD2(4) WD2(5) WD2(6) WD2(7) WD2(8) WD2(9) WD2(10) WD2(11) WD2(12) WD2(13) WD2(14) WD2(15)
#undef WD2
      const f32x2 s = fold_rows(a);
      gu64* fr = (gu64*)(ws + WS_FR) + (size_t)unit * 128 + 8 * c + 2 * r;
      put(fr, tag, s.x); put(fr + 1, tag, s.y);
    }
    STAMP(5);
    const int jj = b >> 5, col = (b & 31) * 4 + 2 * (lane & 1);
    float f0, f1;
    {
      const int rg = lane >> 1;
      const gu64* fr = (const gu64*)(ws + WS_FR) + (size_t)((rg < 28 ? rg : 27) * 8 + jj) * 128 + col;
      for (u32 spin = 0;; spin++) {
        const u64 a0 = get(fr), a1 = get(fr + 1);
        f0 = rg < 28 ? val(a0) : 0.f; f1 = rg < 28 ? val(a1) : 0.f;
        if (__all(tag_ok(a0, tag) & tag_ok(a1, tag)) || spin > SPIN_CAP) break;
        __builtin_amdgcn_s_sleep(8);
      }
      f0 += dpp<XOR2>(f0); f0 += dpp<ROR4>(f0); f0 += dpp<ROR8>(f0); f0 = xrow_sum(f0);
      f1 += dpp<XOR2>(f1); f1 += dpp<ROR4>(f1); f1 += dpp<ROR8>(f1); f1 = xrow_sum(f1);
    }
    STAMP(9);
    wg_barrier();
    STAMP(6);
    {
      float mw[4], lw[4];
#pragma unroll
      for (int k = 0; k < 4; k++) { const f32x2 t = *(const f32x2*)&L.ap[k][128]; mw[k] = t.x; lw[k] = t.y; }
      const float M = fmaxf(fmaxf(mw[0], mw[1]), fmaxf(mw[2], mw[3]));
      float Lt = 0.f; f32x2 o = {0.f, 0.f};
#pragma unroll
      for (int k = 0; k < 4; k++) {
        const float s = __builtin_amdgcn_exp2f(mw[k] - M);
        Lt = fmaf(s, lw[k], Lt);
        o += s * *(const f32x2*)&L.ap[k][2 * lane];
      }
      gu64* s1 = (gu64*)(ws + WS_S1) + (size_t)b * 144;
      put(s1 + 2 * lane, tag, o.x); put(s1 + 2 * lane + 1, tag, o.y);
      if (lane < 2) put(s1 + 128 + lane, tag, lane == 0 ? M : Lt);
    }
    STAMP(7);
    wg_barrier();
    STAMP(8);
    {
      f32x2 s = *(const f32x2*)&L.qp[0][2 * lane];
#pragma unroll
      for (int k = 1; k < 4; k++) s += *(const f32x2*)&L.qp[k][2 * lane];
      gu64* s2 = (gu64*)(ws + WS_S2);
      put(s2 + (size_t)b * 128 + 2 * lane, tag, s.x); put(s2 + (size_t)b * 128 + 2 * lane + 1, tag, s.y);
      const gu64* sr = s2 + (size_t)((lane >> 1) * 8 + jj) * 128 + col;
      float a0, a1;
      for (u32 spin = 0;; spin++) {
        const u64 g0 = get(sr), g1 = get(sr + 1);
        a0 = val(g0); a1 = val(g1);
        if (__all(tag_ok(g0, tag) & tag_ok(g1, tag)) || spin > SPIN_CAP) break;
        __builtin_amdgcn_s_sleep(1);
      }
      a0 += dpp<XOR2>(a0); a0 += dpp<ROR4>(a0); a0 += dpp<ROR8>(a0); a0 = xrow_sum(a0);
      a1 += dpp<XOR2>(a1); a1 += dpp<ROR4>(a1); a1 += dpp<ROR8>(a1); a1 = xrow_sum(a1);
      const float r0 = rbf(bf_lo(xfin) + rbf(a0)) + rbf(f0), r1 = rbf(bf_hi(xfin) + rbf(a1)) + rbf(f1);
      if (lane < 2) ((GAS u32*)out)[2 * b + lane] = cvt_pk(r0, r1);
      if (b == 0 && lane == 0) __hip_atomic_store((gu32*)(ws + WS_EPOCH), tag, __ATOMIC_RELAXED, __HIP_MEMORY_SCOPE_AGENT);
    }
    STAMP(10);
  }
}

}

extern "C" void kernel_launch(void* const* d_in, const int* in_sizes, int n_in, void* d_out, int out_size, void* d_ws, size_t ws_size,
                              hipStream_t stream) {
  (void)in_sizes; (void)n_in; (void)out_size; (void)ws_size;
  fused_decode<<<256, 320, 0, stream>>>((const u16*)d_in[0], (const u16*)d_in[1], (const u16*)d_in[2], (const u16*)d_in[3], (const u16*)d_in[4],
                                        (const u16*)d_in[6], (const u16*)d_in[7], (const u16*)d_in[8], (u16*)d_out, (unsigned char*)d_ws);
}
```

```cpp
#include <hip/hip_runtime.h>
#include <stdint.h>
#include <stdio.h>

namespace {

typedef unsigned u32;
typedef unsigned long long u64;
typedef unsigned short u16;
typedef u32 u32x4 __attribute__((ext_vector_type(4)));
typedef int i32x4 __attribute__((ext_vector_type(4)));
typedef float f32x2 __attribute__((ext_vector_type(2)));
typedef __attribute__((ext_vector_type(2))) __bf16 bf16x2;
#define GAS __attribute__((address_space(1)))
typedef GAS u64 gu64;
typedef GAS u32 gu32;

constexpr int D = 1024, NH = 32, SK = 32768, DH = 128, FF = 3584;
constexpr int NTILE = SK / 16;
#define QDELAY 24
#define SPW_ 48
constexpr int SPW = SPW_;
static_assert(NTILE == 2048 && SPW % 2 == 0 && SPW >= 4 && SPW <= 64, "");
constexpr int DYN0 = SPW * 32;
constexpr u32 HEAD_BYTES = (u32)SK * DH * 2;

constexpr size_t WS_EPOCH = 0;
constexpr size_t WS_CTR = 4096;
constexpr size_t WS_QREC = 16384;
constexpr size_t WS_S1 = WS_QREC + 256 * 144 * 8;
constexpr size_t WS_S2 = WS_S1 + 256 * 144 * 8;
constexpr size_t WS_GU = WS_S2 + 256 * 128 * 8;
constexpr size_t WS_FR = WS_GU + 224 * 256 * 8;
constexpr size_t WS_STAMP = WS_FR + 224 * 128 * 8;
constexpr u32 SPIN_CAP = 1u << 16;

__device__ __forceinline__ float bf_lo(u32 v) { return __uint_as_float(v << 16); }
__device__ __forceinline__ float bf_hi(u32 v) { return __uint_as_float(v & 0xffff0000u); }
__device__ __forceinline__ u32 cvt_pk(float lo, float hi) {
  bf16x2 r = {(__bf16)lo, (__bf16)hi};
  return __builtin_bit_cast(u32, r);
}
__device__ __forceinline__ float rbf(float x) { return bf_lo((u32)__builtin_bit_cast(u16, (__bf16)x)); }
__device__ __forceinline__ float dot2(u32 a, u32 b, float c) {
  return __builtin_amdgcn_fdot2_f32_bf16(__builtin_bit_cast(bf16x2, a), __builtin_bit_cast(bf16x2, b), c, false);
}
template <int CTRL> __device__ __forceinline__ float dpp(float x) {
  return __builtin_bit_cast(float, __builtin_amdgcn_mov_dpp(__builtin_bit_cast(int, x), CTRL, 0xf, 0xf, true));
}
constexpr int XOR1 = 0xB1, XOR2 = 0x4E, HMIRROR = 0x141, ROR8 = 0x128, ROR4 = 0x124, BCAST = 0x150;
__device__ __forceinline__ float swap32_add(float a, float b) {
  auto s = __builtin_amdgcn_permlane32_swap(__float_as_uint(a), __float_as_uint(b), false, false);
  return __uint_as_float(s[0]) + __uint_as_float(s[1]);
}
__device__ __forceinline__ float swap16_add(float a, float b) {
  auto s = __builtin_amdgcn_permlane16_swap(__float_as_uint(a), __float_as_uint(b), false, false);
  return __uint_as_float(s[0]) + __uint_as_float(s[1]);
}
__device__ __forceinline__ float xrow_sum(float x) { return swap32_add(swap16_add(x, x), swap16_add(x, x)); }
__device__ __forceinline__ float xrow_max(float x) {
  auto s = __builtin_amdgcn_permlane16_swap(__float_as_uint(x), __float_as_uint(x), false, false);
  x = fmaxf(__uint_as_float(s[0]), __uint_as_float(s[1]));
  auto t = __builtin_amdgcn_permlane32_swap(__float_as_uint(x), __float_as_uint(x), false, false);
  return fmaxf(__uint_as_float(t[0]), __uint_as_float(t[1]));
}
__device__ __forceinline__ float row_sum(float x) { x += dpp<XOR1>(x); x += dpp<XOR2>(x); x += dpp<ROR4>(x); x += dpp<ROR8>(x); return x; }
__device__ __forceinline__ float wave_sum(float x) { return xrow_sum(row_sum(x)); }
__device__ __forceinline__ f32x2 fold_rows(const float (&a)[8]) {
  float s0 = swap32_add(a[0], a[4]), s1 = swap32_add(a[1], a[5]), s2 = swap32_add(a[2], a[6]), s3 = swap32_add(a[3], a[7]);
  return f32x2{swap16_add(s0, s2), swap16_add(s1, s3)};
}

__device__ __forceinline__ i32x4 mkr(const void* p, u32 n) {
  u64 a = (u64)p;
  i32x4 r; r.x = (int)(u32)a; r.y = (int)((u32)(a >> 32) & 0xffffu); r.z = (int)n; r.w = 0x00020000; return r;
}
template <int IMM> __device__ __forceinline__ void ld_nt(u32x4& d, u32 voff, i32x4 r, u32 soff) {
  asm volatile("buffer_load_dwordx4 %0, %1, %2, %3 offen offset:%4 nt" : "+v"(d) : "v"(voff), "s"(r), "s"(soff), "i"(IMM));
}
template <int IMM> __device__ __forceinline__ void ld_nt_o(u32x4& d, u32 voff, i32x4 r, u32 soff) {
  asm volatile("buffer_load_dwordx4 %0, %1, %2, %3 offen offset:%4 nt" : "=v"(d) : "v"(voff), "s"(r), "s"(soff), "i"(IMM));
}
__device__ __forceinline__ void issue4(u32x4 (&R)[4], u32 voff, i32x4 r, u32 soff) {
  ld_nt<0>(R[0], voff, r, soff); ld_nt<1024>(R[1], voff, r, soff); ld_nt<2048>(R[2], voff, r, soff); ld_nt<3072>(R[3], voff, r, soff);
}
__device__ __forceinline__ void issue4_o(u32x4 (&R)[4], u32 voff, i32x4 r, u32 soff) {
  ld_nt_o<0>(R[0], voff, r, soff); ld_nt_o<1024>(R[1], voff, r, soff); ld_nt_o<2048>(R[2], voff, r, soff); ld_nt_o<3072>(R[3], voff, r, soff);
}

__device__ __forceinline__ void issue4_or(u32x4 (&R)[4], u32 voff, i32x4 r, u32 soff, u32 alt, u32x4& W0, u32x4& W1, u32x4& W2, u32x4& W3,
                                          u32 wvo, i32x4 rw, u32 w0, u32 w1, u32 w2, u32 w3) {
  asm volatile(
      "s_cmp_lg_u32 %11, 0\n\t"
      "s_cbranch_scc1 1f\n\t"
      "buffer_load_dwordx4 %0, %8, %9, %10 offen nt\n\t"
      "buffer_load_dwordx4 %1, %8, %9, %10 offen offset:1024 nt\n\t"
      "buffer_load_dwordx4 %2, %8, %9, %10 offen offset:2048 nt\n\t"
      "buffer_load_dwordx4 %3, %8, %9, %10 offen offset:3072 nt\n\t"
      "s_branch 2f\n"
      "1:\n\t"
      "buffer_load_dwordx4 %4, %12, %13, %14 offen nt\n\t"
      "buffer_load_dwordx4 %5, %12, %13, %15 offen nt\n\t"
      "buffer_load_dwordx4 %6, %12, %13, %16 offen nt\n\t"
      "buffer_load_dwordx4 %7, %12, %13, %17 offen nt\n"
      "2:"
      : "+v"(R[0]), "+v"(R[1]), "+v"(R[2]), "+v"(R[3]), "+v"(W0), "+v"(W1), "+v"(W2), "+v"(W3)
      : "v"(voff), "s"(r), "s"(soff), "s"(alt), "v"(wvo), "s"(rw), "s"(w0), "s"(w1), "s"(w2), "s"(w3)
      : "scc");
}
template <int N> __device__ __forceinline__ void wait4(u32x4 (&R)[4]) {
  asm volatile("s_waitcnt vmcnt(%4)" : "+v"(R[0]), "+v"(R[1]), "+v"(R[2]), "+v"(R[3]) : "i"(N));
}
template <int N> __device__ __forceinline__ void wait8(u32x4 (&R)[8]) {
  asm volatile("s_waitcnt vmcnt(%8)" : "+v"(R[0]), "+v"(R[1]), "+v"(R[2]), "+v"(R[3]), "+v"(R[4]), "+v"(R[5]), "+v"(R[6]), "+v"(R[7]) : "i"(N));
}
template <int N> __device__ __forceinline__ void wait1(u32& t) { asm volatile("s_waitcnt vmcnt(%1)" : "+v"(t) : "i"(N)); }
__device__ __forceinline__ void ticket(u32& t, u32 aoff, i32x4 r) {
  asm volatile("v_mov_b32 %0, 1\n\tbuffer_atomic_add %0, %1, %2, 0 offen sc0" : "+v"(t) : "v"(aoff), "s"(r));
}
__device__ __forceinline__ void wg_barrier() { asm volatile("s_waitcnt lgkmcnt(0)\n\ts_barrier" ::: "memory"); }

__device__ __forceinline__ void put(gu64* g, u32 tag, float v) {
  __hip_atomic_store(g, ((u64)tag << 32) | (u64)__float_as_uint(v), __ATOMIC_RELAXED, __HIP_MEMORY_SCOPE_AGENT);
}
__device__ __forceinline__ u64 get(const gu64* g) { return __hip_atomic_load(g, __ATOMIC_RELAXED, __HIP_MEMORY_SCOPE_AGENT); }
__device__ __forceinline__ bool tag_ok(u64 x, u32 tag) { return (u32)(x >> 32) == tag; }
__device__ __forceinline__ float val(u64 x) { return __uint_as_float((u32)x); }

template <int LN> __device__ __forceinline__ void gemv_step(float (&acc)[8], float xv, u32x4 w) {
  const float xb = dpp<BCAST + LN>(xv);
#pragma unroll
  for (int k = 0; k < 4; k++) { acc[2 * k] = fmaf(xb, bf_lo(w[k]), acc[2 * k]); acc[2 * k + 1] = fmaf(xb, bf_hi(w[k]), acc[2 * k + 1]); }
}

struct Lds {
  float part[4][256];
  float qp[4][128];
  float ap[4][132];
  u32 q[64];
  u32 ctl[4];
};

#define STAMP(i) do {} while (0)

__global__ void __launch_bounds__(320, 2)
fused_decode(const u16* __restrict__ x, const u16* __restrict__ kc, const u16* __restrict__ vc, const u16* __restrict__ wq,
             const u16* __restrict__ wo, const u16* __restrict__ wg, const u16* __restrict__ wu, const u16* __restrict__ wd,
             u16* __restrict__ out, unsigned char* __restrict__ ws) {
  __shared__ Lds L;
  const int b = blockIdx.x, head = b >> 3, j = b & 7;
  const int lane = threadIdx.x & 63, wave = __builtin_amdgcn_readfirstlane(threadIdx.x >> 6);
  const int r = lane >> 4, c = lane & 15;
  const bool has_ffn = j != (head & 7);
  const int unit = has_ffn ? 7 * head + j - (j > (head & 7) ? 1 : 0) : 0;
  const int ug = unit >> 3, us = unit & 7;
  STAMP(0);

  if (wave < 4) {
    const int w = wave;
    u32x4 X0, X1; u32 xs;
    u32x4 WQ[8], WG[8], WU[8], AK[4], AV[4], BK[4], BV[4];
    u32 tkA = 0, tkB = 0;
    const i32x4 rx = mkr(x, D * 2);
    {
      const u32 xo = lane * 16;
      const int k7 = c & 7;
      const u32 so = (u32)(((c < 8 ? j : us) * 128 + 32 * w + 4 * k7 + r) * 2);
      asm volatile("buffer_load_dwordx4 %0, %1, %2, 0 offen" : "=v"(X0) : "v"(xo), "s"(rx));
      asm volatile("buffer_load_dwordx4 %0, %1, %2, 0 offen offset:1024" : "=v"(X1) : "v"(xo), "s"(rx));
      asm volatile("buffer_load_ushort %0, %1, %2, 0 offen" : "=v"(xs) : "v"(so), "s"(rx));
    }
    {
      const i32x4 rq = mkr(wq, (u32)D * 4096 * 2);
      const u32 vo = r * 8192 + c * 16, s0 = (u32)(j * 128 + 32 * w) * 8192 + head * 256;
#pragma unroll
      for (int i = 0; i < 8; i++) ld_nt_o<0>(WQ[i], vo, rq, s0 + i * 32768);
    }
    wg_barrier();
    const u32 nffn = has_ffn ? (u32)D * FF * 2 : 0u;
    const i32x4 rg = mkr(wg, nffn), ru = mkr(wu, nffn);
    const u32 vg = r * (FF * 2) + c * 16, g0 = (u32)(us * 128 + 32 * w) * (FF * 2) + ug * 256;
#pragma unroll
    for (int i = 0; i < 8; i++) ld_nt_o<0>(WG[i], vg, rg, g0 + i * (4 * FF * 2));
    const char* kb = (const char*)kc + (size_t)head * HEAD_BYTES;
    const char* vb = (const char*)vc + (size_t)head * HEAD_BYTES;
    const u32 voff = lane * 16;
    const u32 aoff = lane == 0 ? 0u : 0x7ffffff0u;
    const unsigned char* ctr = ws + WS_CTR + head * 256;
    const int st0 = j * SPW * 4 + w;
    asm volatile("s_waitcnt vmcnt(16)" : "+v"(X0), "+v"(X1), "+v"(xs));
    float xn;
    {
      float v[16];
#pragma unroll
      for (int k = 0; k < 4; k++) { v[2 * k] = bf_lo(X0[k]); v[2 * k + 1] = bf_hi(X0[k]); v[8 + 2 * k] = bf_lo(X1[k]); v[9 + 2 * k] = bf_hi(X1[k]); }
      float s = 0.f;
#pragma unroll
      for (int k = 0; k < 16; k++) s += v[k];
      const float mean = wave_sum(s) * (1.0f / D);
      float q = 0.f;
#pragma unroll
      for (int k = 0; k < 16; k++) { const float d = v[k] - mean; q = fmaf(d, d, q); }
      const float var = wave_sum(q) * (1.0f / D);
      const float den = rbf(sqrtf(rbf(rbf(var) + 1e-5f)));
      xn = rbf(rbf(bf_lo(xs) - rbf(mean)) / den);
    }
    asm volatile("" : "+v"(xn));
    STAMP(1);
    {
      float a[8] = {0.f, 0.f, 0.f, 0.f, 0.f, 0.f, 0.f, 0.f};
      wait8<8>(WQ);
#pragma unroll
      for (int i = 0; i < 8; i++) ld_nt_o<0>(WU[i], vg, ru, g0 + i * (4 * FF * 2));
      wait8<63>(WQ);
      gemv_step<0>(a, xn, WQ[0]); gemv_step<1>(a, xn, WQ[1]); gemv_step<2>(a, xn, WQ[2]); gemv_step<3>(a, xn, WQ[3]);
      gemv_step<4>(a, xn, WQ[4]); gemv_step<5>(a, xn, WQ[5]); gemv_step<6>(a, xn, WQ[6]); gemv_step<7>(a, xn, WQ[7]);
      *(f32x2*)&L.qp[w][8 * c + 2 * r] = fold_rows(a);
    }
    wg_barrier();
    STAMP(2);
    {
      float a[8] = {0.f, 0.f, 0.f, 0.f, 0.f, 0.f, 0.f, 0.f};
      wait8<8>(WG);
      ticket(tkB, aoff, mkr(ctr, 0));
      issue4_o(AK, voff, mkr(kb, HEAD_BYTES), (u32)st0 * 4096); issue4_o(AV, voff, mkr(vb, HEAD_BYTES), (u32)st0 * 4096);
      wait8<63>(WG);
      gemv_step<8>(a, xn, WG[0]); gemv_step<9>(a, xn, WG[1]); gemv_step<10>(a, xn, WG[2]); gemv_step<11>(a, xn, WG[3]);
      gemv_step<12>(a, xn, WG[4]); gemv_step<13>(a, xn, WG[5]); gemv_step<14>(a, xn, WG[6]); gemv_step<15>(a, xn, WG[7]);
      *(f32x2*)&L.part[w][8 * c + 2 * r] = fold_rows(a);
    }
    {
      float a[8] = {0.f, 0.f, 0.f, 0.f, 0.f, 0.f, 0.f, 0.f};
      wait8<9>(WU);
      ticket(tkA, aoff, mkr(ctr, 0));
      issue4_o(BK, voff, mkr(kb, HEAD_BYTES), (u32)(st0 + 4) * 4096); issue4_o(BV, voff, mkr(vb, HEAD_BYTES), (u32)(st0 + 4) * 4096);
      wait8<63>(WU);
      gemv_step<8>(a, xn, WU[0]); gemv_step<9>(a, xn, WU[1]); gemv_step<10>(a, xn, WU[2]); gemv_step<11>(a, xn, WU[3]);
      gemv_step<12>(a, xn, WU[4]); gemv_step<13>(a, xn, WU[5]); gemv_step<14>(a, xn, WU[6]); gemv_step<15>(a, xn, WU[7]);
      *(f32x2*)&L.part[w][128 + 8 * c + 2 * r] = fold_rows(a);
    }
    STAMP(3);
    wg_barrier();
    STAMP(4);
    const u32x4 q = *(const u32x4*)&L.q[4 * c];
    const u32 tag = __builtin_amdgcn_readfirstlane(L.ctl[0]);
    const u32 base = __builtin_amdgcn_readfirstlane(L.ctl[1]);

    u32x4 W[8];
#pragma unroll
    for (int i = 0; i < 8; i++) asm volatile("" : "=v"(W[i]));
    const i32x4 rwo = mkr(wo, (u32)4096 * D * 2);
    const u32 wvo = (16 * (r >> 1) + (r & 1)) * 2048 + c * 16, wso = (u32)(head * 128 + 32 * w) * 2048 + j * 256;
    float m = -INFINITY, l = 0.f, acc[8] = {0.f, 0.f, 0.f, 0.f, 0.f, 0.f, 0.f, 0.f};
    u32 done = 0;

    auto half_step = [&](u32x4 (&K)[4], u32x4 (&V)[4], u32& tk_use, u32& tk_new, int i, u32 invalid) -> u32 {
      ticket(tk_new, aoff, mkr(ctr, (((u32)(SPW - 2 - i) >> 31) & ~done) << 2));
      wait4<14>(K);
      float d[4];
#pragma unroll
      for (int t = 0; t < 4; t++) d[t] = dot2(K[t][3], q[3], dot2(K[t][2], q[2], dot2(K[t][1], q[1], dot2(K[t][0], q[0], 0.f))));
      asm volatile("" : "+v"(d[0]), "+v"(d[1]), "+v"(d[2]), "+v"(d[3]));
      wait1<9>(tk_use);
      const u32 dyn = min((u32)DYN0 + (__builtin_amdgcn_readfirstlane(tk_use) - base), (u32)NTILE) | (done << 11);
      const u32 t = i < SPW ? (u32)(st0 + 4 * i) : dyn;
      const u32 inv = t >> 11;
      const u32 first_end = inv & ~done;
      const u32 nr = (inv - 1u) & HEAD_BYTES;
      done = inv;
      issue4_or(K, voff, mkr(kb, nr), (u32)t * 4096, first_end, W[0], W[1], W[2], W[3], wvo, rwo, wso, wso + 4096, wso + 8192, wso + 12288);
      asm volatile("" : "+v"(d[0]), "+v"(d[1]), "+v"(d[2]), "+v"(d[3]));
      float p = 0.f;
      if (!invalid) {
        const bool b8 = (c & 8) != 0, b4 = (c & 4) != 0;
        const float e0 = (b8 ? d[2] : d[0]) + dpp<ROR8>(b8 ? d[0] : d[2]);
        const float e1 = (b8 ? d[3] : d[1]) + dpp<ROR8>(b8 ? d[1] : d[3]);
        float f = (b4 ? e1 : e0) + dpp<HMIRROR>(b4 ? e0 : e1);
        f += dpp<XOR1>(f); f += dpp<XOR2>(f);
        const float y = rbf(rbf(f) * 0.08838834764831845f) * 1.4426950408889634f;
        float ym = fmaxf(y, dpp<ROR4>(y)); ym = fmaxf(ym, dpp<ROR8>(ym));
        if (__any(ym > m)) {
          const float mn = fmaxf(m, ym), al = __builtin_amdgcn_exp2f(m - mn);
          l *= al;
#pragma unroll
          for (int k = 0; k < 8; k++) acc[k] *= al;
          m = mn;
        }
        p = __builtin_amdgcn_exp2f(y - m);
        l += p;
      }
      wait4<14>(V);
      if (!invalid) {
        gemv_step<0>(acc, p, V[0]); gemv_step<4>(acc, p, V[1]); gemv_step<8>(acc, p, V[2]); gemv_step<12>(acc, p, V[3]);
      }
      asm volatile("" : "+v"(acc[0]), "+v"(acc[1]), "+v"(acc[2]), "+v"(acc[3]), "+v"(acc[4]), "+v"(acc[5]), "+v"(acc[6]), "+v"(acc[7]));
      issue4_or(V, voff, mkr(vb, nr), (u32)t * 4096, first_end, W[4], W[5], W[6], W[7], wvo, rwo, wso + 16384, wso + 20480, wso + 24576, wso + 28672);
      return inv;
    };
    wait1<17>(tkB);
    int i = 2; u32 iA, iB = 0;
    do {
      iA = half_step(AK, AV, tkA, tkB, i, 0u);
      iB = half_step(BK, BV, tkB, tkA, i + 1, iB);
      i += 2;
    } while (!iA);
    STAMP(5);
    {
      const float M = xrow_max(m), sc = __builtin_amdgcn_exp2f(m - M);
#pragma unroll
      for (int k = 0; k < 8; k++) acc[k] *= sc;
      const float ls = wave_sum(l * sc) * 0.25f;
      *(f32x2*)&L.ap[w][8 * c + 2 * r] = fold_rows(acc);
      if (lane == 0) *(f32x2*)&L.ap[w][128] = f32x2{M, ls};
    }
    wg_barrier();
    STAMP(6);
    {
      const gu64* s1 = (const gu64*)(ws + WS_S1) + (size_t)(head * 8 + r) * 144;
      u64 g[8];
      for (u32 spin = 0;; spin++) {
        g[0] = get(s1 + 32 * w + 2 * c); g[1] = get(s1 + 32 * w + 2 * c + 1); g[2] = get(s1 + 128); g[3] = get(s1 + 129);
        g[4] = get(s1 + 4 * 144 + 32 * w + 2 * c); g[5] = get(s1 + 4 * 144 + 32 * w + 2 * c + 1); g[6] = get(s1 + 4 * 144 + 128); g[7] = get(s1 + 4 * 144 + 129);
        bool ok = true;
#pragma unroll
        for (int k = 0; k < 8; k++) ok &= tag_ok(g[k], tag);
        if (__all(ok) || spin > SPIN_CAP) break;
        __builtin_amdgcn_s_sleep(1);
      }
      STAMP(7);
      const float M = xrow_max(fmaxf(val(g[2]), val(g[6])));
      const float w0 = __builtin_amdgcn_exp2f(val(g[2]) - M), w1 = __builtin_amdgcn_exp2f(val(g[6]) - M);
      const float Lt = xrow_sum(fmaf(w0, val(g[3]), w1 * val(g[7])));
      const float o0 = xrow_sum(fmaf(w0, val(g[0]), w1 * val(g[4]))), o1 = xrow_sum(fmaf(w0, val(g[1]), w1 * val(g[5])));
      float o = rbf(((r & 1) ? o1 : o0) / Lt);
      const float orot = dpp<ROR8>(o);
      o = (r & 2) ? orot : o;
      float a[8] = {0.f, 0.f, 0.f, 0.f, 0.f, 0.f, 0.f, 0.f};
      wait8<0>(W);
      wait4<0>(AK); wait4<0>(AV); wait4<0>(BK); wait4<0>(BV); wait1<0>(tkA); wait1<0>(tkB);
      gemv_step<0>(a, o, W[0]); gemv_step<1>(a, o, W[1]); gemv_step<2>(a, o, W[2]); gemv_step<3>(a, o, W[3]);
      gemv_step<4>(a, o, W[4]); gemv_step<5>(a, o, W[5]); gemv_step<6>(a, o, W[6]); gemv_step<7>(a, o, W[7]);
      *(f32x2*)&L.qp[w][8 * c + 2 * r] = fold_rows(a);
    }
    wg_barrier();
    STAMP(8);
  } else {
    const u32 ep = __hip_atomic_load((gu32*)(ws + WS_EPOCH), __ATOMIC_RELAXED, __HIP_MEMORY_SCOPE_AGENT);
    const u32 cb = __hip_atomic_load((gu32*)(ws + WS_CTR + head * 256), __ATOMIC_RELAXED, __HIP_MEMORY_SCOPE_AGENT);
    const u32 xfin = ((const GAS u32*)x)[2 * b + (lane & 1)];
    wg_barrier();
    const u32 tag = __builtin_amdgcn_readfirstlane(ep) + 1u;
    gu64* qrec = (gu64*)(ws + WS_QREC);
    if (j == 0 && lane == 0) put(qrec + (size_t)b * 144 + 128, tag, __uint_as_float(cb));
    STAMP(1);
    wg_barrier();
    {
      f32x2 s = *(const f32x2*)&L.qp[0][2 * lane];
#pragma unroll
      for (int k = 1; k < 4; k++) s += *(const f32x2*)&L.qp[k][2 * lane];
      put(qrec + (size_t)b * 144 + 2 * lane, tag, s.x); put(qrec + (size_t)b * 144 + 2 * lane + 1, tag, s.y);
      STAMP(2);
      const gu64* qr = qrec + (size_t)head * 8 * 144;
      __builtin_amdgcn_s_sleep(QDELAY);
      u64 g[17];
      for (u32 spin = 0;; spin++) {
#pragma unroll
        for (int k = 0; k < 8; k++) { g[2 * k] = get(qr + k * 144 + 2 * lane); g[2 * k + 1] = get(qr + k * 144 + 2 * lane + 1); }
        g[16] = get(qr + 128);
        bool ok = true;
#pragma unroll
        for (int k = 0; k < 17; k++) ok &= tag_ok(g[k], tag);
        if (__all(ok) || spin > SPIN_CAP) break;
        __builtin_amdgcn_s_sleep(1);
      }
      float q0 = 0.f, q1 = 0.f;
#pragma unroll
      for (int k = 0; k < 8; k++) { q0 += val(g[2 * k]); q1 += val(g[2 * k + 1]); }
      L.q[lane] = cvt_pk(q0, q1);
      if (lane == 0) { L.ctl[0] = tag; L.ctl[1] = (u32)g[16]; }
    }
    STAMP(3);
    wg_barrier();
    STAMP(4);
    if (has_ffn) {
      gu64* gu = (gu64*)(ws + WS_GU);
      f32x2 sg = *(const f32x2*)&L.part[0][2 * lane], su = *(const f32x2*)&L.part[0][128 + 2 * lane];
#pragma unroll
      for (int k = 1; k < 4; k++) { sg += *(const f32x2*)&L.part[k][2 * lane]; su += *(const f32x2*)&L.part[k][128 + 2 * lane]; }
      gu64* mine = gu + (size_t)unit * 256;
      put(mine + 2 * lane, tag, sg.x); put(mine + 2 * lane + 1, tag, sg.y);
      put(mine + 128 + 2 * lane, tag, su.x); put(mine + 128 + 2 * lane + 1, tag, su.y);
      const GAS u32x4* wp = (const GAS u32x4*)(wd + (size_t)(128 * ug + 32 * r) * D + 128 * us + 8 * c);
      u32x4 WD[32];
#pragma unroll
      for (int i = 0; i < 32; i++) WD[i] = __builtin_nontemporal_load(wp + i * (D / 8));
      const gu64* gr = gu + (size_t)ug * 8 * 256;
      float g0, g1, u0, u1;
      for (u32 spin = 0;; spin++) {
        bool ok = true;
        g0 = g1 = u0 = u1 = 0.f;
#pragma unroll
        for (int k = 0; k < 8; k++) {
          const u64 a0 = get(gr + k * 256 + 2 * lane), a1 = get(gr + k * 256 + 2 * lane + 1), b0 = get(gr + k * 256 + 128 + 2 * lane), b1 = get(gr + k * 256 + 129 + 2 * lane);
          ok &= tag_ok(a0, tag) & tag_ok(a1, tag) & tag_ok(b0, tag) & tag_ok(b1, tag);
          g0 += val(a0); g1 += val(a1); u0 += val(b0); u1 += val(b1);
        }
        if (__all(ok) || spin > SPIN_CAP) break;
        __builtin_amdgcn_s_sleep(8);
      }
      auto act = [](float g, float u) {
        g = rbf(g); u = rbf(u);
        const float inner = 0.7978845608028654f * (g + 0.044715f * g * g * g);
        return rbf(rbf(0.5f * g * (1.f + tanhf(inner))) * u);
      };
      const float h0 = act(g0, u0), h1 = act(g1, u1);
      float a[8] = {0.f, 0.f, 0.f, 0.f, 0.f, 0.f, 0.f, 0.f};
#define WD2(k) gemv_step<k>(a, h0, WD[2 * k]); gemv_step<k>(a, h1, WD[2 * k + 1]);
      WD2(0) WD2(1) WD2(2) WD2(3) WD2(4) WD2(5) WD2(6) WD2(7) WD2(8) WD2(9) WD2(10) WD2(11) WD2(12) WD2(13) WD2(14) WD2(15)
#undef WD2
      const f32x2 s = fold_rows(a);
      gu64* fr = (gu64*)(ws + WS_FR) + (size_t)unit * 128 + 8 * c + 2 * r;
      put(fr, tag, s.x); put(fr + 1, tag, s.y);
    }
    STAMP(5);
    const int jj = b >> 5, col = (b & 31) * 4 + 2 * (lane & 1);
    float f0, f1;
    {
      const int rg = lane >> 1;
      const gu64* fr = (const gu64*)(ws + WS_FR) + (size_t)((rg < 28 ? rg : 27) * 8 + jj) * 128 + col;
      for (u32 spin = 0;; spin++) {
        const u64 a0 = get(fr), a1 = get(fr + 1);
        f0 = rg < 28 ? val(a0) : 0.f; f1 = rg < 28 ? val(a1) : 0.f;
        if (__all(tag_ok(a0, tag) & tag_ok(a1, tag)) || spin > SPIN_CAP) break;
        __builtin_amdgcn_s_sleep(8);
      }
      f0 += dpp<XOR2>(f0); f0 += dpp<ROR4>(f0); f0 += dpp<ROR8>(f0); f0 = xrow_sum(f0);
      f1 += dpp<XOR2>(f1); f1 += dpp<ROR4>(f1); f1 += dpp<ROR8>(f1); f1 = xrow_sum(f1);
    }
    STAMP(9);
    wg_barrier();
    STAMP(6);
    {
      float mw[4], lw[4];
#pragma unroll
      for (int k = 0; k < 4; k++) { const f32x2 t = *(const f32x2*)&L.ap[k][128]; mw[k] = t.x; lw[k] = t.y; }
      const float M = fmaxf(fmaxf(mw[0], mw[1]), fmaxf(mw[2], mw[3]));
      float Lt = 0.f; f32x2 o = {0.f, 0.f};
#pragma unroll
      for (int k = 0; k < 4; k++) {
        const float s = __builtin_amdgcn_exp2f(mw[k] - M);
        Lt = fmaf(s, lw[k], Lt);
        o += s * *(const f32x2*)&L.ap[k][2 * lane];
      }
      gu64* s1 = (gu64*)(ws + WS_S1) + (size_t)b * 144;
      put(s1 + 2 * lane, tag, o.x); put(s1 + 2 * lane + 1, tag, o.y);
      if (lane < 2) put(s1 + 128 + lane, tag, lane == 0 ? M : Lt);
    }
    STAMP(7);
    wg_barrier();
    STAMP(8);
    {
      f32x2 s = *(const f32x2*)&L.qp[0][2 * lane];
#pragma unroll
      for (int k = 1; k < 4; k++) s += *(const f32x2*)&L.qp[k][2 * lane];
      gu64* s2 = (gu64*)(ws + WS_S2);
      put(s2 + (size_t)b * 128 + 2 * lane, tag, s.x); put(s2 + (size_t)b * 128 + 2 * lane + 1, tag, s.y);
      const gu64* sr = s2 + (size_t)((lane >> 1) * 8 + jj) * 128 + col;
      float a0, a1;
      for (u32 spin = 0;; spin++) {
        const u64 g0 = get(sr), g1 = get(sr + 1);
        a0 = val(g0); a1 = val(g1);
        if (__all(tag_ok(g0, tag) & tag_ok(g1, tag)) || spin > SPIN_CAP) break;
        __builtin_amdgcn_s_sleep(1);
      }
      a0 += dpp<XOR2>(a0); a0 += dpp<ROR4>(a0); a0 += dpp<ROR8>(a0); a0 = xrow_sum(a0);
      a1 += dpp<XOR2>(a1); a1 += dpp<ROR4>(a1); a1 += dpp<ROR8>(a1); a1 = xrow_sum(a1);
      const float r0 = rbf(bf_lo(xfin) + rbf(a0)) + rbf(f0), r1 = rbf(bf_hi(xfin) + rbf(a1)) + rbf(f1);
      if (lane < 2) ((GAS u32*)out)[2 * b + lane] = cvt_pk(r0, r1);
      if (b == 0 && lane == 0) __hip_atomic_store((gu32*)(ws + WS_EPOCH), tag, __ATOMIC_RELAXED, __HIP_MEMORY_SCOPE_AGENT);
    }
    STAMP(10);
  }
}

}

extern "C" void kernel_launch(void* const* d_in, const int* in_sizes, int n_in, void* d_out, int out_size, void* d_ws, size_t ws_size,
                              hipStream_t stream) {
  for (int i = 0; i < n_in; i++) {
    hipPointerAttribute_t at; hipError_t e = hipPointerGetAttributes(&at, d_in[i]);
    void* base = nullptr; size_t sz = 0; hipError_t e2 = hipMemGetAddressRange((hipDeviceptr_t*)&base, &sz, (hipDeviceptr_t)d_in[i]);
    fprintf(stderr, "in[%d] %p n=%d attr(e=%d type=%d managed=%d flags=%u) range(e=%d base=%p size=%zu)\n", i, d_in[i], in_sizes[i], (int)e, (int)at.type, (int)at.isManaged, at.allocationFlags, (int)e2, base, sz);
  }
  fprintf(stderr, "out %p ws %p %zu\n", d_out, d_ws, ws_size);
  (void)hipGetLastError();
  fused_decode<<<256, 320, 0, stream>>>((const u16*)d_in[0], (const u16*)d_in[1], (const u16*)d_in[2], (const u16*)d_in[3], (const u16*)d_in[4],
                                        (const u16*)d_in[6], (const u16*)d_in[7], (const u16*)d_in[8], (u16*)d_out, (unsigned char*)d_ws);
}
```

```cpp
#include <hip/hip_runtime.h>
#include <stdint.h>

namespace {

typedef unsigned u32;
typedef unsigned long long u64;
typedef unsigned short u16;
typedef u32 u32x4 __attribute__((ext_vector_type(4)));
typedef int i32x4 __attribute__((ext_vector_type(4)));
typedef float f32x2 __attribute__((ext_vector_type(2)));
typedef __attribute__((ext_vector_type(2))) __bf16 bf16x2;
#define GAS __attribute__((address_space(1)))
typedef GAS u64 gu64;
typedef GAS u32 gu32;

constexpr int D = 1024, NH = 32, SK = 32768, DH = 128, FF = 3584;
constexpr int NTILE = SK / 16;
#define QDELAY 24
#define SPW_ 48
constexpr int SPW = SPW_;
static_assert(NTILE == 2048 && SPW % 2 == 0 && SPW >= 4 && SPW <= 64, "");
constexpr int DYN0 = SPW * 32;
constexpr u32 HEAD_BYTES = (u32)SK * DH * 2;

constexpr size_t WS_EPOCH = 0;
constexpr size_t WS_CTR = 4096;
constexpr size_t WS_QREC = 16384;
constexpr size_t WS_S1 = WS_QREC + 256 * 144 * 8;
constexpr size_t WS_S2 = WS_S1 + 256 * 144 * 8;
constexpr size_t WS_GU = WS_S2 + 256 * 128 * 8;
constexpr size_t WS_FR = WS_GU + 224 * 256 * 8;
constexpr size_t WS_STAMP = WS_FR + 224 * 128 * 8;
constexpr u32 SPIN_CAP = 1u << 16;

__device__ __forceinline__ float bf_lo(u32 v) { return __uint_as_float(v << 16); }
__device__ __forceinline__ float bf_hi(u32 v) { return __uint_as_float(v & 0xffff0000u); }
__device__ __forceinline__ u32 cvt_pk(float lo, float hi) {
  bf16x2 r = {(__bf16)lo, (__bf16)hi};
  return __builtin_bit_cast(u32, r);
}
__device__ __forceinline__ float rbf(float x) { return bf_lo((u32)__builtin_bit_cast(u16, (__bf16)x)); }
__device__ __forceinline__ float dot2(u32 a, u32 b, float c) {
  return __builtin_amdgcn_fdot2_f32_bf16(__builtin_bit_cast(bf16x2, a), __builtin_bit_cast(bf16x2, b), c, false);
}
template <int CTRL> __device__ __forceinline__ float dpp(float x) {
  return __builtin_bit_cast(float, __builtin_amdgcn_mov_dpp(__builtin_bit_cast(int, x), CTRL, 0xf, 0xf, true));
}
constexpr int XOR1 = 0xB1, XOR2 = 0x4E, HMIRROR = 0x141, ROR8 = 0x128, ROR4 = 0x124, BCAST = 0x150;
__device__ __forceinline__ float swap32_add(float a, float b) {
  auto s = __builtin_amdgcn_permlane32_swap(__float_as_uint(a), __float_as_uint(b), false, false);
  return __uint_as_float(s[0]) + __uint_as_float(s[1]);
}
__device__ __forceinline__ float swap16_add(float a, float b) {
  auto s = __builtin_amdgcn_permlane16_swap(__float_as_uint(a), __float_as_uint(b), false, false);
  return __uint_as_float(s[0]) + __uint_as_float(s[1]);
}
__device__ __forceinline__ float xrow_sum(float x) { return swap32_add(swap16_add(x, x), swap16_add(x, x)); }
__device__ __forceinline__ float xrow_max(float x) {
  auto s = __builtin_amdgcn_permlane16_swap(__float_as_uint(x), __float_as_uint(x), false, false);
  x = fmaxf(__uint_as_float(s[0]), __uint_as_float(s[1]));
  auto t = __builtin_amdgcn_permlane32_swap(__float_as_uint(x), __float_as_uint(x), false, false);
  return fmaxf(__uint_as_float(t[0]), __uint_as_float(t[1]));
}
__device__ __forceinline__ float row_sum(float x) { x += dpp<XOR1>(x); x += dpp<XOR2>(x); x += dpp<ROR4>(x); x += dpp<ROR8>(x); return x; }
__device__ __forceinline__ float wave_sum(float x) { return xrow_sum(row_sum(x)); }
__device__ __forceinline__ f32x2 fold_rows(const float (&a)[8]) {
  float s0 = swap32_add(a[0], a[4]), s1 = swap32_add(a[1], a[5]), s2 = swap32_add(a[2], a[6]), s3 = swap32_add(a[3], a[7]);
  return f32x2{swap16_add(s0, s2), swap16_add(s1, s3)};
}

__device__ __forceinline__ i32x4 mkr(const void* p, u32 n) {
  u64 a = (u64)p;
  i32x4 r; r.x = (int)(u32)a; r.y = (int)((u32)(a >> 32) & 0xffffu); r.z = (int)n; r.w = 0x00020000; return r;
}
template <int IMM> __device__ __forceinline__ void ld_nt(u32x4& d, u32 voff, i32x4 r, u32 soff) {
  asm volatile("buffer_load_dwordx4 %0, %1, %2, %3 offen offset:%4 nt" : "+v"(d) : "v"(voff), "s"(r), "s"(soff), "i"(IMM));
}
template <int IMM> __device__ __forceinline__ void ld_nt_o(u32x4& d, u32 voff, i32x4 r, u32 soff) {
  asm volatile("buffer_load_dwordx4 %0, %1, %2, %3 offen offset:%4 nt" : "=v"(d) : "v"(voff), "s"(r), "s"(soff), "i"(IMM));
}
__device__ __forceinline__ void issue4(u32x4 (&R)[4], u32 voff, i32x4 r, u32 soff) {
  ld_nt<0>(R[0], voff, r, soff); ld_nt<1024>(R[1], voff, r, soff); ld_nt<2048>(R[2], voff, r, soff); ld_nt<3072>(R[3], voff, r, soff);
}
__device__ __forceinline__ void issue4_o(u32x4 (&R)[4], u32 voff, i32x4 r, u32 soff) {
  ld_nt_o<0>(R[0], voff, r, soff); ld_nt_o<1024>(R[1], voff, r, soff); ld_nt_o<2048>(R[2], voff, r, soff); ld_nt_o<3072>(R[3], voff, r, soff);
}

__device__ __forceinline__ void issue4_or(u32x4 (&R)[4], u32 voff, i32x4 r, u32 soff, u32 alt, u32x4& W0, u32x4& W1, u32x4& W2, u32x4& W3,
                                          u32 wvo, i32x4 rw, u32 w0, u32 w1, u32 w2, u32 w3) {
  asm volatile(
      "s_cmp_lg_u32 %11, 0\n\t"
      "s_cbranch_scc1 1f\n\t"
      "buffer_load_dwordx4 %0, %8, %9, %10 offen nt\n\t"
      "buffer_load_dwordx4 %1, %8, %9, %10 offen offset:1024 nt\n\t"
      "buffer_load_dwordx4 %2, %8, %9, %10 offen offset:2048 nt\n\t"
      "buffer_load_dwordx4 %3, %8, %9, %10 offen offset:3072 nt\n\t"
      "s_branch 2f\n"
      "1:\n\t"
      "buffer_load_dwordx4 %4, %12, %13, %14 offen nt\n\t"
      "buffer_load_dwordx4 %5, %12, %13, %15 offen nt\n\t"
      "buffer_load_dwordx4 %6, %12, %13, %16 offen nt\n\t"
      "buffer_load_dwordx4 %7, %12, %13, %17 offen nt\n"
      "2:"
      : "+v"(R[0]), "+v"(R[1]), "+v"(R[2]), "+v"(R[3]), "+v"(W0), "+v"(W1), "+v"(W2), "+v"(W3)
      : "v"(voff), "s"(r), "s"(soff), "s"(alt), "v"(wvo), "s"(rw), "s"(w0), "s"(w1), "s"(w2), "s"(w3)
      : "scc");
}
template <int N> __device__ __forceinline__ void wait4(u32x4 (&R)[4]) {
  asm volatile("s_waitcnt vmcnt(%4)" : "+v"(R[0]), "+v"(R[1]), "+v"(R[2]), "+v"(R[3]) : "i"(N));
}
template <int N> __device__ __forceinline__ void wait8(u32x4 (&R)[8]) {
  asm volatile("s_waitcnt vmcnt(%8)" : "+v"(R[0]), "+v"(R[1]), "+v"(R[2]), "+v"(R[3]), "+v"(R[4]), "+v"(R[5]), "+v"(R[6]), "+v"(R[7]) : "i"(N));
}
template <int N> __device__ __forceinline__ void wait1(u32& t) { asm volatile("s_waitcnt vmcnt(%1)" : "+v"(t) : "i"(N)); }
__device__ __forceinline__ void ticket(u32& t, u32 aoff, i32x4 r) {
  asm volatile("v_mov_b32 %0, 1\n\tbuffer_atomic_add %0, %1, %2, 0 offen sc0" : "+v"(t) : "v"(aoff), "s"(r));
}
__device__ __forceinline__ void wg_barrier() { asm volatile("s_waitcnt lgkmcnt(0)\n\ts_barrier" ::: "memory"); }

__device__ __forceinline__ void put(gu64* g, u32 tag, float v) {
  __hip_atomic_store(g, ((u64)tag << 32) | (u64)__float_as_uint(v), __ATOMIC_RELAXED, __HIP_MEMORY_SCOPE_AGENT);
}
__device__ __forceinline__ u64 get(const gu64* g) { return __hip_atomic_load(g, __ATOMIC_RELAXED, __HIP_MEMORY_SCOPE_AGENT); }
__device__ __forceinline__ bool tag_ok(u64 x, u32 tag) { return (u32)(x >> 32) == tag; }
__device__ __forceinline__ float val(u64 x) { return __uint_as_float((u32)x); }

template <int LN> __device__ __forceinline__ void gemv_step(float (&acc)[8], float xv, u32x4 w) {
  const float xb = dpp<BCAST + LN>(xv);
#pragma unroll
  for (int k = 0; k < 4; k++) { acc[2 * k] = fmaf(xb, bf_lo(w[k]), acc[2 * k]); acc[2 * k + 1] = fmaf(xb, bf_hi(w[k]), acc[2 * k + 1]); }
}

struct Lds {
  float part[4][256];
  float qp[4][128];
  float ap[4][132];
  u32 q[64];
  u32 ctl[4];
};

#define STAMP(i) do {} while (0)

__global__ void __launch_bounds__(320, 2)
fused_decode(const u16* __restrict__ x, const u16* __restrict__ kc, const u16* __restrict__ vc, const u16* __restrict__ wq,
             const u16* __restrict__ wo, const u16* __restrict__ wg, const u16* __restrict__ wu, const u16* __restrict__ wd,
             u16* __restrict__ out, unsigned char* __restrict__ ws) {
  __shared__ Lds L;
  const int b = blockIdx.x, head = b >> 3, j = b & 7;
  const int lane = threadIdx.x & 63, wave = __builtin_amdgcn_readfirstlane(threadIdx.x >> 6);
  const int r = lane >> 4, c = lane & 15;
  const bool has_ffn = j != (head & 7);
  const int unit = has_ffn ? 7 * head + j - (j > (head & 7) ? 1 : 0) : 0;
  const int ug = unit >> 3, us = unit & 7;
  STAMP(0);

  if (wave < 4) {
    const int w = wave;
    u32x4 X0, X1; u32 xs;
    u32x4 WQ[8], WG[8], WU[8], AK[4], AV[4], BK[4], BV[4];
    u32 tkA = 0, tkB = 0;
    const i32x4 rx = mkr(x, D * 2);
    {
      const u32 xo = lane * 16;
      const int k7 = c & 7;
      const u32 so = (u32)(((c < 8 ? j : us) * 128 + 32 * w + 4 * k7 + r) * 2);
      asm volatile("buffer_load_dwordx4 %0, %1, %2, 0 offen" : "=v"(X0) : "v"(xo), "s"(rx));
      asm volatile("buffer_load_dwordx4 %0, %1, %2, 0 offen offset:1024" : "=v"(X1) : "v"(xo), "s"(rx));
      asm volatile("buffer_load_ushort %0, %1, %2, 0 offen" : "=v"(xs) : "v"(so), "s"(rx));
    }
    {
      const i32x4 rq = mkr(wq, (u32)D * 4096 * 2);
      const u32 vo = r * 8192 + c * 16, s0 = (u32)(j * 128 + 32 * w) * 8192 + head * 256;
#pragma unroll
      for (int i = 0; i < 8; i++) ld_nt_o<0>(WQ[i], vo, rq, s0 + i * 32768);
    }
    wg_barrier();
    const u32 nffn = has_ffn ? (u32)D * FF * 2 : 0u;
    const i32x4 rg = mkr(wg, nffn), ru = mkr(wu, nffn);
    const u32 vg = r * (FF * 2) + c * 16, g0 = (u32)(us * 128 + 32 * w) * (FF * 2) + ug * 256;
#pragma unroll
    for (int i = 0; i < 8; i++) ld_nt_o<0>(WG[i], vg, rg, g0 + i * (4 * FF * 2));
    const char* kb = (const char*)kc + (size_t)head * HEAD_BYTES;
    const char* vb = (const char*)vc + (size_t)head * HEAD_BYTES;
    const u32 voff = lane * 16;
    const u32 aoff = lane == 0 ? 0u : 0x7ffffff0u;
    const unsigned char* ctr = ws + WS_CTR + head * 256;
    const int st0 = j * SPW * 4 + w;
    asm volatile("s_waitcnt vmcnt(16)" : "+v"(X0), "+v"(X1), "+v"(xs));
    float xn;
    {
      float v[16];
#pragma unroll
      for (int k = 0; k < 4; k++) { v[2 * k] = bf_lo(X0[k]); v[2 * k + 1] = bf_hi(X0[k]); v[8 + 2 * k] = bf_lo(X1[k]); v[9 + 2 * k] = bf_hi(X1[k]); }
      float s = 0.f;
#pragma unroll
      for (int k = 0; k < 16; k++) s += v[k];
      const float mean = wave_sum(s) * (1.0f / D);
      float q = 0.f;
#pragma unroll
      for (int k = 0; k < 16; k++) { const float d = v[k] - mean; q = fmaf(d, d, q); }
      const float var = wave_sum(q) * (1.0f / D);
      const float den = rbf(sqrtf(rbf(rbf(var) + 1e-5f)));
      xn = rbf(rbf(bf_lo(xs) - rbf(mean)) / den);
    }
    asm volatile("" : "+v"(xn));
    STAMP(1);
    {
      float a[8] = {0.f, 0.f, 0.f, 0.f, 0.f, 0.f, 0.f, 0.f};
      wait8<8>(WQ);
#pragma unroll
      for (int i = 0; i < 8; i++) ld_nt_o<0>(WU[i], vg, ru, g0 + i * (4 * FF * 2));
      wait8<63>(WQ);
      gemv_step<0>(a, xn, WQ[0]); gemv_step<1>(a, xn, WQ[1]); gemv_step<2>(a, xn, WQ[2]); gemv_step<3>(a, xn, WQ[3]);
      gemv_step<4>(a, xn, WQ[4]); gemv_step<5>(a, xn, WQ[5]); gemv_step<6>(a, xn, WQ[6]); gemv_step<7>(a, xn, WQ[7]);
      *(f32x2*)&L.qp[w][8 * c + 2 * r] = fold_rows(a);
    }
    wg_barrier();
    STAMP(2);
    {
      float a[8] = {0.f, 0.f, 0.f, 0.f, 0.f, 0.f, 0.f, 0.f};
      wait8<8>(WG);
      ticket(tkB, aoff, mkr(ctr, 0));
      issue4_o(AK, voff, mkr(kb, HEAD_BYTES), (u32)st0 * 4096); issue4_o(AV, voff, mkr(vb, HEAD_BYTES), (u32)st0 * 4096);
      wait8<63>(WG);
      gemv_step<8>(a, xn, WG[0]); gemv_step<9>(a, xn, WG[1]); gemv_step<10>(a, xn, WG[2]); gemv_step<11>(a, xn, WG[3]);
      gemv_step<12>(a, xn, WG[4]); gemv_step<13>(a, xn, WG[5]); gemv_step<14>(a, xn, WG[6]); gemv_step<15>(a, xn, WG[7]);
      *(f32x2*)&L.part[w][8 * c + 2 * r] = fold_rows(a);
    }
    {
      float a[8] = {0.f, 0.f, 0.f, 0.f, 0.f, 0.f, 0.f, 0.f};
      wait8<9>(WU);
      ticket(tkA, aoff, mkr(ctr, 0));
      issue4_o(BK, voff, mkr(kb, HEAD_BYTES), (u32)(st0 + 4) * 4096); issue4_o(BV, voff, mkr(vb, HEAD_BYTES), (u32)(st0 + 4) * 4096);
      wait8<63>(WU);
      gemv_step<8>(a, xn, WU[0]); gemv_step<9>(a, xn, WU[1]); gemv_step<10>(a, xn, WU[2]); gemv_step<11>(a, xn, WU[3]);
      gemv_step<12>(a, xn, WU[4]); gemv_step<13>(a, xn, WU[5]); gemv_step<14>(a, xn, WU[6]); gemv_step<15>(a, xn, WU[7]);
      *(f32x2*)&L.part[w][128 + 8 * c + 2 * r] = fold_rows(a);
    }
    STAMP(3);
    wg_barrier();
    STAMP(4);
    const u32x4 q = *(const u32x4*)&L.q[4 * c];
    const u32 tag = __builtin_amdgcn_readfirstlane(L.ctl[0]);
    const u32 base = __builtin_amdgcn_readfirstlane(L.ctl[1]);

    u32x4 W[8];
#pragma unroll
    for (int i = 0; i < 8; i++) asm volatile("" : "=v"(W[i]));
    const i32x4 rwo = mkr(wo, (u32)4096 * D * 2);
    const u32 wvo = (16 * (r >> 1) + (r & 1)) * 2048 + c * 16, wso = (u32)(head * 128 + 32 * w) * 2048 + j * 256;
    float m = -INFINITY, l = 0.f, acc[8] = {0.f, 0.f, 0.f, 0.f, 0.f, 0.f, 0.f, 0.f};
    u32 done = 0;

    auto half_step = [&](u32x4 (&K)[4], u32x4 (&V)[4], u32& tk_use, u32& tk_new, int i, u32 invalid) -> u32 {
      ticket(tk_new, aoff, mkr(ctr, (((u32)(SPW - 2 - i) >> 31) & ~done) << 2));
      wait4<14>(K);
      float d[4];
#pragma unroll
      for (int t = 0; t < 4; t++) d[t] = dot2(K[t][3], q[3], dot2(K[t][2], q[2], dot2(K[t][1], q[1], dot2(K[t][0], q[0], 0.f))));
      asm volatile("" : "+v"(d[0]), "+v"(d[1]), "+v"(d[2]), "+v"(d[3]));
      wait1<9>(tk_use);
      const u32 dyn = min((u32)DYN0 + (__builtin_amdgcn_readfirstlane(tk_use) - base), (u32)NTILE) | (done << 11);
      const u32 t = i < SPW ? (u32)(st0 + 4 * i) : dyn;
      const u32 inv = t >> 11;
      const u32 first_end = inv & ~done;
      const u32 nr = (inv - 1u) & HEAD_BYTES;
      done = inv;
      issue4_or(K, voff, mkr(kb, nr), (u32)t * 4096, first_end, W[0], W[1], W[2], W[3], wvo, rwo, wso, wso + 4096, wso + 8192, wso + 12288);
      asm volatile("" : "+v"(d[0]), "+v"(d[1]), "+v"(d[2]), "+v"(d[3]));
      float p = 0.f;
      if (!invalid) {
        const bool b8 = (c & 8) != 0, b4 = (c & 4) != 0;
        const float e0 = (b8 ? d[2] : d[0]) + dpp<ROR8>(b8 ? d[0] : d[2]);
        const float e1 = (b8 ? d[3] : d[1]) + dpp<ROR8>(b8 ? d[1] : d[3]);
        float f = (b4 ? e1 : e0) + dpp<HMIRROR>(b4 ? e0 : e1);
        f += dpp<XOR1>(f); f += dpp<XOR2>(f);
        const float y = rbf(rbf(f) * 0.08838834764831845f) * 1.4426950408889634f;
        float ym = fmaxf(y, dpp<ROR4>(y)); ym = fmaxf(ym, dpp<ROR8>(ym));
        if (__any(ym > m)) {
          const float mn = fmaxf(m, ym), al = __builtin_amdgcn_exp2f(m - mn);
          l *= al;
#pragma unroll
          for (int k = 0; k < 8; k++) acc[k] *= al;
          m = mn;
        }
        p = __builtin_amdgcn_exp2f(y - m);
        l += p;
      }
      wait4<14>(V);
      if (!invalid) {
        gemv_step<0>(acc, p, V[0]); gemv_step<4>(acc, p, V[1]); gemv_step<8>(acc, p, V[2]); gemv_step<12>(acc, p, V[3]);
      }
      asm volatile("" : "+v"(acc[0]), "+v"(acc[1]), "+v"(acc[2]), "+v"(acc[3]), "+v"(acc[4]), "+v"(acc[5]), "+v"(acc[6]), "+v"(acc[7]));
      issue4_or(V, voff, mkr(vb, nr), (u32)t * 4096, first_end, W[4], W[5], W[6], W[7], wvo, rwo, wso + 16384, wso + 20480, wso + 24576, wso + 28672);
      return inv;
    };
    wait1<17>(tkB);
    int i = 2; u32 iA, iB = 0;
    do {
      iA = half_step(AK, AV, tkA, tkB, i, 0u);
      iB = half_step(BK, BV, tkB, tkA, i + 1, iB);
      i += 2;
    } while (!iA);
    STAMP(5);
    {
      const float M = xrow_max(m), sc = __builtin_amdgcn_exp2f(m - M);
#pragma unroll
      for (int k = 0; k < 8; k++) acc[k] *= sc;
      const float ls = wave_sum(l * sc) * 0.25f;
      *(f32x2*)&L.ap[w][8 * c + 2 * r] = fold_rows(acc);
      if (lane == 0) *(f32x2*)&L.ap[w][128] = f32x2{M, ls};
    }
    wg_barrier();
    STAMP(6);
    {
      const gu64* s1 = (const gu64*)(ws + WS_S1) + (size_t)(head * 8 + r) * 144;
      u64 g[8];
      for (u32 spin = 0;; spin++) {
        g[0] = get(s1 + 32 * w + 2 * c); g[1] = get(s1 + 32 * w + 2 * c + 1); g[2] = get(s1 + 128); g[3] = get(s1 + 129);
        g[4] = get(s1 + 4 * 144 + 32 * w + 2 * c); g[5] = get(s1 + 4 * 144 + 32 * w + 2 * c + 1); g[6] = get(s1 + 4 * 144 + 128); g[7] = get(s1 + 4 * 144 + 129);
        bool ok = true;
#pragma unroll
        for (int k = 0; k < 8; k++) ok &= tag_ok(g[k], tag);
        if (__all(ok) || spin > SPIN_CAP) break;
        __builtin_amdgcn_s_sleep(1);
      }
      STAMP(7);
      const float M = xrow_max(fmaxf(val(g[2]), val(g[6])));
      const float w0 = __builtin_amdgcn_exp2f(val(g[2]) - M), w1 = __builtin_amdgcn_exp2f(val(g[6]) - M);
      const float Lt = xrow_sum(fmaf(w0, val(g[3]), w1 * val(g[7])));
      const float o0 = xrow_sum(fmaf(w0, val(g[0]), w1 * val(g[4]))), o1 = xrow_sum(fmaf(w0, val(g[1]), w1 * val(g[5])));
      float o = rbf(((r & 1) ? o1 : o0) / Lt);
      const float orot = dpp<ROR8>(o);
      o = (r & 2) ? orot : o;
      float a[8] = {0.f, 0.f, 0.f, 0.f, 0.f, 0.f, 0.f, 0.f};
      wait8<0>(W);
      wait4<0>(AK); wait4<0>(AV); wait4<0>(BK); wait4<0>(BV); wait1<0>(tkA); wait1<0>(tkB);
      gemv_step<0>(a, o, W[0]); gemv_step<1>(a, o, W[1]); gemv_step<2>(a, o, W[2]); gemv_step<3>(a, o, W[3]);
      gemv_step<4>(a, o, W[4]); gemv_step<5>(a, o, W[5]); gemv_step<6>(a, o, W[6]); gemv_step<7>(a, o, W[7]);
      *(f32x2*)&L.qp[w][8 * c + 2 * r] = fold_rows(a);
    }
    wg_barrier();
    STAMP(8);
  } else {
    const u32 ep = __hip_atomic_load((gu32*)(ws + WS_EPOCH), __ATOMIC_RELAXED, __HIP_MEMORY_SCOPE_AGENT);
    const u32 cb = __hip_atomic_load((gu32*)(ws + WS_CTR + head * 256), __ATOMIC_RELAXED, __HIP_MEMORY_SCOPE_AGENT);
    const u32 xfin = ((const GAS u32*)x)[2 * b + (lane & 1)];
    wg_barrier();
    const u32 tag = __builtin_amdgcn_readfirstlane(ep) + 1u;
    gu64* qrec = (gu64*)(ws + WS_QREC);
    if (j == 0 && lane == 0) put(qrec + (size_t)b * 144 + 128, tag, __uint_as_float(cb));
    STAMP(1);
    wg_barrier();
    {
      f32x2 s = *(const f32x2*)&L.qp[0][2 * lane];
#pragma unroll
      for (int k = 1; k < 4; k++) s += *(const f32x2*)&L.qp[k][2 * lane];
      put(qrec + (size_t)b * 144 + 2 * lane, tag, s.x); put(qrec + (size_t)b * 144 + 2 * lane + 1, tag, s.y);
      STAMP(2);
      const gu64* qr = qrec + (size_t)head * 8 * 144;
      __builtin_amdgcn_s_sleep(QDELAY);
      u64 g[17];
      for (u32 spin = 0;; spin++) {
#pragma unroll
        for (int k = 0; k < 8; k++) { g[2 * k] = get(qr + k * 144 + 2 * lane); g[2 * k + 1] = get(qr + k * 144 + 2 * lane + 1); }
        g[16] = get(qr + 128);
        bool ok = true;
#pragma unroll
        for (int k = 0; k < 17; k++) ok &= tag_ok(g[k], tag);
        if (__all(ok) || spin > SPIN_CAP) break;
        __builtin_amdgcn_s_sleep(1);
      }
      float q0 = 0.f, q1 = 0.f;
#pragma unroll
      for (int k = 0; k < 8; k++) { q0 += val(g[2 * k]); q1 += val(g[2 * k + 1]); }
      L.q[lane] = cvt_pk(q0, q1);
      if (lane == 0) { L.ctl[0] = tag; L.ctl[1] = (u32)g[16]; }
    }
    STAMP(3);
    wg_barrier();
    STAMP(4);
    if (has_ffn) {
      gu64* gu = (gu64*)(ws + WS_GU);
      f32x2 sg = *(const f32x2*)&L.part[0][2 * lane], su = *(const f32x2*)&L.part[0][128 + 2 * lane];
#pragma unroll
      for (int k = 1; k < 4; k++) { sg += *(const f32x2*)&L.part[k][2 * lane]; su += *(const f32x2*)&L.part[k][128 + 2 * lane]; }
      gu64* mine = gu + (size_t)unit * 256;
      put(mine + 2 * lane, tag, sg.x); put(mine + 2 * lane + 1, tag, sg.y);
      put(mine + 128 + 2 * lane, tag, su.x); put(mine + 128 + 2 * lane + 1, tag, su.y);
      const GAS u32x4* wp = (const GAS u32x4*)(wd + (size_t)(128 * ug + 32 * r) * D + 128 * us + 8 * c);
      u32x4 WD[32];
#pragma unroll
      for (int i = 0; i < 32; i++) WD[i] = __builtin_nontemporal_load(wp + i * (D / 8));
      const gu64* gr = gu + (size_t)ug * 8 * 256;
      float g0, g1, u0, u1;
      for (u32 spin = 0;; spin++) {
        bool ok = true;
        g0 = g1 = u0 = u1 = 0.f;
#pragma unroll
        for (int k = 0; k < 8; k++) {
          const u64 a0 = get(gr + k * 256 + 2 * lane), a1 = get(gr + k * 256 + 2 * lane + 1), b0 = get(gr + k * 256 + 128 + 2 * lane), b1 = get(gr + k * 256 + 129 + 2 * lane);
          ok &= tag_ok(a0, tag) & tag_ok(a1, tag) & tag_ok(b0, tag) & tag_ok(b1, tag);
          g0 += val(a0); g1 += val(a1); u0 += val(b0); u1 += val(b1);
        }
        if (__all(ok) || spin > SPIN_CAP) break;
        __builtin_amdgcn_s_sleep(8);
      }
      auto act = [](float g, float u) {
        g = rbf(g); u = rbf(u);
        const float inner = 0.7978845608028654f * (g + 0.044715f * g * g * g);
        return rbf(rbf(0.5f * g * (1.f + tanhf(inner))) * u);
      };
      const float h0 = act(g0, u0), h1 = act(g1, u1);
      float a[8] = {0.f, 0.f, 0.f, 0.f, 0.f, 0.f, 0.f, 0.f};
#define WD2(k) gemv_step<k>(a, h0, WD[2 * k]); gemv_step<k>(a, h1, WD[2 * k + 1]);
      WD2(0) WD2(1) WD2(2) WD2(3) WD2(4) WD2(5) WD2(6) WD2(7) WD2(8) WD2(9) WD2(10) WD2(11) WD2(12) WD2(13) WD2(14) WD2(15)
#undef WD2
      const f32x2 s = fold_rows(a);
      gu64* fr = (gu64*)(ws + WS_FR) + (size_t)unit * 128 + 8 * c + 2 * r;
      put(fr, tag, s.x); put(fr + 1, tag, s.y);
    }
    STAMP(5);
    const int jj = b >> 5, col = (b & 31) * 4 + 2 * (lane & 1);
    float f0, f1;
    {
      const int rg = lane >> 1;
      const gu64* fr = (const gu64*)(ws + WS_FR) + (size_t)((rg < 28 ? rg : 27) * 8 + jj) * 128 + col;
      for (u32 spin = 0;; spin++) {
        const u64 a0 = get(fr), a1 = get(fr + 1);
        f0 = rg < 28 ? val(a0) : 0.f; f1 = rg < 28 ? val(a1) : 0.f;
        if (__all(tag_ok(a0, tag) & tag_ok(a1, tag)) || spin > SPIN_CAP) break;
        __builtin_amdgcn_s_sleep(8);
      }
      f0 += dpp<XOR2>(f0); f0 += dpp<ROR4>(f0); f0 += dpp<ROR8>(f0); f0 = xrow_sum(f0);
      f1 += dpp<XOR2>(f1); f1 += dpp<ROR4>(f1); f1 += dpp<ROR8>(f1); f1 = xrow_sum(f1);
    }
    STAMP(9);
    wg_barrier();
    STAMP(6);
    {
      float mw[4], lw[4];
#pragma unroll
      for (int k = 0; k < 4; k++) { const f32x2 t = *(const f32x2*)&L.ap[k][128]; mw[k] = t.x; lw[k] = t.y; }
      const float M = fmaxf(fmaxf(mw[0], mw[1]), fmaxf(mw[2], mw[3]));
      float Lt = 0.f; f32x2 o = {0.f, 0.f};
#pragma unroll
      for (int k = 0; k < 4; k++) {
        const float s = __builtin_amdgcn_exp2f(mw[k] - M);
        Lt = fmaf(s, lw[k], Lt);
        o += s * *(const f32x2*)&L.ap[k][2 * lane];
      }
      gu64* s1 = (gu64*)(ws + WS_S1) + (size_t)b * 144;
      put(s1 + 2 * lane, tag, o.x); put(s1 + 2 * lane + 1, tag, o.y);
      if (lane < 2) put(s1 + 128 + lane, tag, lane == 0 ? M : Lt);
    }
    STAMP(7);
    wg_barrier();
    STAMP(8);
    {
      f32x2 s = *(const f32x2*)&L.qp[0][2 * lane];
#pragma unroll
      for (int k = 1; k < 4; k++) s += *(const f32x2*)&L.qp[k][2 * lane];
      gu64* s2 = (gu64*)(ws + WS_S2);
      put(s2 + (size_t)b * 128 + 2 * lane, tag, s.x); put(s2 + (size_t)b * 128 + 2 * lane + 1, tag, s.y);
      const gu64* sr = s2 + (size_t)((lane >> 1) * 8 + jj) * 128 + col;
      float a0, a1;
      for (u32 spin = 0;; spin++) {
        const u64 g0 = get(sr), g1 = get(sr + 1);
        a0 = val(g0); a1 = val(g1);
        if (__all(tag_ok(g0, tag) & tag_ok(g1, tag)) || spin > SPIN_CAP) break;
        __builtin_amdgcn_s_sleep(1);
      }
      a0 += dpp<XOR2>(a0); a0 += dpp<ROR4>(a0); a0 += dpp<ROR8>(a0); a0 = xrow_sum(a0);
      a1 += dpp<XOR2>(a1); a1 += dpp<ROR4>(a1); a1 += dpp<ROR8>(a1); a1 = xrow_sum(a1);
      const float r0 = rbf(bf_lo(xfin) + rbf(a0)) + rbf(f0), r1 = rbf(bf_hi(xfin) + rbf(a1)) + rbf(f1);
      if (lane < 2) ((GAS u32*)out)[2 * b + lane] = cvt_pk(r0, r1);
      if (b == 0 && lane == 0) __hip_atomic_store((gu32*)(ws + WS_EPOCH), tag, __ATOMIC_RELAXED, __HIP_MEMORY_SCOPE_AGENT);
    }
    STAMP(10);
  }
}

}

extern "C" void kernel_launch(void* const* d_in, const int* in_sizes, int n_in, void* d_out, int out_size, void* d_ws, size_t ws_size,
                              hipStream_t stream) {
  (void)in_sizes; (void)n_in; (void)out_size; (void)ws_size;
  fused_decode<<<256, 320, 0, stream>>>((const u16*)d_in[0], (const u16*)d_in[1], (const u16*)d_in[2], (const u16*)d_in[3], (const u16*)d_in[4],
                                        (const u16*)d_in[6], (const u16*)d_in[7], (const u16*)d_in[8], (u16*)d_out, (unsigned char*)d_ws);
}
```

```cpp
#define PROBE 1
#include <hip/hip_runtime.h>
#include <stdint.h>

namespace {

typedef unsigned u32;
typedef unsigned long long u64;
typedef unsigned short u16;
typedef u32 u32x4 __attribute__((ext_vector_type(4)));
typedef int i32x4 __attribute__((ext_vector_type(4)));
typedef float f32x2 __attribute__((ext_vector_type(2)));
typedef __attribute__((ext_vector_type(2))) __bf16 bf16x2;
#define GAS __attribute__((address_space(1)))
typedef GAS u64 gu64;
typedef GAS u32 gu32;

constexpr int D = 1024, NH = 32, SK = 32768, DH = 128, FF = 3584;
constexpr int NTILE = SK / 16;
#define QDELAY 24
#define SPW_ 48
constexpr int SPW = SPW_;
static_assert(NTILE == 2048 && SPW % 2 == 0 && SPW >= 4 && SPW <= 64, "");
constexpr int DYN0 = SPW * 32;
constexpr u32 HEAD_BYTES = (u32)SK * DH * 2;

constexpr size_t WS_EPOCH = 0;
constexpr size_t WS_CTR = 4096;
constexpr size_t WS_QREC = 16384;
constexpr size_t WS_S1 = WS_QREC + 256 * 144 * 8;
constexpr size_t WS_S2 = WS_S1 + 256 * 144 * 8;
constexpr size_t WS_GU = WS_S2 + 256 * 128 * 8;
constexpr size_t WS_FR = WS_GU + 224 * 256 * 8;
constexpr size_t WS_STAMP = WS_FR + 224 * 128 * 8;
constexpr u32 SPIN_CAP = 1u << 16;

__device__ __forceinline__ float bf_lo(u32 v) { return __uint_as_float(v << 16); }
__device__ __forceinline__ float bf_hi(u32 v) { return __uint_as_float(v & 0xffff0000u); }
__device__ __forceinline__ u32 cvt_pk(float lo, float hi) {
  bf16x2 r = {(__bf16)lo, (__bf16)hi};
  return __builtin_bit_cast(u32, r);
}
__device__ __forceinline__ float rbf(float x) { return bf_lo((u32)__builtin_bit_cast(u16, (__bf16)x)); }
__device__ __forceinline__ float dot2(u32 a, u32 b, float c) {
  return __builtin_amdgcn_fdot2_f32_bf16(__builtin_bit_cast(bf16x2, a), __builtin_bit_cast(bf16x2, b), c, false);
}
template <int CTRL> __device__ __forceinline__ float dpp(float x) {
  return __builtin_bit_cast(float, __builtin_amdgcn_mov_dpp(__builtin_bit_cast(int, x), CTRL, 0xf, 0xf, true));
}
constexpr int XOR1 = 0xB1, XOR2 = 0x4E, HMIRROR = 0x141, ROR8 = 0x128, ROR4 = 0x124, BCAST = 0x150;
__device__ __forceinline__ float swap32_add(float a, float b) {
  auto s = __builtin_amdgcn_permlane32_swap(__float_as_uint(a), __float_as_uint(b), false, false);
  return __uint_as_float(s[0]) + __uint_as_float(s[1]);
}
__device__ __forceinline__ float swap16_add(float a, float b) {
  auto s = __builtin_amdgcn_permlane16_swap(__float_as_uint(a), __float_as_uint(b), false, false);
  return __uint_as_float(s[0]) + __uint_as_float(s[1]);
}
__device__ __forceinline__ float xrow_sum(float x) { return swap32_add(swap16_add(x, x), swap16_add(x, x)); }
__device__ __forceinline__ float xrow_max(float x) {
  auto s = __builtin_amdgcn_permlane16_swap(__float_as_uint(x), __float_as_uint(x), false, false);
  x = fmaxf(__uint_as_float(s[0]), __uint_as_float(s[1]));
  auto t = __builtin_amdgcn_permlane32_swap(__float_as_uint(x), __float_as_uint(x), false, false);
  return fmaxf(__uint_as_float(t[0]), __uint_as_float(t[1]));
}
__device__ __forceinline__ float row_sum(float x) { x += dpp<XOR1>(x); x += dpp<XOR2>(x); x += dpp<ROR4>(x); x += dpp<ROR8>(x); return x; }
__device__ __forceinline__ float wave_sum(float x) { return xrow_sum(row_sum(x)); }
__device__ __forceinline__ f32x2 fold_rows(const float (&a)[8]) {
  float s0 = swap32_add(a[0], a[4]), s1 = swap32_add(a[1], a[5]), s2 = swap32_add(a[2], a[6]), s3 = swap32_add(a[3], a[7]);
  return f32x2{swap16_add(s0, s2), swap16_add(s1, s3)};
}

__device__ __forceinline__ i32x4 mkr(const void* p, u32 n) {
  u64 a = (u64)p;
  i32x4 r; r.x = (int)(u32)a; r.y = (int)((u32)(a >> 32) & 0xffffu); r.z = (int)n; r.w = 0x00020000; return r;
}
template <int IMM> __device__ __forceinline__ void ld_nt(u32x4& d, u32 voff, i32x4 r, u32 soff) {
  asm volatile("buffer_load_dwordx4 %0, %1, %2, %3 offen offset:%4 nt" : "+v"(d) : "v"(voff), "s"(r), "s"(soff), "i"(IMM));
}
template <int IMM> __device__ __forceinline__ void ld_nt_o(u32x4& d, u32 voff, i32x4 r, u32 soff) {
  asm volatile("buffer_load_dwordx4 %0, %1, %2, %3 offen offset:%4 nt" : "=v"(d) : "v"(voff), "s"(r), "s"(soff), "i"(IMM));
}
__device__ __forceinline__ void issue4(u32x4 (&R)[4], u32 voff, i32x4 r, u32 soff) {
  ld_nt<0>(R[0], voff, r, soff); ld_nt<1024>(R[1], voff, r, soff); ld_nt<2048>(R[2], voff, r, soff); ld_nt<3072>(R[3], voff, r, soff);
}
__device__ __forceinline__ void issue4_o(u32x4 (&R)[4], u32 voff, i32x4 r, u32 soff) {
  ld_nt_o<0>(R[0], voff, r, soff); ld_nt_o<1024>(R[1], voff, r, soff); ld_nt_o<2048>(R[2], voff, r, soff); ld_nt_o<3072>(R[3], voff, r, soff);
}

__device__ __forceinline__ void issue4_or(u32x4 (&R)[4], u32 voff, i32x4 r, u32 soff, u32 alt, u32x4& W0, u32x4& W1, u32x4& W2, u32x4& W3,
                                          u32 wvo, i32x4 rw, u32 w0, u32 w1, u32 w2, u32 w3) {
  asm volatile(
      "s_cmp_lg_u32 %11, 0\n\t"
      "s_cbranch_scc1 1f\n\t"
      "buffer_load_dwordx4 %0, %8, %9, %10 offen nt\n\t"
      "buffer_load_dwordx4 %1, %8, %9, %10 offen offset:1024 nt\n\t"
      "buffer_load_dwordx4 %2, %8, %9, %10 offen offset:2048 nt\n\t"
      "buffer_load_dwordx4 %3, %8, %9, %10 offen offset:3072 nt\n\t"
      "s_branch 2f\n"
      "1:\n\t"
      "buffer_load_dwordx4 %4, %12, %13, %14 offen nt\n\t"
      "buffer_load_dwordx4 %5, %12, %13, %15 offen nt\n\t"
      "buffer_load_dwordx4 %6, %12, %13, %16 offen nt\n\t"
      "buffer_load_dwordx4 %7, %12, %13, %17 offen nt\n"
      "2:"
      : "+v"(R[0]), "+v"(R[1]), "+v"(R[2]), "+v"(R[3]), "+v"(W0), "+v"(W1), "+v"(W2), "+v"(W3)
      : "v"(voff), "s"(r), "s"(soff), "s"(alt), "v"(wvo), "s"(rw), "s"(w0), "s"(w1), "s"(w2), "s"(w3)
      : "scc");
}
template <int N> __device__ __forceinline__ void wait4(u32x4 (&R)[4]) {
  asm volatile("s_waitcnt vmcnt(%4)" : "+v"(R[0]), "+v"(R[1]), "+v"(R[2]), "+v"(R[3]) : "i"(N));
}
template <int N> __device__ __forceinline__ void wait8(u32x4 (&R)[8]) {
  asm volatile("s_waitcnt vmcnt(%8)" : "+v"(R[0]), "+v"(R[1]), "+v"(R[2]), "+v"(R[3]), "+v"(R[4]), "+v"(R[5]), "+v"(R[6]), "+v"(R[7]) : "i"(N));
}
template <int N> __device__ __forceinline__ void wait1(u32& t) { asm volatile("s_waitcnt vmcnt(%1)" : "+v"(t) : "i"(N)); }
__device__ __forceinline__ void ticket(u32& t, u32 aoff, i32x4 r) {
  asm volatile("v_mov_b32 %0, 1\n\tbuffer_atomic_add %0, %1, %2, 0 offen sc0" : "+v"(t) : "v"(aoff), "s"(r));
}
__device__ __forceinline__ void wg_barrier() { asm volatile("s_waitcnt lgkmcnt(0)\n\ts_barrier" ::: "memory"); }

__device__ __forceinline__ void put(gu64* g, u32 tag, float v) {
  __hip_atomic_store(g, ((u64)tag << 32) | (u64)__float_as_uint(v), __ATOMIC_RELAXED, __HIP_MEMORY_SCOPE_AGENT);
}
__device__ __forceinline__ u64 get(const gu64* g) { return __hip_atomic_load(g, __ATOMIC_RELAXED, __HIP_MEMORY_SCOPE_AGENT); }
__device__ __forceinline__ bool tag_ok(u64 x, u32 tag) { return (u32)(x >> 32) == tag; }
__device__ __forceinline__ float val(u64 x) { return __uint_as_float((u32)x); }

template <int LN> __device__ __forceinline__ void gemv_step(float (&acc)[8], float xv, u32x4 w) {
  const float xb = dpp<BCAST + LN>(xv);
#pragma unroll
  for (int k = 0; k < 4; k++) { acc[2 * k] = fmaf(xb, bf_lo(w[k]), acc[2 * k]); acc[2 * k + 1] = fmaf(xb, bf_hi(w[k]), acc[2 * k + 1]); }
}

struct Lds {
  float part[4][256];
  float qp[4][128];
  float ap[4][132];
  u32 q[64];
  u32 ctl[4];
};

#define STAMP(i) do {} while (0)

__global__ void __launch_bounds__(320, 2)
fused_decode(const u16* __restrict__ x, const u16* __restrict__ kc, const u16* __restrict__ vc, const u16* __restrict__ wq,
             const u16* __restrict__ wo, const u16* __restrict__ wg, const u16* __restrict__ wu, const u16* __restrict__ wd,
             u16* __restrict__ out, unsigned char* __restrict__ ws) {
  __shared__ Lds L;
  const int b = blockIdx.x, head = b >> 3, j = b & 7;
  const int lane = threadIdx.x & 63, wave = __builtin_amdgcn_readfirstlane(threadIdx.x >> 6);
  const int r = lane >> 4, c = lane & 15;
  const bool has_ffn = j != (head & 7);
  const int unit = has_ffn ? 7 * head + j - (j > (head & 7) ? 1 : 0) : 0;
  const int ug = unit >> 3, us = unit & 7;
  STAMP(0);

  if (wave < 4) {
    const int w = wave;
    u32x4 X0, X1; u32 xs;
    u32x4 WQ[8], WG[8], WU[8], AK[4], AV[4], BK[4], BV[4];
    u32 tkA = 0, tkB = 0;
    const i32x4 rx = mkr(x, D * 2);
    {
      const u32 xo = lane * 16;
      const int k7 = c & 7;
      const u32 so = (u32)(((c < 8 ? j : us) * 128 + 32 * w + 4 * k7 + r) * 2);
      asm volatile("buffer_load_dwordx4 %0, %1, %2, 0 offen" : "=v"(X0) : "v"(xo), "s"(rx));
      asm volatile("buffer_load_dwordx4 %0, %1, %2, 0 offen offset:1024" : "=v"(X1) : "v"(xo), "s"(rx));
      asm volatile("buffer_load_ushort %0, %1, %2, 0 offen" : "=v"(xs) : "v"(so), "s"(rx));
    }
    {
      const i32x4 rq = mkr(wq, (u32)D * 4096 * 2);
      const u32 vo = r * 8192 + c * 16, s0 = (u32)(j * 128 + 32 * w) * 8192 + head * 256;
#pragma unroll
      for (int i = 0; i < 8; i++) ld_nt_o<0>(WQ[i], vo, rq, s0 + i * 32768);
    }
    wg_barrier();
    const u32 nffn = has_ffn ? (u32)D * FF * 2 : 0u;
    const i32x4 rg = mkr(wg, nffn), ru = mkr(wu, nffn);
    const u32 vg = r * (FF * 2) + c * 16, g0 = (u32)(us * 128 + 32 * w) * (FF * 2) + ug * 256;
#pragma unroll
    for (int i = 0; i < 8; i++) ld_nt_o<0>(WG[i], vg, rg, g0 + i * (4 * FF * 2));
    const char* kb = (const char*)kc + (size_t)head * HEAD_BYTES;
    const char* vb = (const char*)vc + (size_t)head * HEAD_BYTES;
    const u32 voff = lane * 16;
    const u32 aoff = lane == 0 ? 0u : 0x7ffffff0u;
    const unsigned char* ctr = ws + WS_CTR + head * 256;
    const int st0 = j * SPW * 4 + w;
    asm volatile("s_waitcnt vmcnt(16)" : "+v"(X0), "+v"(X1), "+v"(xs));
    float xn;
    {
      float v[16];
#pragma unroll
      for (int k = 0; k < 4; k++) { v[2 * k] = bf_lo(X0[k]); v[2 * k + 1] = bf_hi(X0[k]); v[8 + 2 * k] = bf_lo(X1[k]); v[9 + 2 * k] = bf_hi(X1[k]); }
      float s = 0.f;
#pragma unroll
      for (int k = 0; k < 16; k++) s += v[k];
      const float mean = wave_sum(s) * (1.0f / D);
      float q = 0.f;
#pragma unroll
      for (int k = 0; k < 16; k++) { const float d = v[k] - mean; q = fmaf(d, d, q); }
      const float var = wave_sum(q) * (1.0f / D);
      const float den = rbf(sqrtf(rbf(rbf(var) + 1e-5f)));
      xn = rbf(rbf(bf_lo(xs) - rbf(mean)) / den);
    }
    asm volatile("" : "+v"(xn));
    STAMP(1);
    {
      float a[8] = {0.f, 0.f, 0.f, 0.f, 0.f, 0.f, 0.f, 0.f};
      wait8<8>(WQ);
#pragma unroll
      for (int i = 0; i < 8; i++) ld_nt_o<0>(WU[i], vg, ru, g0 + i * (4 * FF * 2));
      wait8<63>(WQ);
      gemv_step<0>(a, xn, WQ[0]); gemv_step<1>(a, xn, WQ[1]); gemv_step<2>(a, xn, WQ[2]); gemv_step<3>(a, xn, WQ[3]);
      gemv_step<4>(a, xn, WQ[4]); gemv_step<5>(a, xn, WQ[5]); gemv_step<6>(a, xn, WQ[6]); gemv_step<7>(a, xn, WQ[7]);
      *(f32x2*)&L.qp[w][8 * c + 2 * r] = fold_rows(a);
    }
    wg_barrier();
    STAMP(2);
    {
      float a[8] = {0.f, 0.f, 0.f, 0.f, 0.f, 0.f, 0.f, 0.f};
      wait8<8>(WG);
      ticket(tkB, aoff, mkr(ctr, 0));
      issue4_o(AK, voff, mkr(kb, HEAD_BYTES), (u32)st0 * 4096); issue4_o(AV, voff, mkr(vb, HEAD_BYTES), (u32)st0 * 4096);
      wait8<63>(WG);
      gemv_step<8>(a, xn, WG[0]); gemv_step<9>(a, xn, WG[1]); gemv_step<10>(a, xn, WG[2]); gemv_step<11>(a, xn, WG[3]);
      gemv_step<12>(a, xn, WG[4]); gemv_step<13>(a, xn, WG[5]); gemv_step<14>(a, xn, WG[6]); gemv_step<15>(a, xn, WG[7]);
      *(f32x2*)&L.part[w][8 * c + 2 * r] = fold_rows(a);
    }
    {
      float a[8] = {0.f, 0.f, 0.f, 0.f, 0.f, 0.f, 0.f, 0.f};
      wait8<9>(WU);
      ticket(tkA, aoff, mkr(ctr, 0));
      issue4_o(BK, voff, mkr(kb, HEAD_BYTES), (u32)(st0 + 4) * 4096); issue4_o(BV, voff, mkr(vb, HEAD_BYTES), (u32)(st0 + 4) * 4096);
      wait8<63>(WU);
      gemv_step<8>(a, xn, WU[0]); gemv_step<9>(a, xn, WU[1]); gemv_step<10>(a, xn, WU[2]); gemv_step<11>(a, xn, WU[3]);
      gemv_step<12>(a, xn, WU[4]); gemv_step<13>(a, xn, WU[5]); gemv_step<14>(a, xn, WU[6]); gemv_step<15>(a, xn, WU[7]);
      *(f32x2*)&L.part[w][128 + 8 * c + 2 * r] = fold_rows(a);
    }
    STAMP(3);
    wg_barrier();
    STAMP(4);
    const u32x4 q = *(const u32x4*)&L.q[4 * c];
    const u32 tag = __builtin_amdgcn_readfirstlane(L.ctl[0]);
    const u32 base = __builtin_amdgcn_readfirstlane(L.ctl[1]);

    u32x4 W[8];
#pragma unroll
    for (int i = 0; i < 8; i++) asm volatile("" : "=v"(W[i]));
    const i32x4 rwo = mkr(wo, (u32)4096 * D * 2);
    const u32 wvo = (16 * (r >> 1) + (r & 1)) * 2048 + c * 16, wso = (u32)(head * 128 + 32 * w) * 2048 + j * 256;
    float m = -INFINITY, l = 0.f, acc[8] = {0.f, 0.f, 0.f, 0.f, 0.f, 0.f, 0.f, 0.f};
    u32 done = 0;

    auto half_step = [&](u32x4 (&K)[4], u32x4 (&V)[4], u32& tk_use, u32& tk_new, int i, u32 invalid) -> u32 {
      ticket(tk_new, aoff, mkr(ctr, (((u32)(SPW - 2 - i) >> 31) & ~done) << 2));
      wait4<14>(K);
      float d[4];
#pragma unroll
      for (int t = 0; t < 4; t++) d[t] = dot2(K[t][3], q[3], dot2(K[t][2], q[2], dot2(K[t][1], q[1], dot2(K[t][0], q[0], 0.f))));
      asm volatile("" : "+v"(d[0]), "+v"(d[1]), "+v"(d[2]), "+v"(d[3]));
      wait1<9>(tk_use);
      const u32 dyn = min((u32)DYN0 + (__builtin_amdgcn_readfirstlane(tk_use) - base), (u32)NTILE) | (done << 11);
      const u32 t = i < SPW ? (u32)(st0 + 4 * i) : dyn;
      const u32 inv = t >> 11;
      const u32 first_end = inv & ~done;
      const u32 nr = (inv - 1u) & HEAD_BYTES;
      done = inv;
      issue4_or(K, voff, mkr(kb, nr), (u32)t * 4096, first_end, W[0], W[1], W[2], W[3], wvo, rwo, wso, wso + 4096, wso + 8192, wso + 12288);
      asm volatile("" : "+v"(d[0]), "+v"(d[1]), "+v"(d[2]), "+v"(d[3]));
      float p = 0.f;
      if (!invalid) {
        const bool b8 = (c & 8) != 0, b4 = (c & 4) != 0;
        const float e0 = (b8 ? d[2] : d[0]) + dpp<ROR8>(b8 ? d[0] : d[2]);
        const float e1 = (b8 ? d[3] : d[1]) + dpp<ROR8>(b8 ? d[1] : d[3]);
        float f = (b4 ? e1 : e0) + dpp<HMIRROR>(b4 ? e0 : e1);
        f += dpp<XOR1>(f); f += dpp<XOR2>(f);
        const float y = rbf(rbf(f) * 0.08838834764831845f) * 1.4426950408889634f;
        float ym = fmaxf(y, dpp<ROR4>(y)); ym = fmaxf(ym, dpp<ROR8>(ym));
        if (__any(ym > m)) {
          const float mn = fmaxf(m, ym), al = __builtin_amdgcn_exp2f(m - mn);
          l *= al;
#pragma unroll
          for (int k = 0; k < 8; k++) acc[k] *= al;
          m = mn;
        }
        p = __builtin_amdgcn_exp2f(y - m);
        l += p;
      }
      wait4<14>(V);
      if (!invalid) {
        gemv_step<0>(acc, p, V[0]); gemv_step<4>(acc, p, V[1]); gemv_step<8>(acc, p, V[2]); gemv_step<12>(acc, p, V[3]);
      }
      asm volatile("" : "+v"(acc[0]), "+v"(acc[1]), "+v"(acc[2]), "+v"(acc[3]), "+v"(acc[4]), "+v"(acc[5]), "+v"(acc[6]), "+v"(acc[7]));
      issue4_or(V, voff, mkr(vb, nr), (u32)t * 4096, first_end, W[4], W[5], W[6], W[7], wvo, rwo, wso + 16384, wso + 20480, wso + 24576, wso + 28672);
      return inv;
    };
    wait1<17>(tkB);
    int i = 2; u32 iA, iB = 0;
    do {
      iA = half_step(AK, AV, tkA, tkB, i, 0u);
      iB = half_step(BK, BV, tkB, tkA, i + 1, iB);
      i += 2;
    } while (!iA);
    STAMP(5);
    {
      const float M = xrow_max(m), sc = __builtin_amdgcn_exp2f(m - M);
#pragma unroll
      for (int k = 0; k < 8; k++) acc[k] *= sc;
      const float ls = wave_sum(l * sc) * 0.25f;
      *(f32x2*)&L.ap[w][8 * c + 2 * r] = fold_rows(acc);
      if (lane == 0) *(f32x2*)&L.ap[w][128] = f32x2{M, ls};
    }
    wg_barrier();
    STAMP(6);
    {
      const gu64* s1 = (const gu64*)(ws + WS_S1) + (size_t)(head * 8 + r) * 144;
      u64 g[8];
      for (u32 spin = 0;; spin++) {
        g[0] = get(s1 + 32 * w + 2 * c); g[1] = get(s1 + 32 * w + 2 * c + 1); g[2] = get(s1 + 128); g[3] = get(s1 + 129);
        g[4] = get(s1 + 4 * 144 + 32 * w + 2 * c); g[5] = get(s1 + 4 * 144 + 32 * w + 2 * c + 1); g[6] = get(s1 + 4 * 144 + 128); g[7] = get(s1 + 4 * 144 + 129);
        bool ok = true;
#pragma unroll
        for (int k = 0; k < 8; k++) ok &= tag_ok(g[k], tag);
        if (__all(ok) || spin > SPIN_CAP) break;
        __builtin_amdgcn_s_sleep(1);
      }
      STAMP(7);
      const float M = xrow_max(fmaxf(val(g[2]), val(g[6])));
      const float w0 = __builtin_amdgcn_exp2f(val(g[2]) - M), w1 = __builtin_amdgcn_exp2f(val(g[6]) - M);
      const float Lt = xrow_sum(fmaf(w0, val(g[3]), w1 * val(g[7])));
      const float o0 = xrow_sum(fmaf(w0, val(g[0]), w1 * val(g[4]))), o1 = xrow_sum(fmaf(w0, val(g[1]), w1 * val(g[5])));
      float o = rbf(((r & 1) ? o1 : o0) / Lt);
      const float orot = dpp<ROR8>(o);
      o = (r & 2) ? orot : o;
      float a[8] = {0.f, 0.f, 0.f, 0.f, 0.f, 0.f, 0.f, 0.f};
      wait8<0>(W);
      wait4<0>(AK); wait4<0>(AV); wait4<0>(BK); wait4<0>(BV); wait1<0>(tkA); wait1<0>(tkB);
      gemv_step<0>(a, o, W[0]); gemv_step<1>(a, o, W[1]); gemv_step<2>(a, o, W[2]); gemv_step<3>(a, o, W[3]);
      gemv_step<4>(a, o, W[4]); gemv_step<5>(a, o, W[5]); gemv_step<6>(a, o, W[6]); gemv_step<7>(a, o, W[7]);
      *(f32x2*)&L.qp[w][8 * c + 2 * r] = fold_rows(a);
    }
    wg_barrier();
    STAMP(8);
  } else {
    const u64 T0 = __builtin_amdgcn_s_memrealtime(); u64 T2 = 0, T3 = 0;
    const u32 ep = __hip_atomic_load((gu32*)(ws + WS_EPOCH), __ATOMIC_RELAXED, __HIP_MEMORY_SCOPE_AGENT);
    const u32 cb = __hip_atomic_load((gu32*)(ws + WS_CTR + head * 256), __ATOMIC_RELAXED, __HIP_MEMORY_SCOPE_AGENT);
    const u32 xfin = ((const GAS u32*)x)[2 * b + (lane & 1)];
    wg_barrier();
    const u32 tag = __builtin_amdgcn_readfirstlane(ep) + 1u;
    gu64* qrec = (gu64*)(ws + WS_QREC);
    if (j == 0 && lane == 0) put(qrec + (size_t)b * 144 + 128, tag, __uint_as_float(cb));
    STAMP(1);
    wg_barrier();
    {
      f32x2 s = *(const f32x2*)&L.qp[0][2 * lane];
#pragma unroll
      for (int k = 1; k < 4; k++) s += *(const f32x2*)&L.qp[k][2 * lane];
      put(qrec + (size_t)b * 144 + 2 * lane, tag, s.x); put(qrec + (size_t)b * 144 + 2 * lane + 1, tag, s.y);
      STAMP(2);
      const gu64* qr = qrec + (size_t)head * 8 * 144;
      __builtin_amdgcn_s_sleep(QDELAY);
      u64 g[17];
      for (u32 spin = 0;; spin++) {
#pragma unroll
        for (int k = 0; k < 8; k++) { g[2 * k] = get(qr + k * 144 + 2 * lane); g[2 * k + 1] = get(qr + k * 144 + 2 * lane + 1); }
        g[16] = get(qr + 128);
        bool ok = true;
#pragma unroll
        for (int k = 0; k < 17; k++) ok &= tag_ok(g[k], tag);
        if (__all(ok) || spin > SPIN_CAP) break;
        __builtin_amdgcn_s_sleep(1);
      }
      float q0 = 0.f, q1 = 0.f;
#pragma unroll
      for (int k = 0; k < 8; k++) { q0 += val(g[2 * k]); q1 += val(g[2 * k + 1]); }
      L.q[lane] = cvt_pk(q0, q1);
      if (lane == 0) { L.ctl[0] = tag; L.ctl[1] = (u32)g[16]; }
    }
    STAMP(3);
    wg_barrier();
    T2 = __builtin_amdgcn_s_memrealtime();
    if (has_ffn) {
      gu64* gu = (gu64*)(ws + WS_GU);
      f32x2 sg = *(const f32x2*)&L.part[0][2 * lane], su = *(const f32x2*)&L.part[0][128 + 2 * lane];
#pragma unroll
      for (int k = 1; k < 4; k++) { sg += *(const f32x2*)&L.part[k][2 * lane]; su += *(const f32x2*)&L.part[k][128 + 2 * lane]; }
      gu64* mine = gu + (size_t)unit * 256;
      put(mine + 2 * lane, tag, sg.x); put(mine + 2 * lane + 1, tag, sg.y);
      put(mine + 128 + 2 * lane, tag, su.x); put(mine + 128 + 2 * lane + 1, tag, su.y);
      const GAS u32x4* wp = (const GAS u32x4*)(wd + (size_t)(128 * ug + 32 * r) * D + 128 * us + 8 * c);
      u32x4 WD[32];
#pragma unroll
      for (int i = 0; i < 32; i++) WD[i] = __builtin_nontemporal_load(wp + i * (D / 8));
      const gu64* gr = gu + (size_t)ug * 8 * 256;
      float g0, g1, u0, u1;
      for (u32 spin = 0;; spin++) {
        bool ok = true;
        g0 = g1 = u0 = u1 = 0.f;
#pragma unroll
        for (int k = 0; k < 8; k++) {
          const u64 a0 = get(gr + k * 256 + 2 * lane), a1 = get(gr + k * 256 + 2 * lane + 1), b0 = get(gr + k * 256 + 128 + 2 * lane), b1 = get(gr + k * 256 + 129 + 2 * lane);
          ok &= tag_ok(a0, tag) & tag_ok(a1, tag) & tag_ok(b0, tag) & tag_ok(b1, tag);
          g0 += val(a0); g1 += val(a1); u0 += val(b0); u1 += val(b1);
        }
        if (__all(ok) || spin > SPIN_CAP) break;
        __builtin_amdgcn_s_sleep(8);
      }
      auto act = [](float g, float u) {
        g = rbf(g); u = rbf(u);
        const float inner = 0.7978845608028654f * (g + 0.044715f * g * g * g);
        return rbf(rbf(0.5f * g * (1.f + tanhf(inner))) * u);
      };
      const float h0 = act(g0, u0), h1 = act(g1, u1);
      float a[8] = {0.f, 0.f, 0.f, 0.f, 0.f, 0.f, 0.f, 0.f};
#define WD2(k) gemv_step<k>(a, h0, WD[2 * k]); gemv_step<k>(a, h1, WD[2 * k + 1]);
      WD2(0) WD2(1) WD2(2) WD2(3) WD2(4) WD2(5) WD2(6) WD2(7) WD2(8) WD2(9) WD2(10) WD2(11) WD2(12) WD2(13) WD2(14) WD2(15)
#undef WD2
      const f32x2 s = fold_rows(a);
      gu64* fr = (gu64*)(ws + WS_FR) + (size_t)unit * 128 + 8 * c + 2 * r;
      put(fr, tag, s.x); put(fr + 1, tag, s.y);
    }
    STAMP(5);
    const int jj = b >> 5, col = (b & 31) * 4 + 2 * (lane & 1);
    float f0, f1;
    {
      const int rg = lane >> 1;
      const gu64* fr = (const gu64*)(ws + WS_FR) + (size_t)((rg < 28 ? rg : 27) * 8 + jj) * 128 + col;
      for (u32 spin = 0;; spin++) {
        const u64 a0 = get(fr), a1 = get(fr + 1);
        f0 = rg < 28 ? val(a0) : 0.f; f1 = rg < 28 ? val(a1) : 0.f;
        if (__all(tag_ok(a0, tag) & tag_ok(a1, tag)) || spin > SPIN_CAP) break;
        __builtin_amdgcn_s_sleep(8);
      }
      f0 += dpp<XOR2>(f0); f0 += dpp<ROR4>(f0); f0 += dpp<ROR8>(f0); f0 = xrow_sum(f0);
      f1 += dpp<XOR2>(f1); f1 += dpp<ROR4>(f1); f1 += dpp<ROR8>(f1); f1 = xrow_sum(f1);
    }
    STAMP(9);
    wg_barrier();
    T3 = __builtin_amdgcn_s_memrealtime();
    STAMP(6);
    {
      float mw[4], lw[4];
#pragma unroll
      for (int k = 0; k < 4; k++) { const f32x2 t = *(const f32x2*)&L.ap[k][128]; mw[k] = t.x; lw[k] = t.y; }
      const float M = fmaxf(fmaxf(mw[0], mw[1]), fmaxf(mw[2], mw[3]));
      float Lt = 0.f; f32x2 o = {0.f, 0.f};
#pragma unroll
      for (int k = 0; k < 4; k++) {
        const float s = __builtin_amdgcn_exp2f(mw[k] - M);
        Lt = fmaf(s, lw[k], Lt);
        o += s * *(const f32x2*)&L.ap[k][2 * lane];
      }
      gu64* s1 = (gu64*)(ws + WS_S1) + (size_t)b * 144;
      put(s1 + 2 * lane, tag, o.x); put(s1 + 2 * lane + 1, tag, o.y);
      if (lane < 2) put(s1 + 128 + lane, tag, lane == 0 ? M : Lt);
    }
    STAMP(7);
    wg_barrier();
    STAMP(8);
    {
      f32x2 s = *(const f32x2*)&L.qp[0][2 * lane];
#pragma unroll
      for (int k = 1; k < 4; k++) s += *(const f32x2*)&L.qp[k][2 * lane];
      gu64* s2 = (gu64*)(ws + WS_S2);
      put(s2 + (size_t)b * 128 + 2 * lane, tag, s.x); put(s2 + (size_t)b * 128 + 2 * lane + 1, tag, s.y);
      const gu64* sr = s2 + (size_t)((lane >> 1) * 8 + jj) * 128 + col;
      float a0, a1;
      for (u32 spin = 0;; spin++) {
        const u64 g0 = get(sr), g1 = get(sr + 1);
        a0 = val(g0); a1 = val(g1);
        if (__all(tag_ok(g0, tag) & tag_ok(g1, tag)) || spin > SPIN_CAP) break;
        __builtin_amdgcn_s_sleep(1);
      }
      a0 += dpp<XOR2>(a0); a0 += dpp<ROR4>(a0); a0 += dpp<ROR8>(a0); a0 = xrow_sum(a0);
      a1 += dpp<XOR2>(a1); a1 += dpp<ROR4>(a1); a1 += dpp<ROR8>(a1); a1 = xrow_sum(a1);
      const float r0 = rbf(bf_lo(xfin) + rbf(a0)) + rbf(f0), r1 = rbf(bf_hi(xfin) + rbf(a1)) + rbf(f1);
      if (lane < 2) ((GAS u32*)out)[2 * b + lane] = cvt_pk(r0, r1);
      if (b == 0 && lane == 0) __hip_atomic_store((gu32*)(ws + WS_EPOCH), tag, __ATOMIC_RELAXED, __HIP_MEMORY_SCOPE_AGENT);
    }
    if (b == 0) {
      u64 pad = 15000;
      const u64 T00 = get((const gu64*)(ws + 2048)); u32 n = 0;
      while (__builtin_amdgcn_s_memrealtime() - T00 < pad && ++n < 100000) __builtin_amdgcn_s_sleep(4);
    }
  }
}

__global__ void t0k(unsigned char* ws) {
  if (threadIdx.x == 0) *(GAS u64*)(ws + 2048) = __builtin_amdgcn_s_memrealtime();
}
}

extern "C" void kernel_launch(void* const* d_in, const int* in_sizes, int n_in, void* d_out, int out_size, void* d_ws, size_t ws_size,
                              hipStream_t stream) {
  (void)in_sizes; (void)n_in; (void)out_size; (void)ws_size;
  t0k<<<1, 64, 0, stream>>>((unsigned char*)d_ws);
  fused_decode<<<256, 320, 0, stream>>>((const u16*)d_in[0], (const u16*)d_in[1], (const u16*)d_in[2], (const u16*)d_in[3], (const u16*)d_in[4],
                                        (const u16*)d_in[6], (const u16*)d_in[7], (const u16*)d_in[8], (u16*)d_out, (unsigned char*)d_ws);
}
```

```cpp
#include <hip/hip_runtime.h>
#include <stdint.h>

namespace {

typedef unsigned u32;
typedef unsigned long long u64;
typedef unsigned short u16;
typedef u32 u32x4 __attribute__((ext_vector_type(4)));
typedef int i32x4 __attribute__((ext_vector_type(4)));
typedef float f32x2 __attribute__((ext_vector_type(2)));
typedef float f32x4 __attribute__((ext_vector_type(4)));
typedef __attribute__((ext_vector_type(2))) __bf16 bf16x2;
#define GAS __attribute__((address_space(1)))
typedef GAS u64 gu64;
typedef GAS u32 gu32;

constexpr int D = 1024, NH = 32, SK = 32768, DH = 128, FF = 3584;
constexpr int NTILE = SK / 16;
#define QDELAY 8
#define FDELAY1 64
#define SPW_ 56
#define AHEAD 1
#define BCOVER 0
constexpr int SPW = SPW_;
static_assert(NTILE == 2048 && SPW >= 4 && SPW <= 64, "");
constexpr int DYN0 = SPW * 32;
constexpr u32 HEAD_BYTES = (u32)SK * DH * 2;

constexpr size_t WS_EPOCH = 0;
constexpr size_t WS_CTR = 4096;
constexpr size_t WS_FLAG1 = 16384;
constexpr size_t WS_FLAG2 = 32768;
constexpr size_t WS_QREC = 49152;
constexpr size_t WS_S1 = WS_QREC + 256 * 144 * 8;
constexpr size_t WS_S2 = WS_S1 + 256 * 144 * 8;
constexpr size_t WS_GU = WS_S2 + 256 * 128 * 8;
constexpr size_t WS_FR = WS_GU + 224 * 256 * 4;
constexpr size_t WS_STAMP = WS_FR + 224 * 128 * 8;
constexpr u32 SPIN_CAP = 1u << 16;

__device__ __forceinline__ float bf_lo(u32 v) { return __uint_as_float(v << 16); }
__device__ __forceinline__ float bf_hi(u32 v) { return __uint_as_float(v & 0xffff0000u); }
__device__ __forceinline__ u32 cvt_pk(float lo, float hi) {
  bf16x2 r = {(__bf16)lo, (__bf16)hi};
  return __builtin_bit_cast(u32, r);
}
__device__ __forceinline__ u16 bf_bits(float x) { return __builtin_bit_cast(u16, (__bf16)x); }
__device__ __forceinline__ float rbf(float x) { return bf_lo((u32)bf_bits(x)); }
__device__ __forceinline__ float dot2(u32 a, u32 b, float c) {
  return __builtin_amdgcn_fdot2_f32_bf16(__builtin_bit_cast(bf16x2, a), __builtin_bit_cast(bf16x2, b), c, false);
}
template <int CTRL> __device__ __forceinline__ float dpp(float x) {
  return __builtin_bit_cast(float, __builtin_amdgcn_mov_dpp(__builtin_bit_cast(int, x), CTRL, 0xf, 0xf, true));
}
constexpr int XOR1 = 0xB1, XOR2 = 0x4E, HMIRROR = 0x141, ROR8 = 0x128, ROR4 = 0x124, BCAST = 0x150;
__device__ __forceinline__ float swap32_add(float a, float b) {
  auto s = __builtin_amdgcn_permlane32_swap(__float_as_uint(a), __float_as_uint(b), false, false);
  return __uint_as_float(s[0]) + __uint_as_float(s[1]);
}
__device__ __forceinline__ float swap16_add(float a, float b) {
  auto s = __builtin_amdgcn_permlane16_swap(__float_as_uint(a), __float_as_uint(b), false, false);
  return __uint_as_float(s[0]) + __uint_as_float(s[1]);
}
__device__ __forceinline__ float xrow_sum(float x) { x = swap16_add(x, x); return swap32_add(x, x); }
__device__ __forceinline__ float xrow_max(float x) {
  auto s = __builtin_amdgcn_permlane16_swap(__float_as_uint(x), __float_as_uint(x), false, false);
  x = fmaxf(__uint_as_float(s[0]), __uint_as_float(s[1]));
  auto t = __builtin_amdgcn_permlane32_swap(__float_as_uint(x), __float_as_uint(x), false, false);
  return fmaxf(__uint_as_float(t[0]), __uint_as_float(t[1]));
}
__device__ __forceinline__ float row_sum(float x) { x += dpp<XOR1>(x); x += dpp<XOR2>(x); x += dpp<ROR4>(x); x += dpp<ROR8>(x); return x; }
__device__ __forceinline__ float wave_sum(float x) { return xrow_sum(row_sum(x)); }
__device__ __forceinline__ f32x2 fold_rows(const float (&a)[8]) {
  float s0 = swap32_add(a[0], a[4]), s1 = swap32_add(a[1], a[5]), s2 = swap32_add(a[2], a[6]), s3 = swap32_add(a[3], a[7]);
  return f32x2{swap16_add(s0, s2), swap16_add(s1, s3)};
}

__device__ __forceinline__ i32x4 mkr(const void* p, u32 n) {
  u64 a = (u64)p;
  i32x4 r; r.x = (int)(u32)a; r.y = (int)((u32)(a >> 32) & 0xffffu); r.z = (int)n; r.w = 0x00020000; return r;
}
template <int IMM> __device__ __forceinline__ void ld_nt_o(u32x4& d, u32 voff, i32x4 r, u32 soff) {
  asm volatile("buffer_load_dwordx4 %0, %1, %2, %3 offen offset:%4 nt" : "=v"(d) : "v"(voff), "s"(r), "s"(soff), "i"(IMM));
}
__device__ __forceinline__ void issue4_o(u32x4 (&R)[4], u32 voff, i32x4 r, u32 soff) {
  ld_nt_o<0>(R[0], voff, r, soff); ld_nt_o<1024>(R[1], voff, r, soff); ld_nt_o<2048>(R[2], voff, r, soff); ld_nt_o<3072>(R[3], voff, r, soff);
}
__device__ __forceinline__ void rows4_o(u32x4 (&R)[4], u32 voff, i32x4 r, u32 soff, u32 step) {
  ld_nt_o<0>(R[0], voff, r, soff); ld_nt_o<0>(R[1], voff, r, soff + step); ld_nt_o<0>(R[2], voff, r, soff + 2 * step); ld_nt_o<0>(R[3], voff, r, soff + 3 * step);
}
__device__ __forceinline__ void issue4_or(u32x4 (&R)[4], u32 voff, i32x4 r, u32 soff, u32 alt, u32x4 (&W)[4],
                                          u32 wvo, i32x4 rw, u32 w0, u32 w1, u32 w2, u32 w3) {
  asm volatile(
      "s_cmp_lg_u32 %11, 0\n\t"
      "s_cbranch_scc1 1f\n\t"
      "buffer_load_dwordx4 %0, %8, %9, %10 offen nt\n\t"
      "buffer_load_dwordx4 %1, %8, %9, %10 offen offset:1024 nt\n\t"
      "buffer_load_dwordx4 %2, %8, %9, %10 offen offset:2048 nt\n\t"
      "buffer_load_dwordx4 %3, %8, %9, %10 offen offset:3072 nt\n\t"
      "s_branch 2f\n"
      "1:\n\t"
      "buffer_load_dwordx4 %4, %12, %13, %14 offen nt\n\t"
      "buffer_load_dwordx4 %5, %12, %13, %15 offen nt\n\t"
      "buffer_load_dwordx4 %6, %12, %13, %16 offen nt\n\t"
      "buffer_load_dwordx4 %7, %12, %13, %17 offen nt\n"
      "2:"
      : "+v"(R[0]), "+v"(R[1]), "+v"(R[2]), "+v"(R[3]), "+v"(W[0]), "+v"(W[1]), "+v"(W[2]), "+v"(W[3])
      : "v"(voff), "s"(r), "s"(soff), "s"(alt), "v"(wvo), "s"(rw), "s"(w0), "s"(w1), "s"(w2), "s"(w3)
      : "scc");
}
template <int N> __device__ __forceinline__ void wait4(u32x4 (&R)[4]) {
  asm volatile("s_waitcnt vmcnt(%4)" : "+v"(R[0]), "+v"(R[1]), "+v"(R[2]), "+v"(R[3]) : "i"(N));
}
__device__ __forceinline__ void pin4(u32x4 (&R)[4]) { asm volatile("" : "+v"(R[0]), "+v"(R[1]), "+v"(R[2]), "+v"(R[3])); }
template <int N> __device__ __forceinline__ void wait1(u32& t) { asm volatile("s_waitcnt vmcnt(%1)" : "+v"(t) : "i"(N)); }
__device__ __forceinline__ void ticket(u32& t, u32 aoff, i32x4 r) {
  asm volatile("v_mov_b32 %0, 1\n\tbuffer_atomic_add %0, %1, %2, 0 offen sc0" : "+v"(t) : "v"(aoff), "s"(r));
}
__device__ __forceinline__ void wg_barrier() { asm volatile("s_waitcnt lgkmcnt(0)\n\ts_barrier" ::: "memory"); }
__device__ __forceinline__ void drain() { asm volatile("s_waitcnt vmcnt(0)" ::: "memory"); }

__device__ __forceinline__ void put(gu64* g, u32 tag, float v) {
  __hip_atomic_store(g, ((u64)tag << 32) | (u64)__float_as_uint(v), __ATOMIC_RELAXED, __HIP_MEMORY_SCOPE_AGENT);
}
__device__ __forceinline__ u64 get(const gu64* g) { return __hip_atomic_load(g, __ATOMIC_RELAXED, __HIP_MEMORY_SCOPE_AGENT); }
__device__ __forceinline__ void put32(gu32* g, u32 v) { __hip_atomic_store(g, v, __ATOMIC_RELAXED, __HIP_MEMORY_SCOPE_AGENT); }
__device__ __forceinline__ u32 get32(const gu32* g) { return __hip_atomic_load(g, __ATOMIC_RELAXED, __HIP_MEMORY_SCOPE_AGENT); }
__device__ __forceinline__ bool tag_ok(u64 x, u32 tag) { return (u32)(x >> 32) == tag; }
__device__ __forceinline__ float val(u64 x) { return __uint_as_float((u32)x); }
typedef __amdgpu_buffer_rsrc_t rsrc_t;
__device__ __forceinline__ rsrc_t mkrs(void* p, u32 n) { return __builtin_amdgcn_make_buffer_rsrc(p, 0, n, 0x00020000); }
constexpr int SC1 = 16;

template <int LN> __device__ __forceinline__ void gemv_step(float (&acc)[8], float xv, u32x4 w) {
  const float xb = dpp<BCAST + LN>(xv);
#pragma unroll
  for (int k = 0; k < 4; k++) { acc[2 * k] = fmaf(xb, bf_lo(w[k]), acc[2 * k]); acc[2 * k + 1] = fmaf(xb, bf_hi(w[k]), acc[2 * k + 1]); }
}
template <int LN> __device__ __forceinline__ void gemv4(float (&acc)[8], float xv, u32x4 (&w)[4]) {
  gemv_step<LN>(acc, xv, w[0]); gemv_step<LN + 1>(acc, xv, w[1]); gemv_step<LN + 2>(acc, xv, w[2]); gemv_step<LN + 3>(acc, xv, w[3]);
}
__device__ __forceinline__ void pin8(float (&a)[8]) {
  asm volatile("" : "+v"(a[0]), "+v"(a[1]), "+v"(a[2]), "+v"(a[3]), "+v"(a[4]), "+v"(a[5]), "+v"(a[6]), "+v"(a[7]));
}

struct Lds {
  float part[4][256];
  float qp[4][128];
  float ap[4][132];
  u16 q[128];
  u32 ctl[4];
};

#define STAMP(i) do {} while (0)

__global__ void __launch_bounds__(320, 2)
fused_decode(const u16* __restrict__ x, const u16* __restrict__ kc, const u16* __restrict__ vc, const u16* __restrict__ wq,
             const u16* __restrict__ wo, const u16* __restrict__ wg, const u16* __restrict__ wu, const u16* __restrict__ wd,
             u16* __restrict__ out, unsigned char* __restrict__ ws) {
  __shared__ __attribute__((aligned(16))) Lds L;
  const int b = blockIdx.x, head = b >> 3, j = b & 7;
  const int lane = threadIdx.x & 63, wave = __builtin_amdgcn_readfirstlane(threadIdx.x >> 6);
  const int r = lane >> 4, c = lane & 15;
  const bool has_ffn = j != (head & 7);
  const int unit = has_ffn ? 7 * head + j - (j > (head & 7) ? 1 : 0) : 0;
  const int ug = unit >> 3, us = unit & 7;
  STAMP(0);

  if (wave < 4) {
    const int w = wave;
    u32x4 X0, X1; u32 xs;
    u32x4 P0[4], P1[4], P2[4], P3[4], K[4], V[4];
    u32 tk = 0;
    const i32x4 rx = mkr(x, D * 2);
    {
      const u32 xo = lane * 16;
      const u32 so = (u32)(((c < 8 ? j : us) * 128 + 32 * w + 4 * (c & 7) + r) * 2);
      asm volatile("buffer_load_dwordx4 %0, %1, %2, 0 offen" : "=v"(X0) : "v"(xo), "s"(rx));
      asm volatile("buffer_load_dwordx4 %0, %1, %2, 0 offen offset:1024" : "=v"(X1) : "v"(xo), "s"(rx));
      asm volatile("buffer_load_ushort %0, %1, %2, 0 offen" : "=v"(xs) : "v"(so), "s"(rx));
    }
    {
      const i32x4 rq = mkr(wq, (u32)D * 4096 * 2);
      const u32 vo = r * 8192 + c * 16, s0 = (u32)(j * 128 + 32 * w) * 8192 + head * 256;
      rows4_o(P0, vo, rq, s0, 32768); rows4_o(P1, vo, rq, s0 + 4 * 32768, 32768);
    }
    const u32 nffn = has_ffn ? (u32)D * FF * 2 : 0u;
    const i32x4 rg = mkr(wg, nffn), ru = mkr(wu, nffn);
    constexpr u32 GSTEP = 4 * FF * 2;
    const u32 vg = r * (FF * 2) + c * 16, g0 = (u32)(us * 128 + 32 * w) * (FF * 2) + ug * 256;
    const char* kb = (const char*)kc + (size_t)head * HEAD_BYTES;
    const char* vb = (const char*)vc + (size_t)head * HEAD_BYTES;
    const i32x4 rk = mkr(kb, HEAD_BYTES), rv = mkr(vb, HEAD_BYTES);
    const u32 voff = lane * 16;
    const u32 aoff = lane == 0 ? 0u : 0x7ffffff0u;
    const unsigned char* ctr = ws + WS_CTR + head * 256;
    const int st0 = j * SPW * 4 + w;
    asm volatile("s_waitcnt vmcnt(8)" : "+v"(X0), "+v"(X1), "+v"(xs));
    float xn;
    {
      float v[16];
#pragma unroll
      for (int k = 0; k < 4; k++) { v[2 * k] = bf_lo(X0[k]); v[2 * k + 1] = bf_hi(X0[k]); v[8 + 2 * k] = bf_lo(X1[k]); v[9 + 2 * k] = bf_hi(X1[k]); }
      float s = 0.f;
#pragma unroll
      for (int k = 0; k < 16; k++) s += v[k];
      const float mean = wave_sum(s) * (1.0f / D);
      float q = 0.f;
#pragma unroll
      for (int k = 0; k < 16; k++) { const float d = v[k] - mean; q = fmaf(d, d, q); }
      const float var = wave_sum(q) * (1.0f / D);
      const float den = rbf(sqrtf(rbf(rbf(var) + 1e-5f)));
      xn = rbf(rbf(bf_lo(xs) - rbf(mean)) / den);
    }
    asm volatile("" : "+v"(xn));
    STAMP(1);
    {
      float a[8] = {0.f, 0.f, 0.f, 0.f, 0.f, 0.f, 0.f, 0.f};
      wait4<4>(P0); rows4_o(P2, vg, rg, g0, GSTEP); pin4(P0);
      gemv4<0>(a, xn, P0); pin8(a);
      wait4<4>(P1); rows4_o(P3, vg, rg, g0 + 4 * GSTEP, GSTEP); pin4(P1);
      gemv4<4>(a, xn, P1);
      *(f32x2*)&L.qp[w][8 * c + 2 * r] = fold_rows(a);
    }
    wg_barrier();
    STAMP(2);
    {
      float a[8] = {0.f, 0.f, 0.f, 0.f, 0.f, 0.f, 0.f, 0.f};
      wait4<4>(P2); rows4_o(P0, vg, ru, g0, GSTEP); pin4(P2);
      gemv4<8>(a, xn, P2); pin8(a);
      wait4<4>(P3); rows4_o(P1, vg, ru, g0 + 4 * GSTEP, GSTEP); pin4(P3);
      gemv4<12>(a, xn, P3);
      *(f32x2*)&L.part[w][8 * c + 2 * r] = fold_rows(a);
    }
    {
      float a[8] = {0.f, 0.f, 0.f, 0.f, 0.f, 0.f, 0.f, 0.f};
      wait4<4>(P0); issue4_o(K, voff, rk, (u32)st0 * 4096); ticket(tk, aoff, mkr(ctr, 0)); pin4(P0);
      gemv4<8>(a, xn, P0); pin8(a);
      wait4<5>(P1); issue4_o(V, voff, rv, (u32)st0 * 4096); pin4(P1);
      gemv4<12>(a, xn, P1);
      *(f32x2*)&L.part[w][128 + 8 * c + 2 * r] = fold_rows(a);
    }
    STAMP(3);
    wg_barrier();
    STAMP(4);
    const u32x4 q = *(const u32x4*)&L.q[8 * c];
    const u32 tag = __builtin_amdgcn_readfirstlane(L.ctl[0]);
    const u32 base = __builtin_amdgcn_readfirstlane(L.ctl[1]);

    u32x4 W0[4], W1[4];
#pragma unroll
    for (int i = 0; i < 4; i++) { asm volatile("" : "=v"(W0[i])); asm volatile("" : "=v"(W1[i])); }
    const i32x4 rwo = mkr(wo, (u32)4096 * D * 2);
    const u32 wvo = (16 * (r >> 1) + (r & 1)) * 2048 + c * 16, wso = (u32)(head * 128 + 32 * w) * 2048 + j * 256;
    float m = -INFINITY, l = 0.f, acc[8] = {0.f, 0.f, 0.f, 0.f, 0.f, 0.f, 0.f, 0.f};
    int i = 1;
    u32 inv;
    do {
      wait4<5>(K);
      float d[4];
#pragma unroll
      for (int t = 0; t < 4; t++) d[t] = dot2(K[t][3], q[3], dot2(K[t][2], q[2], dot2(K[t][1], q[1], dot2(K[t][0], q[0], 0.f))));
      asm volatile("" : "+v"(d[0]), "+v"(d[1]), "+v"(d[2]), "+v"(d[3]));
      wait1<4>(tk);
      const u32 tnext = __builtin_amdgcn_readfirstlane(tk);
      const u32 dyn = min((u32)DYN0 + (tnext - base), (u32)NTILE);
      const u32 t = i < SPW ? (u32)(st0 + 4 * i) : dyn;
      inv = t >> 11;
      issue4_or(K, voff, rk, t * 4096, inv, W0, wvo, rwo, wso, wso + 4096, wso + 8192, wso + 12288);
      ticket(tk, aoff, mkr(ctr, (((u32)(SPW - 2 - i) >> 31) & ~inv) << 2));
      asm volatile("" : "+v"(d[0]), "+v"(d[1]), "+v"(d[2]), "+v"(d[3]));
      const bool b8 = (c & 8) != 0, b4 = (c & 4) != 0;
      const float e0 = (b8 ? d[2] : d[0]) + dpp<ROR8>(b8 ? d[0] : d[2]);
      const float e1 = (b8 ? d[3] : d[1]) + dpp<ROR8>(b8 ? d[1] : d[3]);
      float f = (b4 ? e1 : e0) + dpp<HMIRROR>(b4 ? e0 : e1);
      f += dpp<XOR1>(f); f += dpp<XOR2>(f);
      const float y = rbf(rbf(f) * 0.08838834764831845f) * 1.4426950408889634f;
      float ym = fmaxf(y, dpp<ROR4>(y)); ym = fmaxf(ym, dpp<ROR8>(ym));
      if (__any(ym > m)) {
        const float mn = fmaxf(m, ym), al = __builtin_amdgcn_exp2f(m - mn);
        l *= al;
#pragma unroll
        for (int k = 0; k < 8; k++) acc[k] *= al;
        m = mn;
      }
      const float p = __builtin_amdgcn_exp2f(y - m);
      l += p;
      wait4<5>(V);
      gemv_step<0>(acc, p, V[0]); gemv_step<4>(acc, p, V[1]); gemv_step<8>(acc, p, V[2]); gemv_step<12>(acc, p, V[3]);
      pin8(acc);
      issue4_or(V, voff, rv, t * 4096, inv, W1, wvo, rwo, wso + 16384, wso + 20480, wso + 24576, wso + 28672);
      i++;
    } while (!inv);
    STAMP(5);
    {
      const float M = xrow_max(m), sc = __builtin_amdgcn_exp2f(m - M);
#pragma unroll
      for (int k = 0; k < 8; k++) acc[k] *= sc;
      const float ls = wave_sum(l * sc) * 0.25f;
      *(f32x2*)&L.ap[w][8 * c + 2 * r] = fold_rows(acc);
      if (lane == 0) *(f32x2*)&L.ap[w][128] = f32x2{M, ls};
    }
    wg_barrier();
    STAMP(6);
    {
      wait4<0>(W0); wait4<0>(W1); wait1<0>(tk);
      float wf[8][8];
#pragma unroll
      for (int i2 = 0; i2 < 4; i2++)
#pragma unroll
        for (int k = 0; k < 4; k++) {
          wf[i2][2 * k] = bf_lo(W0[i2][k]); wf[i2][2 * k + 1] = bf_hi(W0[i2][k]);
          wf[4 + i2][2 * k] = bf_lo(W1[i2][k]); wf[4 + i2][2 * k + 1] = bf_hi(W1[i2][k]);
        }
#pragma unroll
      for (int i2 = 0; i2 < 8; i2++) pin8(wf[i2]);
      const gu64* s1 = (const gu64*)(ws + WS_S1) + (size_t)(head * 8 + r) * 144;
      u64 g[8];
      for (u32 spin = 0;; spin++) {
        g[0] = get(s1 + 32 * w + 2 * c); g[1] = get(s1 + 32 * w + 2 * c + 1); g[2] = get(s1 + 128); g[3] = get(s1 + 129);
        g[4] = get(s1 + 4 * 144 + 32 * w + 2 * c); g[5] = get(s1 + 4 * 144 + 32 * w + 2 * c + 1); g[6] = get(s1 + 4 * 144 + 128); g[7] = get(s1 + 4 * 144 + 129);
        bool ok = true;
#pragma unroll
        for (int k = 0; k < 8; k++) ok &= tag_ok(g[k], tag);
        if (__all(ok) || spin > SPIN_CAP) break;
      }
      STAMP(7);
      const float M = xrow_max(fmaxf(val(g[2]), val(g[6])));
      const float w0 = __builtin_amdgcn_exp2f(val(g[2]) - M), w1 = __builtin_amdgcn_exp2f(val(g[6]) - M);
      const float Lt = xrow_sum(fmaf(w0, val(g[3]), w1 * val(g[7])));
      float o = swap16_add(fmaf(w0, val(g[0]), w1 * val(g[4])), fmaf(w0, val(g[1]), w1 * val(g[5])));
      o = swap32_add(o, o);
      o = rbf(o / Lt);
      const float orot = dpp<ROR8>(o);
      o = (r & 2) ? orot : o;
      float a[8] = {0.f, 0.f, 0.f, 0.f, 0.f, 0.f, 0.f, 0.f};
#define WSTEP(n) { const float xb = dpp<BCAST + n>(o); _Pragma("unroll") for (int k = 0; k < 8; k++) a[k] = fmaf(xb, wf[n][k], a[k]); }
      WSTEP(0) WSTEP(1) WSTEP(2) WSTEP(3) WSTEP(4) WSTEP(5) WSTEP(6) WSTEP(7)
#undef WSTEP
      *(f32x2*)&L.qp[w][8 * c + 2 * r] = fold_rows(a);
    }
    wg_barrier();
    STAMP(8);
  } else {
    const u32 ep = get32((gu32*)(ws + WS_EPOCH));
    const u32 cb = get32((gu32*)(ws + WS_CTR + head * 256));
    const u32 xfin = ((const GAS u32*)x)[2 * b + (lane & 1)];
    const u32 tag = __builtin_amdgcn_readfirstlane(ep) + 1u;
    gu64* qrec = (gu64*)(ws + WS_QREC);
    if (j == 0 && lane == 0) put(qrec + (size_t)b * 144 + 128, tag, __uint_as_float(cb));
    STAMP(1);
    wg_barrier();
    {
      float s0 = L.qp[0][lane], s1 = L.qp[0][64 + lane];
#pragma unroll
      for (int k = 1; k < 4; k++) { s0 += L.qp[k][lane]; s1 += L.qp[k][64 + lane]; }
      put(qrec + (size_t)b * 144 + lane, tag, s0); put(qrec + (size_t)b * 144 + 64 + lane, tag, s1);
      STAMP(2);
      const gu64* qr = qrec + (size_t)head * 8 * 144;
      __builtin_amdgcn_s_sleep(QDELAY);
      u64 g[17];
      for (u32 spin = 0;; spin++) {
#pragma unroll
        for (int k = 0; k < 8; k++) { g[2 * k] = get(qr + k * 144 + lane); g[2 * k + 1] = get(qr + k * 144 + 64 + lane); }
        g[16] = get(qr + 128);
        bool ok = true;
#pragma unroll
        for (int k = 0; k < 17; k++) ok &= tag_ok(g[k], tag);
        if (__all(ok) || spin > SPIN_CAP) break;
      }
      float q0 = 0.f, q1 = 0.f;
#pragma unroll
      for (int k = 0; k < 8; k++) { q0 += val(g[2 * k]); q1 += val(g[2 * k + 1]); }
      L.q[lane] = bf_bits(q0); L.q[64 + lane] = bf_bits(q1);
      if (lane == 0) { L.ctl[0] = tag; L.ctl[1] = (u32)g[16]; }
    }
    STAMP(3);
    wg_barrier();
    STAMP(4);
    if (has_ffn) {
      const rsrc_t rgu = mkrs(ws + WS_GU, 224 * 256 * 4);
      f32x2 sg = *(const f32x2*)&L.part[0][2 * lane], su = *(const f32x2*)&L.part[0][128 + 2 * lane];
#pragma unroll
      for (int k = 1; k < 4; k++) { sg += *(const f32x2*)&L.part[k][2 * lane]; su += *(const f32x2*)&L.part[k][128 + 2 * lane]; }
      __builtin_amdgcn_raw_buffer_store_b128(__builtin_bit_cast(u32x4, f32x4{sg.x, sg.y, su.x, su.y}), rgu, unit * 1024 + lane * 16, 0, SC1);
      drain();
      if (lane == 0) put32((gu32*)(ws + WS_FLAG1 + unit * 64), tag);
      const GAS u32x4* wp = (const GAS u32x4*)(wd + (size_t)(128 * ug + 32 * r) * D + 128 * us + 8 * c);
      u32x4 WD[32];
#pragma unroll
      for (int i = 0; i < 32; i++) WD[i] = __builtin_nontemporal_load(wp + i * (D / 8));
      const gu32* f1 = (const gu32*)(ws + WS_FLAG1 + (ug * 8 + (lane & 7)) * 64);
      __builtin_amdgcn_s_sleep(FDELAY1);
      for (u32 spin = 0; !__all(get32(f1) == tag) && spin < SPIN_CAP; spin++) __builtin_amdgcn_s_sleep(32);
      __builtin_amdgcn_fence(__ATOMIC_ACQUIRE, "wavefront");
      u32x4 pl[8];
#pragma unroll
      for (int k = 0; k < 8; k++) pl[k] = __builtin_amdgcn_raw_buffer_load_b128(rgu, (ug * 8 + k) * 1024 + lane * 16, 0, SC1);
      asm volatile("" : "+v"(pl[0]), "+v"(pl[1]), "+v"(pl[2]), "+v"(pl[3]), "+v"(pl[4]), "+v"(pl[5]), "+v"(pl[6]), "+v"(pl[7]));
      f32x4 s = {0.f, 0.f, 0.f, 0.f};
#pragma unroll
      for (int k = 0; k < 8; k++) s += __builtin_bit_cast(f32x4, pl[k]);
      auto act = [](float g, float u) {
        g = rbf(g); u = rbf(u);
        const float inner = 0.7978845608028654f * (g + 0.044715f * g * g * g);
        return rbf(rbf(0.5f * g * (1.f + tanhf(inner))) * u);
      };
      const float h0 = act(s.x, s.z), h1 = act(s.y, s.w);
      float a[8] = {0.f, 0.f, 0.f, 0.f, 0.f, 0.f, 0.f, 0.f};
#define WD2(k) gemv_step<k>(a, h0, WD[2 * k]); gemv_step<k>(a, h1, WD[2 * k + 1]);
      WD2(0) WD2(1) WD2(2) WD2(3) WD2(4) WD2(5) WD2(6) WD2(7) WD2(8) WD2(9) WD2(10) WD2(11) WD2(12) WD2(13) WD2(14) WD2(15)
#undef WD2
      const f32x2 fs = fold_rows(a);
      gu64* fr = (gu64*)(ws + WS_FR) + (size_t)unit * 128 + 8 * c + 2 * r;
      put(fr, tag, fs.x); put(fr + 1, tag, fs.y);
    } else {
      __builtin_amdgcn_s_sleep(127); __builtin_amdgcn_s_sleep(127);
    }
    STAMP(5);
    const int jj = b >> 5, col = (b & 31) * 4 + 2 * (lane & 1);
    float f0, f1;
    {
      const int rg = lane >> 1, rgc = rg < 28 ? rg : 27;
      const gu64* fr = (const gu64*)(ws + WS_FR) + (size_t)(rgc * 8 + jj) * 128 + col;
      __builtin_amdgcn_s_sleep(127); __builtin_amdgcn_s_sleep(127);
      for (u32 spin = 0;; spin++) {
        const u64 a0 = get(fr), a1 = get(fr + 1);
        f0 = rg < 28 ? val(a0) : 0.f; f1 = rg < 28 ? val(a1) : 0.f;
        if (__all(tag_ok(a0, tag) & tag_ok(a1, tag)) || spin > SPIN_CAP) break;
        __builtin_amdgcn_s_sleep(127);
      }
      f0 += dpp<XOR2>(f0); f0 += dpp<ROR4>(f0); f0 += dpp<ROR8>(f0); f0 = xrow_sum(f0);
      f1 += dpp<XOR2>(f1); f1 += dpp<ROR4>(f1); f1 += dpp<ROR8>(f1); f1 = xrow_sum(f1);
      f0 = rbf(f0); f1 = rbf(f1);
      asm volatile("" : "+v"(f0), "+v"(f1));
    }
    STAMP(9);
    wg_barrier();
    STAMP(6);
    {
      float mw[4], lw[4];
#pragma unroll
      for (int k = 0; k < 4; k++) { const f32x2 t = *(const f32x2*)&L.ap[k][128]; mw[k] = t.x; lw[k] = t.y; }
      const float M = fmaxf(fmaxf(mw[0], mw[1]), fmaxf(mw[2], mw[3]));
      float Lt = 0.f; f32x2 o = {0.f, 0.f};
#pragma unroll
      for (int k = 0; k < 4; k++) {
        const float s = __builtin_amdgcn_exp2f(mw[k] - M);
        Lt = fmaf(s, lw[k], Lt);
        o += s * *(const f32x2*)&L.ap[k][2 * lane];
      }
      gu64* s1 = (gu64*)(ws + WS_S1) + (size_t)b * 144;
      put(s1 + 2 * lane, tag, o.x); put(s1 + 2 * lane + 1, tag, o.y);
      if (lane < 2) put(s1 + 128 + lane, tag, lane == 0 ? M : Lt);
    }
    STAMP(7);
    wg_barrier();
    STAMP(8);
    {
      f32x2 s = *(const f32x2*)&L.qp[0][2 * lane];
#pragma unroll
      for (int k = 1; k < 4; k++) s += *(const f32x2*)&L.qp[k][2 * lane];
      gu64* s2 = (gu64*)(ws + WS_S2);
      put(s2 + (size_t)b * 128 + 2 * lane, tag, s.x); put(s2 + (size_t)b * 128 + 2 * lane + 1, tag, s.y);
      const gu64* sr = s2 + (size_t)((lane >> 1) * 8 + jj) * 128 + col;
      float a0, a1;
      for (u32 spin = 0;; spin++) {
        const u64 g0 = get(sr), g1 = get(sr + 1);
        a0 = val(g0); a1 = val(g1);
        if (__all(tag_ok(g0, tag) & tag_ok(g1, tag)) || spin > SPIN_CAP) break;
      }
      a0 += dpp<XOR2>(a0); a0 += dpp<ROR4>(a0); a0 += dpp<ROR8>(a0); a0 = xrow_sum(a0);
      a1 += dpp<XOR2>(a1); a1 += dpp<ROR4>(a1); a1 += dpp<ROR8>(a1); a1 = xrow_sum(a1);
      const float r0 = rbf(bf_lo(xfin) + rbf(a0)) + f0, r1 = rbf(bf_hi(xfin) + rbf(a1)) + f1;
      if (lane < 2) ((GAS u32*)out)[2 * b + lane] = cvt_pk(r0, r1);
      if (b == 0 && lane == 0) put32((gu32*)(ws + WS_EPOCH), tag);
    }
    STAMP(10);
  }
}

}

extern "C" void kernel_launch(void* const* d_in, const int* in_sizes, int n_in, void* d_out, int out_size, void* d_ws, size_t ws_size,
                              hipStream_t stream) {
  (void)in_sizes; (void)n_in; (void)out_size; (void)ws_size;
  fused_decode<<<256, 320, 0, stream>>>((const u16*)d_in[0], (const u16*)d_in[1], (const u16*)d_in[2], (const u16*)d_in[3], (const u16*)d_in[4],
                                        (const u16*)d_in[6], (const u16*)d_in[7], (const u16*)d_in[8], (u16*)d_out, (unsigned char*)d_ws);
}
```

```cpp
#include <hip/hip_runtime.h>
#include <stdint.h>

namespace {

typedef unsigned u32;
typedef unsigned long long u64;
typedef unsigned short u16;
typedef u32 u32x4 __attribute__((ext_vector_type(4)));
typedef int i32x4 __attribute__((ext_vector_type(4)));
typedef float f32x2 __attribute__((ext_vector_type(2)));
typedef float f32x4 __attribute__((ext_vector_type(4)));
typedef __attribute__((ext_vector_type(2))) __bf16 bf16x2;
#define GAS __attribute__((address_space(1)))
typedef GAS u64 gu64;
typedef GAS u32 gu32;

constexpr int D = 1024, NH = 32, SK = 32768, DH = 128, FF = 3584;
constexpr int NTILE = SK / 16;
#define QDELAY 8
#define FDELAY1 64
#define SPW_ 58
#define AHEAD 1
#define BCOVER 0
constexpr int SPW = SPW_;
static_assert(NTILE == 2048 && SPW >= 4 && SPW <= 64, "");
constexpr u32 HEAD_BYTES = (u32)SK * DH * 2;

constexpr size_t WS_EPOCH = 0;
constexpr size_t CTR_STRIDE = 4352;
constexpr size_t WS_CTR = 1u << 20;
constexpr size_t WS_FLAG1 = 16384;
constexpr size_t WS_QREC = 49152;
constexpr size_t WS_S1 = WS_QREC + 256 * 144 * 8;
constexpr size_t WS_S2 = WS_S1 + 256 * 144 * 8;
constexpr size_t WS_GU = WS_S2 + 256 * 128 * 8;
constexpr size_t WS_STAMP = 2u << 20;
static_assert(WS_GU + 224 * 256 * 4 <= WS_CTR, "");
constexpr u32 SPIN_CAP = 1u << 16;
constexpr u32 EMPTY = 0xffffffffu;
#define LOOKAHEAD 8

__device__ __forceinline__ float bf_lo(u32 v) { return __uint_as_float(v << 16); }
__device__ __forceinline__ float bf_hi(u32 v) { return __uint_as_float(v & 0xffff0000u); }
__device__ __forceinline__ u32 cvt_pk(float lo, float hi) {
  bf16x2 r = {(__bf16)lo, (__bf16)hi};
  return __builtin_bit_cast(u32, r);
}
__device__ __forceinline__ u16 bf_bits(float x) { return __builtin_bit_cast(u16, (__bf16)x); }
__device__ __forceinline__ float rbf(float x) { return bf_lo((u32)bf_bits(x)); }
__device__ __forceinline__ float dot2(u32 a, u32 b, float c) {
  return __builtin_amdgcn_fdot2_f32_bf16(__builtin_bit_cast(bf16x2, a), __builtin_bit_cast(bf16x2, b), c, false);
}
template <int CTRL> __device__ __forceinline__ float dpp(float x) {
  return __builtin_bit_cast(float, __builtin_amdgcn_mov_dpp(__builtin_bit_cast(int, x), CTRL, 0xf, 0xf, true));
}
constexpr int XOR1 = 0xB1, XOR2 = 0x4E, HMIRROR = 0x141, ROR8 = 0x128, ROR4 = 0x124, BCAST = 0x150;
__device__ __forceinline__ float swap32_add(float a, float b) {
  auto s = __builtin_amdgcn_permlane32_swap(__float_as_uint(a), __float_as_uint(b), false, false);
  return __uint_as_float(s[0]) + __uint_as_float(s[1]);
}
__device__ __forceinline__ float swap16_add(float a, float b) {
  auto s = __builtin_amdgcn_permlane16_swap(__float_as_uint(a), __float_as_uint(b), false, false);
  return __uint_as_float(s[0]) + __uint_as_float(s[1]);
}
__device__ __forceinline__ float xrow_sum(float x) { x = swap16_add(x, x); return swap32_add(x, x); }
__device__ __forceinline__ float xrow_max(float x) {
  auto s = __builtin_amdgcn_permlane16_swap(__float_as_uint(x), __float_as_uint(x), false, false);
  x = fmaxf(__uint_as_float(s[0]), __uint_as_float(s[1]));
  auto t = __builtin_amdgcn_permlane32_swap(__float_as_uint(x), __float_as_uint(x), false, false);
  return fmaxf(__uint_as_float(t[0]), __uint_as_float(t[1]));
}
__device__ __forceinline__ float row_sum(float x) { x += dpp<XOR1>(x); x += dpp<XOR2>(x); x += dpp<ROR4>(x); x += dpp<ROR8>(x); return x; }
__device__ __forceinline__ float wave_sum(float x) { return xrow_sum(row_sum(x)); }
__device__ __forceinline__ f32x2 fold_rows(const float (&a)[8]) {
  float s0 = swap32_add(a[0], a[4]), s1 = swap32_add(a[1], a[5]), s2 = swap32_add(a[2], a[6]), s3 = swap32_add(a[3], a[7]);
  return f32x2{swap16_add(s0, s2), swap16_add(s1, s3)};
}

__device__ __forceinline__ i32x4 mkr(const void* p, u32 n) {
  u64 a = (u64)p;
  i32x4 r; r.x = (int)(u32)a; r.y = (int)((u32)(a >> 32) & 0xffffu); r.z = (int)n; r.w = 0x00020000; return r;
}
template <int IMM> __device__ __forceinline__ void ld_nt_o(u32x4& d, u32 voff, i32x4 r, u32 soff) {
  asm volatile("buffer_load_dwordx4 %0, %1, %2, %3 offen offset:%4 nt" : "=v"(d) : "v"(voff), "s"(r), "s"(soff), "i"(IMM));
}
__device__ __forceinline__ void issue4_o(u32x4 (&R)[4], u32 voff, i32x4 r, u32 soff) {
  ld_nt_o<0>(R[0], voff, r, soff); ld_nt_o<1024>(R[1], voff, r, soff); ld_nt_o<2048>(R[2], voff, r, soff); ld_nt_o<3072>(R[3], voff, r, soff);
}
__device__ __forceinline__ void rows4_o(u32x4 (&R)[4], u32 voff, i32x4 r, u32 soff, u32 step) {
  ld_nt_o<0>(R[0], voff, r, soff); ld_nt_o<0>(R[1], voff, r, soff + step); ld_nt_o<0>(R[2], voff, r, soff + 2 * step); ld_nt_o<0>(R[3], voff, r, soff + 3 * step);
}
__device__ __forceinline__ void issue4_or(u32x4 (&R)[4], u32 voff, i32x4 r, u32 soff, u32 alt, u32x4 (&W)[4],
                                          u32 wvo, i32x4 rw, u32 w0, u32 w1, u32 w2, u32 w3) {
  asm volatile(
      "s_cmp_lg_u32 %11, 0\n\t"
      "s_cbranch_scc1 1f\n\t"
      "buffer_load_dwordx4 %0, %8, %9, %10 offen nt\n\t"
      "buffer_load_dwordx4 %1, %8, %9, %10 offen offset:1024 nt\n\t"
      "buffer_load_dwordx4 %2, %8, %9, %10 offen offset:2048 nt\n\t"
      "buffer_load_dwordx4 %3, %8, %9, %10 offen offset:3072 nt\n\t"
      "s_branch 2f\n"
      "1:\n\t"
      "buffer_load_dwordx4 %4, %12, %13, %14 offen nt\n\t"
      "buffer_load_dwordx4 %5, %12, %13, %15 offen nt\n\t"
      "buffer_load_dwordx4 %6, %12, %13, %16 offen nt\n\t"
      "buffer_load_dwordx4 %7, %12, %13, %17 offen nt\n"
      "2:"
      : "+v"(R[0]), "+v"(R[1]), "+v"(R[2]), "+v"(R[3]), "+v"(W[0]), "+v"(W[1]), "+v"(W[2]), "+v"(W[3])
      : "v"(voff), "s"(r), "s"(soff), "s"(alt), "v"(wvo), "s"(rw), "s"(w0), "s"(w1), "s"(w2), "s"(w3)
      : "scc");
}
template <int N> __device__ __forceinline__ void wait4(u32x4 (&R)[4]) {
  asm volatile("s_waitcnt vmcnt(%4)" : "+v"(R[0]), "+v"(R[1]), "+v"(R[2]), "+v"(R[3]) : "i"(N));
}
__device__ __forceinline__ void pin4(u32x4 (&R)[4]) { asm volatile("" : "+v"(R[0]), "+v"(R[1]), "+v"(R[2]), "+v"(R[3])); }
template <int N> __device__ __forceinline__ void wait1(u32& t) { asm volatile("s_waitcnt vmcnt(%1)" : "+v"(t) : "i"(N)); }
__device__ __forceinline__ void ticket(u32& t, u32 aoff, i32x4 r) {
  asm volatile("v_mov_b32 %0, 1\n\tbuffer_atomic_add %0, %1, %2, 0 offen sc0" : "+v"(t) : "v"(aoff), "s"(r));
}
#define LAS __attribute__((address_space(3)))
__device__ __forceinline__ u32 lds_get(const u32* p) { return __hip_atomic_load((const LAS u32*)p, __ATOMIC_RELAXED, __HIP_MEMORY_SCOPE_WORKGROUP); }
__device__ __forceinline__ u32 lds_inc(u32* p) { return __hip_atomic_fetch_add((LAS u32*)p, 1u, __ATOMIC_RELAXED, __HIP_MEMORY_SCOPE_WORKGROUP); }
__device__ __forceinline__ void lds_put(u32* p, u32 v) { __hip_atomic_store((LAS u32*)p, v, __ATOMIC_RELAXED, __HIP_MEMORY_SCOPE_WORKGROUP); }
__device__ __forceinline__ void wg_barrier() { asm volatile("s_waitcnt lgkmcnt(0)\n\ts_barrier" ::: "memory"); }
__device__ __forceinline__ void drain() { asm volatile("s_waitcnt vmcnt(0)" ::: "memory"); }

__device__ __forceinline__ void put(gu64* g, u32 tag, float v) {
  __hip_atomic_store(g, ((u64)tag << 32) | (u64)__float_as_uint(v), __ATOMIC_RELAXED, __HIP_MEMORY_SCOPE_AGENT);
}
__device__ __forceinline__ u64 get(const gu64* g) { return __hip_atomic_load(g, __ATOMIC_RELAXED, __HIP_MEMORY_SCOPE_AGENT); }
__device__ __forceinline__ void put32(gu32* g, u32 v) { __hip_atomic_store(g, v, __ATOMIC_RELAXED, __HIP_MEMORY_SCOPE_AGENT); }
__device__ __forceinline__ u32 get32(const gu32* g) { return __hip_atomic_load(g, __ATOMIC_RELAXED, __HIP_MEMORY_SCOPE_AGENT); }
__device__ __forceinline__ bool tag_ok(u64 x, u32 tag) { return (u32)(x >> 32) == tag; }
__device__ __forceinline__ float val(u64 x) { return __uint_as_float((u32)x); }
typedef __amdgpu_buffer_rsrc_t rsrc_t;
__device__ __forceinline__ rsrc_t mkrs(void* p, u32 n) { return __builtin_amdgcn_make_buffer_rsrc(p, 0, n, 0x00020000); }
constexpr int SC1 = 16;

template <int LN> __device__ __forceinline__ void gemv_step(float (&acc)[8], float xv, u32x4 w) {
  const float xb = dpp<BCAST + LN>(xv);
#pragma unroll
  for (int k = 0; k < 4; k++) { acc[2 * k] = fmaf(xb, bf_lo(w[k]), acc[2 * k]); acc[2 * k + 1] = fmaf(xb, bf_hi(w[k]), acc[2 * k + 1]); }
}
template <int LN> __device__ __forceinline__ void gemv4(float (&acc)[8], float xv, u32x4 (&w)[4]) {
  gemv_step<LN>(acc, xv, w[0]); gemv_step<LN + 1>(acc, xv, w[1]); gemv_step<LN + 2>(acc, xv, w[2]); gemv_step<LN + 3>(acc, xv, w[3]);
}
__device__ __forceinline__ void pin8(float (&a)[8]) {
  asm volatile("" : "+v"(a[0]), "+v"(a[1]), "+v"(a[2]), "+v"(a[3]), "+v"(a[4]), "+v"(a[5]), "+v"(a[6]), "+v"(a[7]));
}

struct Lds {
  float part[4][256];
  float qp[4][128];
  float ap[4][132];
  float h[128];
  u32 chunk[256];
  u32 next;
  u32 pad[3];
  u16 q[128];
  u32 ctl[4];
};

#define STAMP(i) do {} while (0)

__global__ void __launch_bounds__(320, 2)
fused_decode(const u16* __restrict__ x, const u16* __restrict__ kc, const u16* __restrict__ vc, const u16* __restrict__ wq,
             const u16* __restrict__ wo, const u16* __restrict__ wg, const u16* __restrict__ wu, const u16* __restrict__ wd,
             u16* __restrict__ out, unsigned char* __restrict__ ws) {
  __shared__ __attribute__((aligned(16))) Lds L;
  const int b = blockIdx.x, head = b >> 3, j = b & 7;
  const int lane = threadIdx.x & 63, wave = __builtin_amdgcn_readfirstlane(threadIdx.x >> 6);
  const int r = lane >> 4, c = lane & 15;
  const bool has_ffn = j != (head & 7);
  const int unit = has_ffn ? 7 * head + j - (j > (head & 7) ? 1 : 0) : 0;
  const int ug = unit >> 3, us = unit & 7;
  const int dg = head - ((head - j + 7) >> 3);
  STAMP(0);

  if (wave < 4) {
    const int w = wave;
    u32x4 X0, X1; u32 xs;
    u32x4 P0[4], P1[4], P2[4], P3[4], K[4], V[4];
    const i32x4 rx = mkr(x, D * 2);
    {
      const u32 xo = lane * 16;
      const u32 so = (u32)(((c < 8 ? j : us) * 128 + 32 * w + 4 * (c & 7) + r) * 2);
      asm volatile("buffer_load_dwordx4 %0, %1, %2, 0 offen" : "=v"(X0) : "v"(xo), "s"(rx));
      asm volatile("buffer_load_dwordx4 %0, %1, %2, 0 offen offset:1024" : "=v"(X1) : "v"(xo), "s"(rx));
      asm volatile("buffer_load_ushort %0, %1, %2, 0 offen" : "=v"(xs) : "v"(so), "s"(rx));
    }
    {
      const i32x4 rq = mkr(wq, (u32)D * 4096 * 2);
      const u32 vo = r * 8192 + c * 16, s0 = (u32)(j * 128 + 32 * w) * 8192 + head * 256;
      rows4_o(P0, vo, rq, s0, 32768); rows4_o(P1, vo, rq, s0 + 4 * 32768, 32768);
    }
    const u32 nffn = has_ffn ? (u32)D * FF * 2 : 0u;
    const i32x4 rg = mkr(wg, nffn), ru = mkr(wu, nffn);
    constexpr u32 GSTEP = 4 * FF * 2;
    const u32 vg = r * (FF * 2) + c * 16, g0 = (u32)(us * 128 + 32 * w) * (FF * 2) + ug * 256;
    const char* kb = (const char*)kc + (size_t)head * HEAD_BYTES;
    const char* vb = (const char*)vc + (size_t)head * HEAD_BYTES;
    const i32x4 rk = mkr(kb, HEAD_BYTES), rv = mkr(vb, HEAD_BYTES);
    const u32 voff = lane * 16;
    const int st0 = j * 256 + w;
    asm volatile("s_waitcnt vmcnt(8)" : "+v"(X0), "+v"(X1), "+v"(xs));
    float xn;
    {
      float v[16];
#pragma unroll
      for (int k = 0; k < 4; k++) { v[2 * k] = bf_lo(X0[k]); v[2 * k + 1] = bf_hi(X0[k]); v[8 + 2 * k] = bf_lo(X1[k]); v[9 + 2 * k] = bf_hi(X1[k]); }
      float s = 0.f;
#pragma unroll
      for (int k = 0; k < 16; k++) s += v[k];
      const float mean = wave_sum(s) * (1.0f / D);
      float q = 0.f;
#pragma unroll
      for (int k = 0; k < 16; k++) { const float d = v[k] - mean; q = fmaf(d, d, q); }
      const float var = wave_sum(q) * (1.0f / D);
      const float den = rbf(sqrtf(rbf(rbf(var) + 1e-5f)));
      xn = rbf(rbf(bf_lo(xs) - rbf(mean)) / den);
    }
    asm volatile("" : "+v"(xn));
    STAMP(1);
    {
      float a[8] = {0.f, 0.f, 0.f, 0.f, 0.f, 0.f, 0.f, 0.f};
      wait4<4>(P0); rows4_o(P2, vg, rg, g0, GSTEP); pin4(P0);
      gemv4<0>(a, xn, P0); pin8(a);
      wait4<4>(P1); rows4_o(P3, vg, rg, g0 + 4 * GSTEP, GSTEP); pin4(P1);
      gemv4<4>(a, xn, P1);
      *(f32x2*)&L.qp[w][8 * c + 2 * r] = fold_rows(a);
    }
    wg_barrier();
    STAMP(2);
    {
      float a[8] = {0.f, 0.f, 0.f, 0.f, 0.f, 0.f, 0.f, 0.f};
      wait4<4>(P2); rows4_o(P0, vg, ru, g0, GSTEP); pin4(P2);
      gemv4<8>(a, xn, P2); pin8(a);
      wait4<4>(P3); rows4_o(P1, vg, ru, g0 + 4 * GSTEP, GSTEP); pin4(P3);
      gemv4<12>(a, xn, P3);
      *(f32x2*)&L.part[w][8 * c + 2 * r] = fold_rows(a);
    }
    {
      float a[8] = {0.f, 0.f, 0.f, 0.f, 0.f, 0.f, 0.f, 0.f};
      wait4<4>(P0); issue4_o(K, voff, rk, (u32)st0 * 4096); pin4(P0);
      gemv4<8>(a, xn, P0); pin8(a);
      wait4<4>(P1); issue4_o(V, voff, rv, (u32)st0 * 4096); pin4(P1);
      gemv4<12>(a, xn, P1);
      *(f32x2*)&L.part[w][128 + 8 * c + 2 * r] = fold_rows(a);
    }
    STAMP(3);
    wg_barrier();
    STAMP(4);
    const u32x4 q = *(const u32x4*)&L.q[8 * c];
    const u32 tag = __builtin_amdgcn_readfirstlane(L.ctl[0]);

    u32x4 W0[4], W1[4];
#pragma unroll
    for (int i = 0; i < 4; i++) { asm volatile("" : "=v"(W0[i])); asm volatile("" : "=v"(W1[i])); }
    const i32x4 rwo = mkr(wo, (u32)4096 * D * 2);
    const u32 wvo = (16 * (r >> 1) + (r & 1)) * 2048 + c * 16, wso = (u32)(head * 128 + 32 * w) * 2048 + j * 256;
    float m = -INFINITY, l = 0.f, acc[8] = {0.f, 0.f, 0.f, 0.f, 0.f, 0.f, 0.f, 0.f};
    u32 inv;
    auto claim = [&]() -> u32 {
      u32 k = 0;
      if (lane == 0) k = lds_inc(&L.next);
      return __builtin_amdgcn_readfirstlane(k);
    };
    u32 slot = claim();
    u32 cv = lds_get(&L.chunk[slot >> 2]);
    do {
      wait4<4>(K);
      float d[4];
#pragma unroll
      for (int t = 0; t < 4; t++) d[t] = dot2(K[t][3], q[3], dot2(K[t][2], q[2], dot2(K[t][1], q[1], dot2(K[t][0], q[0], 0.f))));
      asm volatile("" : "+v"(d[0]), "+v"(d[1]), "+v"(d[2]), "+v"(d[3]));
      u32 cs = __builtin_amdgcn_readfirstlane(cv);
#pragma unroll 1
      for (u32 spin = 0; cs == EMPTY && spin < 64 * SPIN_CAP; spin++) cs = __builtin_amdgcn_readfirstlane(lds_get(&L.chunk[slot >> 2]));
      const u32 t = min(cs, (u32)NTILE) + (slot & 3u);
      inv = t >> 11;
      issue4_or(K, voff, rk, t * 4096, inv, W0, wvo, rwo, wso, wso + 4096, wso + 8192, wso + 12288);
      asm volatile("" : "+v"(d[0]), "+v"(d[1]), "+v"(d[2]), "+v"(d[3]));
      const bool b8 = (c & 8) != 0, b4 = (c & 4) != 0;
      const float e0 = (b8 ? d[2] : d[0]) + dpp<ROR8>(b8 ? d[0] : d[2]);
      const float e1 = (b8 ? d[3] : d[1]) + dpp<ROR8>(b8 ? d[1] : d[3]);
      float f = (b4 ? e1 : e0) + dpp<HMIRROR>(b4 ? e0 : e1);
      f += dpp<XOR1>(f); f += dpp<XOR2>(f);
      const float y = rbf(rbf(f) * 0.08838834764831845f) * 1.4426950408889634f;
      float ym = fmaxf(y, dpp<ROR4>(y)); ym = fmaxf(ym, dpp<ROR8>(ym));
      if (__any(ym > m)) {
        const float mn = fmaxf(m, ym), al = __builtin_amdgcn_exp2f(m - mn);
        l *= al;
#pragma unroll
        for (int k = 0; k < 8; k++) acc[k] *= al;
        m = mn;
      }
      const float p = __builtin_amdgcn_exp2f(y - m);
      l += p;
      wait4<4>(V);
      gemv_step<0>(acc, p, V[0]); gemv_step<4>(acc, p, V[1]); gemv_step<8>(acc, p, V[2]); gemv_step<12>(acc, p, V[3]);
      pin8(acc);
      issue4_or(V, voff, rv, t * 4096, inv, W1, wvo, rwo, wso + 16384, wso + 20480, wso + 24576, wso + 28672);
      if (!inv) { slot = claim(); cv = lds_get(&L.chunk[slot >> 2]); }
    } while (!inv);
    u32x4 D0[4], D1[4];
    {
      const i32x4 rd = mkr(wd, has_ffn ? (u32)FF * D * 2 : 0u);
      const u32 dso = (u32)(dg * 128 + 32 * w) * 2048 + j * 256;
      rows4_o(D0, wvo, rd, dso, 4096); rows4_o(D1, wvo, rd, dso + 16384, 4096);
    }
    STAMP(5);
    {
      const float M = xrow_max(m), sc = __builtin_amdgcn_exp2f(m - M);
#pragma unroll
      for (int k = 0; k < 8; k++) acc[k] *= sc;
      const float ls = wave_sum(l * sc) * 0.25f;
      *(f32x2*)&L.ap[w][8 * c + 2 * r] = fold_rows(acc);
      if (lane == 0) *(f32x2*)&L.ap[w][128] = f32x2{M, ls};
    }
    wg_barrier();
    STAMP(6);
    {
      const float hv = has_ffn ? L.h[32 * w + 16 * (r >> 1) + 2 * (c & 7) + (r & 1)] : 0.f;
      wait4<8>(W0); wait4<8>(W1);
      float wf[8][8];
#pragma unroll
      for (int i2 = 0; i2 < 4; i2++)
#pragma unroll
        for (int k = 0; k < 4; k++) {
          wf[i2][2 * k] = bf_lo(W0[i2][k]); wf[i2][2 * k + 1] = bf_hi(W0[i2][k]);
          wf[4 + i2][2 * k] = bf_lo(W1[i2][k]); wf[4 + i2][2 * k + 1] = bf_hi(W1[i2][k]);
        }
#pragma unroll
      for (int i2 = 0; i2 < 8; i2++) pin8(wf[i2]);
      float a[8] = {0.f, 0.f, 0.f, 0.f, 0.f, 0.f, 0.f, 0.f};
      wait4<4>(D0); gemv4<0>(a, hv, D0);
      wait4<0>(D1); gemv4<4>(a, hv, D1);
      pin8(a);
      const gu64* s1 = (const gu64*)(ws + WS_S1) + (size_t)(head * 8 + r) * 144;
      u64 g[8];
      for (u32 spin = 0;; spin++) {
        g[0] = get(s1 + 32 * w + 2 * c); g[1] = get(s1 + 32 * w + 2 * c + 1); g[2] = get(s1 + 128); g[3] = get(s1 + 129);
        g[4] = get(s1 + 4 * 144 + 32 * w + 2 * c); g[5] = get(s1 + 4 * 144 + 32 * w + 2 * c + 1); g[6] = get(s1 + 4 * 144 + 128); g[7] = get(s1 + 4 * 144 + 129);
        bool ok = true;
#pragma unroll
        for (int k = 0; k < 8; k++) ok &= tag_ok(g[k], tag);
        if (__all(ok) || spin > SPIN_CAP) break;
      }
      STAMP(7);
      const float M = xrow_max(fmaxf(val(g[2]), val(g[6])));
      const float w0 = __builtin_amdgcn_exp2f(val(g[2]) - M), w1 = __builtin_amdgcn_exp2f(val(g[6]) - M);
      const float Lt = xrow_sum(fmaf(w0, val(g[3]), w1 * val(g[7])));
      float o = swap16_add(fmaf(w0, val(g[0]), w1 * val(g[4])), fmaf(w0, val(g[1]), w1 * val(g[5])));
      o = swap32_add(o, o);
      o = rbf(o / Lt);
      const float orot = dpp<ROR8>(o);
      o = (r & 2) ? orot : o;
#define WSTEP(n) { const float xb = dpp<BCAST + n>(o); _Pragma("unroll") for (int k = 0; k < 8; k++) a[k] = fmaf(xb, wf[n][k], a[k]); }
      WSTEP(0) WSTEP(1) WSTEP(2) WSTEP(3) WSTEP(4) WSTEP(5) WSTEP(6) WSTEP(7)
#undef WSTEP
      *(f32x2*)&L.qp[w][8 * c + 2 * r] = fold_rows(a);
    }
    wg_barrier();
    STAMP(8);
  } else {
    const u32 ep = get32((gu32*)(ws + WS_EPOCH));
    const u32 cb = get32((gu32*)(ws + WS_CTR + head * CTR_STRIDE));
    const u32 xfin = ((const GAS u32*)x)[2 * b + (lane & 1)];
    const u32 tag = __builtin_amdgcn_readfirstlane(ep) + 1u;
    gu64* qrec = (gu64*)(ws + WS_QREC);
    if (j == 0 && lane == 0) put(qrec + (size_t)b * 144 + 128, tag, __uint_as_float(cb));
#pragma unroll
    for (int k = 0; k < 4; k++) { const int rd = lane + 64 * k; L.chunk[rd] = rd < SPW ? (u32)(j * 64 + rd) * 4 : EMPTY; }
    if (lane == 0) L.next = 4;
    STAMP(1);
    wg_barrier();
    u32 cbase;
    {
      float s0 = L.qp[0][lane], s1 = L.qp[0][64 + lane];
#pragma unroll
      for (int k = 1; k < 4; k++) { s0 += L.qp[k][lane]; s1 += L.qp[k][64 + lane]; }
      put(qrec + (size_t)b * 144 + lane, tag, s0); put(qrec + (size_t)b * 144 + 64 + lane, tag, s1);
      STAMP(2);
      const gu64* qr = qrec + (size_t)head * 8 * 144;
      __builtin_amdgcn_s_sleep(QDELAY);
      u64 g[17];
      for (u32 spin = 0;; spin++) {
#pragma unroll
        for (int k = 0; k < 8; k++) { g[2 * k] = get(qr + k * 144 + lane); g[2 * k + 1] = get(qr + k * 144 + 64 + lane); }
        g[16] = get(qr + 128);
        bool ok = true;
#pragma unroll
        for (int k = 0; k < 17; k++) ok &= tag_ok(g[k], tag);
        if (__all(ok) || spin > SPIN_CAP) break;
      }
      float q0 = 0.f, q1 = 0.f;
#pragma unroll
      for (int k = 0; k < 8; k++) { q0 += val(g[2 * k]); q1 += val(g[2 * k + 1]); }
      L.q[lane] = bf_bits(q0); L.q[64 + lane] = bf_bits(q1);
      if (lane == 0) L.ctl[0] = tag;
      cbase = __builtin_amdgcn_readfirstlane((u32)g[16]);
    }
    STAMP(3);
    wg_barrier();
    STAMP(4);
    if (has_ffn) {
      const rsrc_t rgu = mkrs(ws + WS_GU, 224 * 256 * 4);
      f32x2 sg = *(const f32x2*)&L.part[0][2 * lane], su = *(const f32x2*)&L.part[0][128 + 2 * lane];
#pragma unroll
      for (int k = 1; k < 4; k++) { sg += *(const f32x2*)&L.part[k][2 * lane]; su += *(const f32x2*)&L.part[k][128 + 2 * lane]; }
      __builtin_amdgcn_raw_buffer_store_b128(__builtin_bit_cast(u32x4, f32x4{sg.x, sg.y, su.x, su.y}), rgu, unit * 1024 + lane * 16, 0, SC1);
      drain();
      if (lane == 0) put32((gu32*)(ws + WS_FLAG1 + unit * 64), tag);
      const gu32* f1 = (const gu32*)(ws + WS_FLAG1 + (dg * 8 + (lane & 7)) * 64);
      __builtin_amdgcn_s_sleep(FDELAY1);
      for (u32 spin = 0; !__all(get32(f1) == tag) && spin < SPIN_CAP; spin++) __builtin_amdgcn_s_sleep(32);
      __builtin_amdgcn_fence(__ATOMIC_ACQUIRE, "wavefront");
      u32x4 pl[8];
#pragma unroll
      for (int k = 0; k < 8; k++) pl[k] = __builtin_amdgcn_raw_buffer_load_b128(rgu, (dg * 8 + k) * 1024 + lane * 16, 0, SC1);
      asm volatile("" : "+v"(pl[0]), "+v"(pl[1]), "+v"(pl[2]), "+v"(pl[3]), "+v"(pl[4]), "+v"(pl[5]), "+v"(pl[6]), "+v"(pl[7]));
      f32x4 s = {0.f, 0.f, 0.f, 0.f};
#pragma unroll
      for (int k = 0; k < 8; k++) s += __builtin_bit_cast(f32x4, pl[k]);
      auto act = [](float g, float u) {
        g = rbf(g); u = rbf(u);
        const float inner = 0.7978845608028654f * (g + 0.044715f * g * g * g);
        return rbf(rbf(0.5f * g * (1.f + tanhf(inner))) * u);
      };
      *(f32x2*)&L.h[2 * lane] = f32x2{act(s.x, s.z), act(s.y, s.w)};
    }
    STAMP(5);
    if (SPW < 64) {
      const i32x4 rc = mkr(ws + WS_CTR + head * CTR_STRIDE, 4);
      const u32 aoff = lane == 0 ? 0u : 0x7ffffff0u;
      u32 t0 = 0, t1 = 0, t2 = 0;
      int rho = SPW;
      auto demand = [&]() {
#pragma unroll 1
        for (u32 spin = 0; spin < 64 * SPIN_CAP; spin++) {
          if ((int)__builtin_amdgcn_readfirstlane(lds_get(&L.next)) + LOOKAHEAD >= 4 * rho) break;
          __builtin_amdgcn_s_sleep(2);
        }
      };
      auto step = [&](u32& tnew, u32& told) -> bool {
        demand();
        ticket(tnew, aoff, rc);
        wait1<2>(told);
        const u32 n = __builtin_amdgcn_readfirstlane(told) - cbase;
        const u32 cv = n < 8u * (64 - SPW) ? (n & 7u) * 256u + (SPW + (n >> 3)) * 4u : (u32)NTILE;
        if (lane == 0) lds_put(&L.chunk[rho - 2], cv);
        rho++;
        return cv >= (u32)NTILE || rho >= 250;
      };
      while (__builtin_amdgcn_readfirstlane(lds_get(&L.next)) + 40 < 4u * SPW) __builtin_amdgcn_s_sleep(32);
      demand(); ticket(t0, aoff, rc); rho++;
      demand(); ticket(t1, aoff, rc); rho++;
      while (!step(t2, t0) && !step(t0, t1) && !step(t1, t2)) {}
      if (rho - 2 + lane < 256) lds_put(&L.chunk[rho - 2 + lane], (u32)NTILE);
      wait1<0>(t0); wait1<0>(t1); wait1<0>(t2);
    } else lds_put(&L.chunk[64 + lane], (u32)NTILE);
    const int jj = b >> 5, col = (b & 31) * 4 + 2 * (lane & 1);
    STAMP(9);
    wg_barrier();
    STAMP(6);
    {
      float mw[4], lw[4];
#pragma unroll
      for (int k = 0; k < 4; k++) { const f32x2 t = *(const f32x2*)&L.ap[k][128]; mw[k] = t.x; lw[k] = t.y; }
      const float M = fmaxf(fmaxf(mw[0], mw[1]), fmaxf(mw[2], mw[3]));
      float Lt = 0.f, o0 = 0.f, o1 = 0.f;
#pragma unroll
      for (int k = 0; k < 4; k++) {
        const float s = __builtin_amdgcn_exp2f(mw[k] - M);
        Lt = fmaf(s, lw[k], Lt);
        o0 = fmaf(s, L.ap[k][lane], o0); o1 = fmaf(s, L.ap[k][64 + lane], o1);
      }
      gu64* s1 = (gu64*)(ws + WS_S1) + (size_t)b * 144;
      put(s1 + lane, tag, o0); put(s1 + 64 + lane, tag, o1);
      if (lane < 2) put(s1 + 128 + lane, tag, lane == 0 ? M : Lt);
    }
    STAMP(7);
    wg_barrier();
    STAMP(8);
    {
      float s0 = L.qp[0][lane], s1 = L.qp[0][64 + lane];
#pragma unroll
      for (int k = 1; k < 4; k++) { s0 += L.qp[k][lane]; s1 += L.qp[k][64 + lane]; }
      gu64* s2 = (gu64*)(ws + WS_S2);
      put(s2 + (size_t)b * 128 + lane, tag, s0); put(s2 + (size_t)b * 128 + 64 + lane, tag, s1);
      const gu64* sr = s2 + (size_t)((lane >> 1) * 8 + jj) * 128 + col;
      float a0, a1;
      for (u32 spin = 0;; spin++) {
        const u64 g0 = get(sr), g1 = get(sr + 1);
        a0 = val(g0); a1 = val(g1);
        if (__all(tag_ok(g0, tag) & tag_ok(g1, tag)) || spin > SPIN_CAP) break;
      }
      a0 += dpp<XOR2>(a0); a1 += dpp<XOR2>(a1);
      a0 += dpp<ROR4>(a0); a1 += dpp<ROR4>(a1);
      a0 += dpp<ROR8>(a0); a1 += dpp<ROR8>(a1);
      a0 = xrow_sum(a0); a1 = xrow_sum(a1);
      const float r0 = bf_lo(xfin) + a0, r1 = bf_hi(xfin) + a1;
      if (lane < 2) ((GAS u32*)out)[2 * b + lane] = cvt_pk(r0, r1);
      if (b == 0 && lane == 0) put32((gu32*)(ws + WS_EPOCH), tag);
    }
    STAMP(10);
  }
}

}

extern "C" void kernel_launch(void* const* d_in, const int* in_sizes, int n_in, void* d_out, int out_size, void* d_ws, size_t ws_size,
                              hipStream_t stream) {
  (void)in_sizes; (void)n_in; (void)out_size; (void)ws_size;
  fused_decode<<<256, 320, 0, stream>>>((const u16*)d_in[0], (const u16*)d_in[1], (const u16*)d_in[2], (const u16*)d_in[3], (const u16*)d_in[4],
                                        (const u16*)d_in[6], (const u16*)d_in[7], (const u16*)d_in[8], (u16*)d_out, (unsigned char*)d_ws);
}
```

```cpp
#include <hip/hip_runtime.h>
#include <stdint.h>

namespace {

typedef unsigned u32;
typedef unsigned long long u64;
typedef unsigned short u16;
typedef u32 u32x4 __attribute__((ext_vector_type(4)));
typedef int i32x4 __attribute__((ext_vector_type(4)));
typedef float f32x2 __attribute__((ext_vector_type(2)));
typedef float f32x4 __attribute__((ext_vector_type(4)));
typedef __attribute__((ext_vector_type(2))) __bf16 bf16x2;
#define GAS __attribute__((address_space(1)))
typedef GAS u64 gu64;
typedef GAS u32 gu32;

constexpr int D = 1024, NH = 32, SK = 32768, DH = 128, FF = 3584;
constexpr int NTILE = SK / 16;
#define QDELAY 8
#define FDELAY1 64
#define SPW_ 48
#define AHEAD 1
#define BCOVER 0
constexpr int SPW = SPW_;
static_assert(NTILE == 2048 && SPW >= 4 && SPW <= 64, "");
constexpr u32 HEAD_BYTES = (u32)SK * DH * 2;

constexpr size_t WS_EPOCH = 0;
constexpr size_t CTR_STRIDE = 4352;
constexpr size_t WS_CTR = 1u << 20;
constexpr size_t WS_FLAG1 = 16384;
constexpr size_t WS_QREC = 49152;
constexpr size_t WS_S1 = WS_QREC + 256 * 144 * 8;
constexpr size_t WS_S2 = WS_S1 + 256 * 144 * 8;
constexpr size_t WS_GU = WS_S2 + 256 * 128 * 8;
constexpr size_t WS_STAMP = 2u << 20;
static_assert(WS_GU + 224 * 256 * 4 <= WS_CTR, "");
constexpr u32 SPIN_CAP = 1u << 16;
constexpr u32 EMPTY = 0xffffffffu;
#define NMAX 16
#define PHASE_SHIFT 9
#define ALT_ROUND (SPW - 2)
#define LOOKAHEAD 12

__device__ __forceinline__ float bf_lo(u32 v) { return __uint_as_float(v << 16); }
__device__ __forceinline__ float bf_hi(u32 v) { return __uint_as_float(v & 0xffff0000u); }
__device__ __forceinline__ u32 cvt_pk(float lo, float hi) {
  bf16x2 r = {(__bf16)lo, (__bf16)hi};
  return __builtin_bit_cast(u32, r);
}
__device__ __forceinline__ u16 bf_bits(float x) { return __builtin_bit_cast(u16, (__bf16)x); }
__device__ __forceinline__ float rbf(float x) { return bf_lo((u32)bf_bits(x)); }
__device__ __forceinline__ float dot2(u32 a, u32 b, float c) {
  return __builtin_amdgcn_fdot2_f32_bf16(__builtin_bit_cast(bf16x2, a), __builtin_bit_cast(bf16x2, b), c, false);
}
template <int CTRL> __device__ __forceinline__ float dpp(float x) {
  return __builtin_bit_cast(float, __builtin_amdgcn_mov_dpp(__builtin_bit_cast(int, x), CTRL, 0xf, 0xf, true));
}
constexpr int XOR1 = 0xB1, XOR2 = 0x4E, HMIRROR = 0x141, ROR8 = 0x128, ROR4 = 0x124, BCAST = 0x150;
__device__ __forceinline__ float swap32_add(float a, float b) {
  auto s = __builtin_amdgcn_permlane32_swap(__float_as_uint(a), __float_as_uint(b), false, false);
  return __uint_as_float(s[0]) + __uint_as_float(s[1]);
}
__device__ __forceinline__ float swap16_add(float a, float b) {
  auto s = __builtin_amdgcn_permlane16_swap(__float_as_uint(a), __float_as_uint(b), false, false);
  return __uint_as_float(s[0]) + __uint_as_float(s[1]);
}
__device__ __forceinline__ float xrow_sum(float x) { x = swap16_add(x, x); return swap32_add(x, x); }
__device__ __forceinline__ float xrow_max(float x) {
  auto s = __builtin_amdgcn_permlane16_swap(__float_as_uint(x), __float_as_uint(x), false, false);
  x = fmaxf(__uint_as_float(s[0]), __uint_as_float(s[1]));
  auto t = __builtin_amdgcn_permlane32_swap(__float_as_uint(x), __float_as_uint(x), false, false);
  return fmaxf(__uint_as_float(t[0]), __uint_as_float(t[1]));
}
__device__ __forceinline__ float row_sum(float x) { x += dpp<XOR1>(x); x += dpp<XOR2>(x); x += dpp<ROR4>(x); x += dpp<ROR8>(x); return x; }
__device__ __forceinline__ float wave_sum(float x) { return xrow_sum(row_sum(x)); }
__device__ __forceinline__ f32x2 fold_rows(const float (&a)[8]) {
  float s0 = swap32_add(a[0], a[4]), s1 = swap32_add(a[1], a[5]), s2 = swap32_add(a[2], a[6]), s3 = swap32_add(a[3], a[7]);
  return f32x2{swap16_add(s0, s2), swap16_add(s1, s3)};
}

__device__ __forceinline__ i32x4 mkr(const void* p, u32 n) {
  u64 a = (u64)p;
  i32x4 r; r.x = (int)(u32)a; r.y = (int)((u32)(a >> 32) & 0xffffu); r.z = (int)n; r.w = 0x00020000; return r;
}
template <int IMM> __device__ __forceinline__ void ld_nt_o(u32x4& d, u32 voff, i32x4 r, u32 soff) {
  asm volatile("buffer_load_dwordx4 %0, %1, %2, %3 offen offset:%4 nt" : "=v"(d) : "v"(voff), "s"(r), "s"(soff), "i"(IMM));
}
__device__ __forceinline__ void issue4_o(u32x4 (&R)[4], u32 voff, i32x4 r, u32 soff) {
  ld_nt_o<0>(R[0], voff, r, soff); ld_nt_o<1024>(R[1], voff, r, soff); ld_nt_o<2048>(R[2], voff, r, soff); ld_nt_o<3072>(R[3], voff, r, soff);
}
__device__ __forceinline__ void rows4_o(u32x4 (&R)[4], u32 voff, i32x4 r, u32 soff, u32 step) {
  ld_nt_o<0>(R[0], voff, r, soff); ld_nt_o<0>(R[1], voff, r, soff + step); ld_nt_o<0>(R[2], voff, r, soff + 2 * step); ld_nt_o<0>(R[3], voff, r, soff + 3 * step);
}
__device__ __forceinline__ void issue4(u32x4 (&R)[4], u32 voff, i32x4 r, u32 soff) {
  asm volatile(
      "buffer_load_dwordx4 %0, %4, %5, %6 offen nt\n\t"
      "buffer_load_dwordx4 %1, %4, %5, %6 offen offset:1024 nt\n\t"
      "buffer_load_dwordx4 %2, %4, %5, %6 offen offset:2048 nt\n\t"
      "buffer_load_dwordx4 %3, %4, %5, %6 offen offset:3072 nt"
      : "+v"(R[0]), "+v"(R[1]), "+v"(R[2]), "+v"(R[3]) : "v"(voff), "s"(r), "s"(soff));
}
template <int N> __device__ __forceinline__ void wait4(u32x4 (&R)[4]) {
  asm volatile("s_waitcnt vmcnt(%4)" : "+v"(R[0]), "+v"(R[1]), "+v"(R[2]), "+v"(R[3]) : "i"(N));
}
__device__ __forceinline__ void pin4(u32x4 (&R)[4]) { asm volatile("" : "+v"(R[0]), "+v"(R[1]), "+v"(R[2]), "+v"(R[3])); }
template <int N> __device__ __forceinline__ void wait1(u32& t) { asm volatile("s_waitcnt vmcnt(%1)" : "+v"(t) : "i"(N)); }
__device__ __forceinline__ void ticket(u32& t, u32 aoff, i32x4 r) {
  asm volatile("v_mov_b32 %0, 1\n\tbuffer_atomic_add %0, %1, %2, 0 offen sc0" : "+v"(t) : "v"(aoff), "s"(r));
}
#define LAS __attribute__((address_space(3)))
__device__ __forceinline__ u32 lds_get(const u32* p) { return __hip_atomic_load((const LAS u32*)p, __ATOMIC_RELAXED, __HIP_MEMORY_SCOPE_WORKGROUP); }
__device__ __forceinline__ u32 lds_inc(u32* p) { return __hip_atomic_fetch_add((LAS u32*)p, 1u, __ATOMIC_RELAXED, __HIP_MEMORY_SCOPE_WORKGROUP); }
__device__ __forceinline__ void lds_put(u32* p, u32 v) { __hip_atomic_store((LAS u32*)p, v, __ATOMIC_RELAXED, __HIP_MEMORY_SCOPE_WORKGROUP); }
__device__ __forceinline__ void wg_barrier() { asm volatile("s_waitcnt lgkmcnt(0)\n\ts_barrier" ::: "memory"); }
__device__ __forceinline__ void drain() { asm volatile("s_waitcnt vmcnt(0)" ::: "memory"); }

__device__ __forceinline__ void put(gu64* g, u32 tag, float v) {
  __hip_atomic_store(g, ((u64)tag << 32) | (u64)__float_as_uint(v), __ATOMIC_RELAXED, __HIP_MEMORY_SCOPE_AGENT);
}
__device__ __forceinline__ u64 get(const gu64* g) { return __hip_atomic_load(g, __ATOMIC_RELAXED, __HIP_MEMORY_SCOPE_AGENT); }
__device__ __forceinline__ void put32(gu32* g, u32 v) { __hip_atomic_store(g, v, __ATOMIC_RELAXED, __HIP_MEMORY_SCOPE_AGENT); }
__device__ __forceinline__ u32 get32(const gu32* g) { return __hip_atomic_load(g, __ATOMIC_RELAXED, __HIP_MEMORY_SCOPE_AGENT); }
__device__ __forceinline__ bool tag_ok(u64 x, u32 tag) { return (u32)(x >> 32) == tag; }
__device__ __forceinline__ float val(u64 x) { return __uint_as_float((u32)x); }
typedef __amdgpu_buffer_rsrc_t rsrc_t;
__device__ __forceinline__ rsrc_t mkrs(void* p, u32 n) { return __builtin_amdgcn_make_buffer_rsrc(p, 0, n, 0x00020000); }
constexpr int SC1 = 16;

template <int LN> __device__ __forceinline__ void gemv_step(float (&acc)[8], float xv, u32x4 w) {
  const float xb = dpp<BCAST + LN>(xv);
#pragma unroll
  for (int k = 0; k < 4; k++) { acc[2 * k] = fmaf(xb, bf_lo(w[k]), acc[2 * k]); acc[2 * k + 1] = fmaf(xb, bf_hi(w[k]), acc[2 * k + 1]); }
}
template <int LN> __device__ __forceinline__ void gemv4(float (&acc)[8], float xv, u32x4 (&w)[4]) {
  gemv_step<LN>(acc, xv, w[0]); gemv_step<LN + 1>(acc, xv, w[1]); gemv_step<LN + 2>(acc, xv, w[2]); gemv_step<LN + 3>(acc, xv, w[3]);
}
#define PVDOT 0
template <int LN> __device__ __forceinline__ void pv_dot(float (&acc)[8], u32 plo, u32 phi, u32x4 v) {
  const u32 a = (u32)__builtin_amdgcn_mov_dpp((int)plo, BCAST + LN, 0xf, 0xf, true), b = (u32)__builtin_amdgcn_mov_dpp((int)phi, BCAST + LN, 0xf, 0xf, true);
#pragma unroll
  for (int k = 0; k < 4; k++) { acc[2 * k] = dot2(a, v[k], acc[2 * k]); acc[2 * k + 1] = dot2(b, v[k], acc[2 * k + 1]); }
}
__device__ __forceinline__ void pv_tile(float (&acc)[8], float p, u32x4 (&V)[4]) {
  gemv_step<0>(acc, p, V[0]); gemv_step<4>(acc, p, V[1]); gemv_step<8>(acc, p, V[2]); gemv_step<12>(acc, p, V[3]);
}
#define PHASE_OFF 0
#define PACE_T0 590
#define PACE_D16 890
#define PACE_SPREAD 0
#define COMMON_REF 1
__device__ __forceinline__ u32 min_time(u32 a, u32 b) { return (int)(a - b) < 0 ? a : b; }
#define KCOVER 1
#define DMID 0
template <int N> struct Int { static constexpr int value = N; };
#define KPAD 0
#define VPAD 0
#define KEARLY 0
#define RELCLK 0
#define PHASE_TICKS 1024
__device__ __forceinline__ bool is_k_phase(u32 now) {
  return ((now >> PHASE_SHIFT) & 1u) == 0u;
}
__device__ __forceinline__ void pin8(float (&a)[8]) {
  asm volatile("" : "+v"(a[0]), "+v"(a[1]), "+v"(a[2]), "+v"(a[3]), "+v"(a[4]), "+v"(a[5]), "+v"(a[6]), "+v"(a[7]));
}

struct Lds {
  float part[4][256];
  float qp[4][128];
  float ap[4][132];
  float h[128];
  float y[4][64][64];
  u16 q[128];
  u32 ctl[4];
};

#define STAMP(i) do {} while (0)

__global__ void __launch_bounds__(320, 2)
fused_decode(const u16* __restrict__ x, const u16* __restrict__ kc, const u16* __restrict__ vc, const u16* __restrict__ wq,
             const u16* __restrict__ wo, const u16* __restrict__ wg, const u16* __restrict__ wu, const u16* __restrict__ wd,
             u16* __restrict__ out, unsigned char* __restrict__ ws) {
  __shared__ __attribute__((aligned(16))) Lds L;
  const int b = blockIdx.x, head = b >> 3, j = b & 7;
  const int lane = threadIdx.x & 63, wave = __builtin_amdgcn_readfirstlane(threadIdx.x >> 6);
  const int r = lane >> 4, c = lane & 15;
  const bool has_ffn = j != (head & 7);
  const int unit = has_ffn ? 7 * head + j - (j > (head & 7) ? 1 : 0) : 0;
  const int ug = unit >> 3, us = unit & 7;
  const int dg = head - ((head - j + 7) >> 3);
  STAMP(0);

  if (wave < 4) {
    const int w = wave;
    u32x4 X0, X1; u32 xs;
    u32x4 P0[4], P1[4], P2[4], P3[4], A[4], B[4];
    const i32x4 rx = mkr(x, D * 2);
    {
      const u32 xo = lane * 16;
      const u32 so = (u32)(((c < 8 ? j : us) * 128 + 32 * w + 4 * (c & 7) + r) * 2);
      asm volatile("buffer_load_dwordx4 %0, %1, %2, 0 offen" : "=v"(X0) : "v"(xo), "s"(rx));
      asm volatile("buffer_load_dwordx4 %0, %1, %2, 0 offen offset:1024" : "=v"(X1) : "v"(xo), "s"(rx));
      asm volatile("buffer_load_ushort %0, %1, %2, 0 offen" : "=v"(xs) : "v"(so), "s"(rx));
    }
    {
      const i32x4 rq = mkr(wq, (u32)D * 4096 * 2);
      const u32 vo = r * 8192 + c * 16, s0 = (u32)(j * 128 + 32 * w) * 8192 + head * 256;
      rows4_o(P0, vo, rq, s0, 32768); rows4_o(P1, vo, rq, s0 + 4 * 32768, 32768);
    }
    const u32 nffn = has_ffn ? (u32)D * FF * 2 : 0u;
    const i32x4 rg = mkr(wg, nffn), ru = mkr(wu, nffn);
    constexpr u32 GSTEP = 4 * FF * 2;
    const u32 vg = r * (FF * 2) + c * 16, g0 = (u32)(us * 128 + 32 * w) * (FF * 2) + ug * 256;
    const char* kb = (const char*)kc + (size_t)head * HEAD_BYTES;
    const char* vb = (const char*)vc + (size_t)head * HEAD_BYTES;
    const i32x4 rk = mkr(kb, HEAD_BYTES), rv = mkr(vb, HEAD_BYTES);
    const u32 voff = lane * 16;
    const int st0 = j * 256 + w;
    asm volatile("s_waitcnt vmcnt(8)" : "+v"(X0), "+v"(X1), "+v"(xs));
    float xn;
    {
      float v[16];
#pragma unroll
      for (int k = 0; k < 4; k++) { v[2 * k] = bf_lo(X0[k]); v[2 * k + 1] = bf_hi(X0[k]); v[8 + 2 * k] = bf_lo(X1[k]); v[9 + 2 * k] = bf_hi(X1[k]); }
      float s = 0.f;
#pragma unroll
      for (int k = 0; k < 16; k++) s += v[k];
      const float mean = wave_sum(s) * (1.0f / D);
      float q = 0.f;
#pragma unroll
      for (int k = 0; k < 16; k++) { const float d = v[k] - mean; q = fmaf(d, d, q); }
      const float var = wave_sum(q) * (1.0f / D);
      const float den = rbf(sqrtf(rbf(rbf(var) + 1e-5f)));
      xn = rbf(rbf(bf_lo(xs) - rbf(mean)) / den);
    }
    asm volatile("" : "+v"(xn));
    STAMP(1);
    {
      float a[8] = {0.f, 0.f, 0.f, 0.f, 0.f, 0.f, 0.f, 0.f};
      wait4<4>(P0); rows4_o(P2, vg, rg, g0, GSTEP); pin4(P0);
      gemv4<0>(a, xn, P0); pin8(a);
      wait4<4>(P1); rows4_o(P3, vg, rg, g0 + 4 * GSTEP, GSTEP); pin4(P1);
      gemv4<4>(a, xn, P1);
      *(f32x2*)&L.qp[w][8 * c + 2 * r] = fold_rows(a);
    }
    wg_barrier();
    STAMP(2);
    {
      float a[8] = {0.f, 0.f, 0.f, 0.f, 0.f, 0.f, 0.f, 0.f};
      wait4<4>(P2); rows4_o(P0, vg, ru, g0, GSTEP); pin4(P2);
      gemv4<8>(a, xn, P2); pin8(a);
      wait4<4>(P3); rows4_o(P1, vg, ru, g0 + 4 * GSTEP, GSTEP); pin4(P3);
      gemv4<12>(a, xn, P3);
      *(f32x2*)&L.part[w][8 * c + 2 * r] = fold_rows(a);
    }
    {
      float a[8] = {0.f, 0.f, 0.f, 0.f, 0.f, 0.f, 0.f, 0.f};
      wait4<4>(P0); issue4_o(A, voff, rk, (u32)st0 * 4096); pin4(P0);
      gemv4<8>(a, xn, P0); pin8(a);
      wait4<4>(P1); issue4_o(B, voff, rk, (u32)(st0 + 4) * 4096); pin4(P1);
      gemv4<12>(a, xn, P1);
      *(f32x2*)&L.part[w][128 + 8 * c + 2 * r] = fold_rows(a);
    }
    wait4<4>(A); issue4_o(P2, voff, rk, (u32)(st0 + 8) * 4096);
    wait4<4>(B); issue4_o(P3, voff, rk, (u32)(st0 + 12) * 4096);
    STAMP(3);
    wg_barrier();
    STAMP(4);
    const u32x4 q = *(const u32x4*)&L.q[8 * c];
    const u32 tag = __builtin_amdgcn_readfirstlane(L.ctl[0]);
    const u32 tref = __builtin_amdgcn_readfirstlane(L.ctl[1]);

    float m = -INFINITY, l = 0.f, acc[8] = {0.f, 0.f, 0.f, 0.f, 0.f, 0.f, 0.f, 0.f};
#define PROG() do {} while (0)
    u32 due16 = (tref << 4) + PACE_T0 * 16 + (w * 32 + (PACE_SPREAD ? head : 0)) * PACE_D16 / 128;
    auto clock16 = [&]() -> u32 { return (u32)__builtin_amdgcn_s_memrealtime() << 4; };
    auto pace = [&](u32 early) {
      if ((int)(early - due16) < 0) {
#pragma unroll 1
        while ((int)(clock16() - due16) < 0) __builtin_amdgcn_s_sleep(1);
      }
      due16 += PACE_D16;
    };
    auto kscore = [&](u32x4 (&R)[4], int n) {
      float d[4];
#pragma unroll
      for (int u = 0; u < 4; u++) d[u] = dot2(R[u][3], q[3], dot2(R[u][2], q[2], dot2(R[u][1], q[1], dot2(R[u][0], q[0], 0.f))));
      const bool b8 = (c & 8) != 0, b4 = (c & 4) != 0;
      const float e0 = (b8 ? d[2] : d[0]) + dpp<ROR8>(b8 ? d[0] : d[2]);
      const float e1 = (b8 ? d[3] : d[1]) + dpp<ROR8>(b8 ? d[1] : d[3]);
      float f = (b4 ? e1 : e0) + dpp<HMIRROR>(b4 ? e0 : e1);
      f += dpp<XOR1>(f); f += dpp<XOR2>(f);
      const float y = rbf(rbf(f) * 0.08838834764831845f) * 1.4426950408889634f;
      L.y[w][n][lane] = y;
      m = fmaxf(m, y);
      asm volatile("" : "+v"(m));
    };
    auto kstep = [&](auto wait_tag, u32x4 (&R)[4], int n, int nq) {
      i32x4 rr = rk;
      rr.x = nq < 64 ? rk.x : rv.x; rr.y = nq < 64 ? rk.y : rv.y;
      u32 soff = (u32)(st0 + 4 * (nq & 63)) * 4096;
      u32 early = clock16();
      asm volatile("" : "+s"(rr), "+s"(soff), "+s"(early));
      wait4<decltype(wait_tag)::value>(R);
      kscore(R, n);
      if (KPAD) __builtin_amdgcn_s_sleep(KPAD);
      pace(early);
      issue4(R, voff, rr, soff);
      PROG();
    };
    auto vload = [&](int n) -> float { return __builtin_amdgcn_exp2f(L.y[w][n][lane] - m); };
    auto vstep = [&](auto wait_tag, u32x4 (&R)[4], int n) {
      u32 soff = (u32)(st0 + 4 * (n + 2)) * 4096;
      u32 early = clock16();
      float p = vload(n);
      asm volatile("" : "+s"(soff), "+v"(p), "+s"(early));
      wait4<decltype(wait_tag)::value>(R);
      pv_tile(acc, p, R);
      l += p;
      pin8(acc);
      if (VPAD) __builtin_amdgcn_s_sleep(VPAD);
      pace(early);
      issue4(R, voff, rv, soff);
      PROG();
    };
    kstep(Int<8>{}, A, 0, 4); kstep(Int<12>{}, B, 1, 5);
    wait4<12>(P2); kscore(P2, 2); wait4<8>(P3); kscore(P3, 3);
    PROG(); PROG();
#pragma unroll 1
    for (int n = 4; n < 64; n += 2) { kstep(Int<4>{}, A, n, n + 2); kstep(Int<4>{}, B, n + 1, n + 3); }
    m = fmaxf(m, dpp<ROR4>(m)); m = fmaxf(m, dpp<ROR8>(m)); m = xrow_max(m);
    const u32 wvo = (16 * (r >> 1) + (r & 1)) * 2048 + c * 16;
    const i32x4 rd = mkr(wd, has_ffn ? (u32)FF * D * 2 : 0u);
    const u32 dso = (u32)(dg * 128 + 32 * w) * 2048 + j * 256;
    u32x4 W0[4], W1[4], D0[4], D1[4];
    float a[8] = {0.f, 0.f, 0.f, 0.f, 0.f, 0.f, 0.f, 0.f};
#pragma unroll 1
    for (int n = 0; n < 62; n += 2) { vstep(Int<4>{}, A, n); vstep(Int<4>{}, B, n + 1); }
    {
      const i32x4 rwo = mkr(wo, (u32)4096 * D * 2);
      const u32 wso = (u32)(head * 128 + 32 * w) * 2048 + j * 256;
      const float p0 = vload(62), p1 = vload(63);
      wait4<4>(A);
      rows4_o(W0, wvo, rwo, wso, 4096); rows4_o(W1, wvo, rwo, wso + 16384, 4096);
      pin4(A);
      pv_tile(acc, p0, A); pin8(acc);
      wait4<8>(B);
      rows4_o(D0, wvo, rd, dso, 4096); rows4_o(D1, wvo, rd, dso + 16384, 4096);
      pin4(B);
      pv_tile(acc, p1, B);
      l += p0 + p1;
    }
    STAMP(5);
    {
      const float ls = wave_sum(l) * 0.25f;
      *(f32x2*)&L.ap[w][8 * c + 2 * r] = fold_rows(acc);
      if (lane == 0) *(f32x2*)&L.ap[w][128] = f32x2{m, ls};
    }
    wg_barrier();
    STAMP(6);
    {
      const float hv = has_ffn ? L.h[32 * w + 16 * (r >> 1) + 2 * (c & 7) + (r & 1)] : 0.f;
      wait4<8>(W0); wait4<8>(W1);
      float wf[8][8];
#pragma unroll
      for (int i2 = 0; i2 < 4; i2++)
#pragma unroll
        for (int k = 0; k < 4; k++) {
          wf[i2][2 * k] = bf_lo(W0[i2][k]); wf[i2][2 * k + 1] = bf_hi(W0[i2][k]);
          wf[4 + i2][2 * k] = bf_lo(W1[i2][k]); wf[4 + i2][2 * k + 1] = bf_hi(W1[i2][k]);
        }
#pragma unroll
      for (int i2 = 0; i2 < 8; i2++) pin8(wf[i2]);
      wait4<4>(D0); gemv4<0>(a, hv, D0);
      wait4<0>(D1); gemv4<4>(a, hv, D1);
      pin8(a);
      const gu64* s1 = (const gu64*)(ws + WS_S1) + (size_t)(head * 8 + r) * 144;
      u64 g[8];
      for (u32 spin = 0;; spin++) {
        g[0] = get(s1 + 32 * w + 2 * c); g[1] = get(s1 + 32 * w + 2 * c + 1); g[2] = get(s1 + 128); g[3] = get(s1 + 129);
        g[4] = get(s1 + 4 * 144 + 32 * w + 2 * c); g[5] = get(s1 + 4 * 144 + 32 * w + 2 * c + 1); g[6] = get(s1 + 4 * 144 + 128); g[7] = get(s1 + 4 * 144 + 129);
        bool ok = true;
#pragma unroll
        for (int k = 0; k < 8; k++) ok &= tag_ok(g[k], tag);
        if (__all(ok) || spin > SPIN_CAP) break;
      }
      STAMP(7);
      const float M = xrow_max(fmaxf(val(g[2]), val(g[6])));
      const float w0 = __builtin_amdgcn_exp2f(val(g[2]) - M), w1 = __builtin_amdgcn_exp2f(val(g[6]) - M);
      const float Lt = xrow_sum(fmaf(w0, val(g[3]), w1 * val(g[7])));
      float o = swap16_add(fmaf(w0, val(g[0]), w1 * val(g[4])), fmaf(w0, val(g[1]), w1 * val(g[5])));
      o = swap32_add(o, o);
      o = rbf(o / Lt);
      const float orot = dpp<ROR8>(o);
      o = (r & 2) ? orot : o;
#define WSTEP(n) { const float xb = dpp<BCAST + n>(o); _Pragma("unroll") for (int k = 0; k < 8; k++) a[k] = fmaf(xb, wf[n][k], a[k]); }
      WSTEP(0) WSTEP(1) WSTEP(2) WSTEP(3) WSTEP(4) WSTEP(5) WSTEP(6) WSTEP(7)
#undef WSTEP
      *(f32x2*)&L.qp[w][8 * c + 2 * r] = fold_rows(a);
    }
    wg_barrier();
    STAMP(8);
  } else {
    const u32 tstart = (u32)__builtin_amdgcn_s_memrealtime();
    const u32 ep = get32((gu32*)(ws + WS_EPOCH));
    const u32 xfin = ((const GAS u32*)x)[2 * b + (lane & 1)];
    const u32 tag = __builtin_amdgcn_readfirstlane(ep) + 1u;
    gu64* qrec = (gu64*)(ws + WS_QREC);
    if (j == 0 && lane == 0) put(qrec + (size_t)b * 144 + 128, tag, __uint_as_float(tstart));
    STAMP(1);
    wg_barrier();
    {
      float s0 = L.qp[0][lane], s1 = L.qp[0][64 + lane];
#pragma unroll
      for (int k = 1; k < 4; k++) { s0 += L.qp[k][lane]; s1 += L.qp[k][64 + lane]; }
      put(qrec + (size_t)b * 144 + lane, tag, s0); put(qrec + (size_t)b * 144 + 64 + lane, tag, s1);
      STAMP(2);
      const gu64* qr = qrec + (size_t)head * 8 * 144;
      __builtin_amdgcn_s_sleep(QDELAY);
      u64 g[17];
      for (u32 spin = 0;; spin++) {
#pragma unroll
        for (int k = 0; k < 8; k++) { g[2 * k] = get(qr + k * 144 + lane); g[2 * k + 1] = get(qr + k * 144 + 64 + lane); }
        g[16] = get(qr + 128);
        bool ok = true;
#pragma unroll
        for (int k = 0; k < 17; k++) ok &= tag_ok(g[k], tag);
        if (__all(ok) || spin > SPIN_CAP) break;
      }
      float q0 = 0.f, q1 = 0.f;
#pragma unroll
      for (int k = 0; k < 8; k++) { q0 += val(g[2 * k]); q1 += val(g[2 * k + 1]); }
      L.q[lane] = bf_bits(q0); L.q[64 + lane] = bf_bits(q1);
      if (lane == 0) { L.ctl[2] = ~tag; L.ctl[0] = tag; L.ctl[1] = COMMON_REF ? min_time((u32)g[16], tstart) : tstart; }
    }
    STAMP(3);
    wg_barrier();
    STAMP(4);
    if (has_ffn) {
      const rsrc_t rgu = mkrs(ws + WS_GU, 224 * 256 * 4);
      f32x2 sg = *(const f32x2*)&L.part[0][2 * lane], su = *(const f32x2*)&L.part[0][128 + 2 * lane];
#pragma unroll
      for (int k = 1; k < 4; k++) { sg += *(const f32x2*)&L.part[k][2 * lane]; su += *(const f32x2*)&L.part[k][128 + 2 * lane]; }
      __builtin_amdgcn_raw_buffer_store_b128(__builtin_bit_cast(u32x4, f32x4{sg.x, sg.y, su.x, su.y}), rgu, unit * 1024 + lane * 16, 0, SC1);
      drain();
      if (lane == 0) put32((gu32*)(ws + WS_FLAG1 + unit * 64), tag);
      const gu32* f1 = (const gu32*)(ws + WS_FLAG1 + (dg * 8 + (lane & 7)) * 64);
      __builtin_amdgcn_s_sleep(FDELAY1);
      for (u32 spin = 0; !__all(get32(f1) == tag) && spin < SPIN_CAP; spin++) __builtin_amdgcn_s_sleep(32);
      __builtin_amdgcn_fence(__ATOMIC_ACQUIRE, "wavefront");
      u32x4 pl[8];
#pragma unroll
      for (int k = 0; k < 8; k++) pl[k] = __builtin_amdgcn_raw_buffer_load_b128(rgu, (dg * 8 + k) * 1024 + lane * 16, 0, SC1);
      asm volatile("" : "+v"(pl[0]), "+v"(pl[1]), "+v"(pl[2]), "+v"(pl[3]), "+v"(pl[4]), "+v"(pl[5]), "+v"(pl[6]), "+v"(pl[7]));
      f32x4 s = {0.f, 0.f, 0.f, 0.f};
#pragma unroll
      for (int k = 0; k < 8; k++) s += __builtin_bit_cast(f32x4, pl[k]);
      auto act = [](float g, float u) {
        g = rbf(g); u = rbf(u);
        const float inner = 0.7978845608028654f * (g + 0.044715f * g * g * g);
        return rbf(rbf(0.5f * g * (1.f + tanhf(inner))) * u);
      };
      *(f32x2*)&L.h[2 * lane] = f32x2{act(s.x, s.z), act(s.y, s.w)};
    }
    asm volatile("s_waitcnt lgkmcnt(0)" ::: "memory");
    if (lane == 0) lds_put(&L.ctl[2], tag);
    STAMP(5);
    const int jj = b >> 5, col = (b & 31) * 4 + 2 * (lane & 1);
    STAMP(9);
    wg_barrier();
    STAMP(6);
    {
      float mw[4], lw[4];
#pragma unroll
      for (int k = 0; k < 4; k++) { const f32x2 t = *(const f32x2*)&L.ap[k][128]; mw[k] = t.x; lw[k] = t.y; }
      const float M = fmaxf(fmaxf(mw[0], mw[1]), fmaxf(mw[2], mw[3]));
      float Lt = 0.f, o0 = 0.f, o1 = 0.f;
#pragma unroll
      for (int k = 0; k < 4; k++) {
        const float s = __builtin_amdgcn_exp2f(mw[k] - M);
        Lt = fmaf(s, lw[k], Lt);
        o0 = fmaf(s, L.ap[k][lane], o0); o1 = fmaf(s, L.ap[k][64 + lane], o1);
      }
      gu64* s1 = (gu64*)(ws + WS_S1) + (size_t)b * 144;
      put(s1 + lane, tag, o0); put(s1 + 64 + lane, tag, o1);
      if (lane < 2) put(s1 + 128 + lane, tag, lane == 0 ? M : Lt);
    }
    STAMP(7);
    wg_barrier();
    STAMP(8);
    {
      float s0 = L.qp[0][lane], s1 = L.qp[0][64 + lane];
#pragma unroll
      for (int k = 1; k < 4; k++) { s0 += L.qp[k][lane]; s1 += L.qp[k][64 + lane]; }
      gu64* s2 = (gu64*)(ws + WS_S2);
      put(s2 + (size_t)b * 128 + lane, tag, s0); put(s2 + (size_t)b * 128 + 64 + lane, tag, s1);
      const gu64* sr = s2 + (size_t)((lane >> 1) * 8 + jj) * 128 + col;
      float a0, a1;
      for (u32 spin = 0;; spin++) {
        const u64 g0 = get(sr), g1 = get(sr + 1);
        a0 = val(g0); a1 = val(g1);
        if (__all(tag_ok(g0, tag) & tag_ok(g1, tag)) || spin > SPIN_CAP) break;
      }
      a0 += dpp<XOR2>(a0); a1 += dpp<XOR2>(a1);
      a0 += dpp<ROR4>(a0); a1 += dpp<ROR4>(a1);
      a0 += dpp<ROR8>(a0); a1 += dpp<ROR8>(a1);
      a0 = xrow_sum(a0); a1 = xrow_sum(a1);
      const float r0 = bf_lo(xfin) + a0, r1 = bf_hi(xfin) + a1;
      if (lane < 2) ((GAS u32*)out)[2 * b + lane] = cvt_pk(r0, r1);
      if (b == 0 && lane == 0) put32((gu32*)(ws + WS_EPOCH), tag);
    }
    STAMP(10);
  }
}

}

extern "C" void kernel_launch(void* const* d_in, const int* in_sizes, int n_in, void* d_out, int out_size, void* d_ws, size_t ws_size,
                              hipStream_t stream) {
  (void)in_sizes; (void)n_in; (void)out_size; (void)ws_size;
  fused_decode<<<256, 320, 0, stream>>>((const u16*)d_in[0], (const u16*)d_in[1], (const u16*)d_in[2], (const u16*)d_in[3], (const u16*)d_in[4],
                                        (const u16*)d_in[6], (const u16*)d_in[7], (const u16*)d_in[8], (u16*)d_out, (unsigned char*)d_ws);
}
```

```cpp
#include <hip/hip_runtime.h>
#include <stdint.h>

namespace {

typedef unsigned u32;
typedef unsigned long long u64;
typedef unsigned short u16;
typedef u32 u32x4 __attribute__((ext_vector_type(4)));
typedef int i32x4 __attribute__((ext_vector_type(4)));
typedef float f32x2 __attribute__((ext_vector_type(2)));
typedef float f32x4 __attribute__((ext_vector_type(4)));
typedef __attribute__((ext_vector_type(2))) __bf16 bf16x2;
#define GAS __attribute__((address_space(1)))
#define LAS __attribute__((address_space(3)))
typedef GAS u64 gu64;
typedef GAS u32 gu32;

constexpr int D = 1024, SK = 32768, DH = 128, FF = 3584;
constexpr u32 HEAD_BYTES = (u32)SK * DH * 2;

#define PACE_T0 590
#define PACE_D16 882
#define PACE_NA 24
#define PACE_D16A 940
#define PACE_D16V PACE_D16
#define WS_BASE 0
#define WD_DELAY 10
#define DMID 0
template <int N> struct Int { static constexpr int value = N; };
#define S1_DELAY 0
#define S2_DELAY 8
#define QDELAY 8
#define FDELAY1 64
#define POLL_SLEEP 0

constexpr size_t WS_EPOCH = 0;
constexpr size_t WS_FLAG1 = 16384;
constexpr size_t WS_ML = 32768;
constexpr size_t WS_QREC = 65536;
constexpr size_t WS_S1 = WS_QREC + 256 * 80 * 8;
constexpr size_t WS_S2 = WS_S1 + 256 * 128 * 8;
constexpr size_t WS_GU = WS_S2 + 256 * 128 * 8;
constexpr size_t WS_STAMP = 2u << 20;
constexpr u32 SPIN_CAP = 1u << 16;

__device__ __forceinline__ float bf_lo(u32 v) { return __uint_as_float(v << 16); }
__device__ __forceinline__ float bf_hi(u32 v) { return __uint_as_float(v & 0xffff0000u); }
__device__ __forceinline__ u32 cvt_pk(float lo, float hi) {
  bf16x2 r = {(__bf16)lo, (__bf16)hi};
  return __builtin_bit_cast(u32, r);
}
__device__ __forceinline__ u16 bf_bits(float x) { return __builtin_bit_cast(u16, (__bf16)x); }
__device__ __forceinline__ float rbf(float x) { return bf_lo((u32)bf_bits(x)); }
__device__ __forceinline__ float dot2(u32 a, u32 b, float c) {
  return __builtin_amdgcn_fdot2_f32_bf16(__builtin_bit_cast(bf16x2, a), __builtin_bit_cast(bf16x2, b), c, false);
}
template <int CTRL> __device__ __forceinline__ float dpp(float x) {
  return __builtin_bit_cast(float, __builtin_amdgcn_mov_dpp(__builtin_bit_cast(int, x), CTRL, 0xf, 0xf, true));
}
constexpr int XOR1 = 0xB1, XOR2 = 0x4E, HMIRROR = 0x141, ROR8 = 0x128, ROR4 = 0x124, BCAST = 0x150;
__device__ __forceinline__ float swap32_add(float a, float b) {
  auto s = __builtin_amdgcn_permlane32_swap(__float_as_uint(a), __float_as_uint(b), false, false);
  return __uint_as_float(s[0]) + __uint_as_float(s[1]);
}
__device__ __forceinline__ float swap16_add(float a, float b) {
  auto s = __builtin_amdgcn_permlane16_swap(__float_as_uint(a), __float_as_uint(b), false, false);
  return __uint_as_float(s[0]) + __uint_as_float(s[1]);
}
__device__ __forceinline__ float xrow_sum(float x) { x = swap16_add(x, x); return swap32_add(x, x); }
__device__ __forceinline__ float xrow_max(float x) {
  auto s = __builtin_amdgcn_permlane16_swap(__float_as_uint(x), __float_as_uint(x), false, false);
  x = fmaxf(__uint_as_float(s[0]), __uint_as_float(s[1]));
  auto t = __builtin_amdgcn_permlane32_swap(__float_as_uint(x), __float_as_uint(x), false, false);
  return fmaxf(__uint_as_float(t[0]), __uint_as_float(t[1]));
}
__device__ __forceinline__ float row_sum(float x) { x += dpp<XOR1>(x); x += dpp<XOR2>(x); x += dpp<ROR4>(x); x += dpp<ROR8>(x); return x; }
__device__ __forceinline__ float wave_sum(float x) { return xrow_sum(row_sum(x)); }
__device__ __forceinline__ f32x2 fold_rows(const float (&a)[8]) {
  float s0 = swap32_add(a[0], a[4]), s1 = swap32_add(a[1], a[5]), s2 = swap32_add(a[2], a[6]), s3 = swap32_add(a[3], a[7]);
  return f32x2{swap16_add(s0, s2), swap16_add(s1, s3)};
}

__device__ __forceinline__ i32x4 mkr(const void* p, u32 n) {
  u64 a = (u64)p;
  i32x4 r; r.x = (int)(u32)a; r.y = (int)((u32)(a >> 32) & 0xffffu); r.z = (int)n; r.w = 0x00020000; return r;
}
__device__ __forceinline__ void ld_o(u32x4& d, u32 voff, i32x4 r, u32 soff) {
  asm volatile("buffer_load_dwordx4 %0, %1, %2, %3 offen nt" : "=v"(d) : "v"(voff), "s"(r), "s"(soff));
}
__device__ __forceinline__ void rows4_o(u32x4 (&R)[4], u32 voff, i32x4 r, u32 soff, u32 step) {
  ld_o(R[0], voff, r, soff); ld_o(R[1], voff, r, soff + step); ld_o(R[2], voff, r, soff + 2 * step); ld_o(R[3], voff, r, soff + 3 * step);
}
#define TILE_LOADS \
      "buffer_load_dwordx4 %0, %4, %5, %6 offen nt\n\t" \
      "buffer_load_dwordx4 %1, %4, %5, %6 offen offset:1024 nt\n\t" \
      "buffer_load_dwordx4 %2, %4, %5, %6 offen offset:2048 nt\n\t" \
      "buffer_load_dwordx4 %3, %4, %5, %6 offen offset:3072 nt"
__device__ __forceinline__ void tile_o(u32x4 (&R)[4], u32 voff, i32x4 r, u32 soff) {
  asm volatile(TILE_LOADS : "=&v"(R[0]), "=&v"(R[1]), "=&v"(R[2]), "=&v"(R[3]) : "v"(voff), "s"(r), "s"(soff));
}
__device__ __forceinline__ void tile(u32x4 (&R)[4], u32 voff, i32x4 r, u32 soff) {
  asm volatile(TILE_LOADS : "+v"(R[0]), "+v"(R[1]), "+v"(R[2]), "+v"(R[3]) : "v"(voff), "s"(r), "s"(soff));
}
template <int N> __device__ __forceinline__ void wait4(u32x4 (&R)[4]) {
  asm volatile("s_waitcnt vmcnt(%4)" : "+v"(R[0]), "+v"(R[1]), "+v"(R[2]), "+v"(R[3]) : "i"(N));
}
__device__ __forceinline__ void pin4(u32x4 (&R)[4]) { asm volatile("" : "+v"(R[0]), "+v"(R[1]), "+v"(R[2]), "+v"(R[3])); }
__device__ __forceinline__ void pin8(float (&a)[8]) {
  asm volatile("" : "+v"(a[0]), "+v"(a[1]), "+v"(a[2]), "+v"(a[3]), "+v"(a[4]), "+v"(a[5]), "+v"(a[6]), "+v"(a[7]));
}
__device__ __forceinline__ void wg_barrier() { asm volatile("s_waitcnt lgkmcnt(0)\n\ts_barrier" ::: "memory"); }
__device__ __forceinline__ void drain() { asm volatile("s_waitcnt vmcnt(0)" ::: "memory"); }
__device__ __forceinline__ void lds_done() { asm volatile("s_waitcnt lgkmcnt(0)" ::: "memory"); }
__device__ __forceinline__ u32 lds_get(const u32* p) { return __hip_atomic_load((const LAS u32*)p, __ATOMIC_RELAXED, __HIP_MEMORY_SCOPE_WORKGROUP); }
__device__ __forceinline__ void lds_put(u32* p, u32 v) { __hip_atomic_store((LAS u32*)p, v, __ATOMIC_RELAXED, __HIP_MEMORY_SCOPE_WORKGROUP); }

__device__ __forceinline__ void put(gu64* g, u32 tag, float v) {
  __hip_atomic_store(g, ((u64)tag << 32) | (u64)__float_as_uint(v), __ATOMIC_RELAXED, __HIP_MEMORY_SCOPE_AGENT);
}
__device__ __forceinline__ u64 get(const gu64* g) { return __hip_atomic_load(g, __ATOMIC_RELAXED, __HIP_MEMORY_SCOPE_AGENT); }
__device__ __forceinline__ void put32(gu32* g, u32 v) { __hip_atomic_store(g, v, __ATOMIC_RELAXED, __HIP_MEMORY_SCOPE_AGENT); }
__device__ __forceinline__ u32 get32(const gu32* g) { return __hip_atomic_load(g, __ATOMIC_RELAXED, __HIP_MEMORY_SCOPE_AGENT); }
__device__ __forceinline__ bool tag_ok(u64 x, u32 tag) { return (u32)(x >> 32) == tag; }
__device__ __forceinline__ float val(u64 x) { return __uint_as_float((u32)x); }

template <int LN> __device__ __forceinline__ void gemv_step(float (&acc)[8], float xv, u32x4 w) {
  const float xb = dpp<BCAST + LN>(xv);
#pragma unroll
  for (int k = 0; k < 4; k++) { acc[2 * k] = fmaf(xb, bf_lo(w[k]), acc[2 * k]); acc[2 * k + 1] = fmaf(xb, bf_hi(w[k]), acc[2 * k + 1]); }
}
template <int LN, int ST> __device__ __forceinline__ void gemv4(float (&acc)[8], float xv, u32x4 (&w)[4]) {
  gemv_step<LN>(acc, xv, w[0]); gemv_step<LN + ST>(acc, xv, w[1]); gemv_step<LN + 2 * ST>(acc, xv, w[2]); gemv_step<LN + 3 * ST>(acc, xv, w[3]);
}

__device__ __forceinline__ void gemv8(float (&acc)[8], const float* xv, u32x4 (&w)[4]) {
#pragma unroll
  for (int i = 0; i < 4; i++)
#pragma unroll
    for (int k = 0; k < 4; k++) { acc[2 * k] = fmaf(xv[i], bf_lo(w[i][k]), acc[2 * k]); acc[2 * k + 1] = fmaf(xv[i], bf_hi(w[i][k]), acc[2 * k + 1]); }
}
__device__ __forceinline__ float fold8(const float (&a)[8], bool b8) {
  const f32x2 s = fold_rows(a);
  return (b8 ? s.y : s.x) + dpp<ROR8>(b8 ? s.x : s.y);
}

struct Lds {
  float y[4][64][64];
  float part[4][128];
  float qp[4][128];
  float op[4][128];
  float h[128];
  float mw[4];
  float fw[4];
  u32 kdone[4];
  u16 q[128];
  u32 ctl[4];
};

#define STAMP(i) do {} while (0)
#define PROG() do {} while (0)

__global__ void __launch_bounds__(320, 2)
fused_decode(const u16* __restrict__ x, const u16* __restrict__ kc, const u16* __restrict__ vc, const u16* __restrict__ wq,
             const u16* __restrict__ wo, const u16* __restrict__ wg, const u16* __restrict__ wu, const u16* __restrict__ wd,
             u16* __restrict__ out, unsigned char* __restrict__ ws) {
  __shared__ __attribute__((aligned(16))) Lds L;
  const int b = blockIdx.x, head = b >> 3, j = b & 7;
  const int lane = threadIdx.x & 63, wave = __builtin_amdgcn_readfirstlane(threadIdx.x >> 6);
  const int r = lane >> 4, c = lane & 15;
  const bool has_ffn = j != (head & 7);
  const int ks = j >> 1, jb = j & 1, hr = head & 7;
  const int cg = 2 * head + jb - 2 * (head >> 3) - ((hr > 2 * ks || (hr == 2 * ks && jb)) ? 1 : 0) - (hr > 2 * ks + 1 ? 1 : 0);
  const int unit = cg * 4 + ks;
  const int dg = head - ((head - j + 7) >> 3);
  STAMP(0);

  if (wave < 4) {
    const int w = wave;
    u32x4 X0, X1, XS;
    u32x4 P0[4], P1[4], P2[4], P3[4], R0[4], R1[4], R2[4], R3[4];
    const i32x4 rx = mkr(x, D * 2);
    const u32 voff = lane * 16;
    const int r8 = lane >> 3, c8 = lane & 7;
    const u32 row0 = ks * 256 + 64 * w;
    {
      const u32 so = (row0 + 8 * r8) * 2;
      asm volatile("buffer_load_dwordx4 %0, %1, %2, 0 offen" : "=v"(X0) : "v"(voff), "s"(rx));
      asm volatile("buffer_load_dwordx4 %0, %1, %2, 0 offen offset:1024" : "=v"(X1) : "v"(voff), "s"(rx));
      asm volatile("buffer_load_dwordx4 %0, %1, %2, 0 offen" : "=v"(XS) : "v"(so), "s"(rx));
    }
    {
      const i32x4 rq = mkr(wq, (u32)D * 4096 * 2);
      const u32 vo = r8 * 8 * 8192 + c8 * 16, s0 = row0 * 8192 + head * 256 + jb * 128;
      rows4_o(P0, vo, rq, s0, 8192); rows4_o(P1, vo, rq, s0 + 4 * 8192, 8192);
    }
    const u32 nffn = has_ffn ? (u32)D * FF * 2 : 0u;
    const i32x4 rg = mkr(wg, nffn), ru = mkr(wu, nffn);
    constexpr u32 GSTEP = FF * 2;
    const u32 vg = r8 * 8 * (FF * 2) + c8 * 16, g0 = row0 * (FF * 2) + cg * 128;
    const i32x4 rk = mkr((const char*)kc + (size_t)head * HEAD_BYTES, HEAD_BYTES), rv = mkr((const char*)vc + (size_t)head * HEAD_BYTES, HEAD_BYTES);
    const u32 t0 = (u32)(j * 256 + w) * 4096;
    asm volatile("s_waitcnt vmcnt(%3)" : "+v"(X0), "+v"(X1), "+v"(XS) : "i"(8));
    float xn[8];
    {
      float v[16];
#pragma unroll
      for (int k = 0; k < 4; k++) { v[2 * k] = bf_lo(X0[k]); v[2 * k + 1] = bf_hi(X0[k]); v[8 + 2 * k] = bf_lo(X1[k]); v[9 + 2 * k] = bf_hi(X1[k]); }
      float s = 0.f;
#pragma unroll
      for (int k = 0; k < 16; k++) s += v[k];
      const float mean = wave_sum(s) * (1.0f / D);
      float q = 0.f;
#pragma unroll
      for (int k = 0; k < 16; k++) { const float d = v[k] - mean; q = fmaf(d, d, q); }
      const float var = wave_sum(q) * (1.0f / D);
      const float den = rbf(sqrtf(rbf(rbf(var) + 1e-5f)));
      const float mb = rbf(mean);
#pragma unroll
      for (int k = 0; k < 4; k++) { xn[2 * k] = rbf(rbf(bf_lo(XS[k]) - mb) / den); xn[2 * k + 1] = rbf(rbf(bf_hi(XS[k]) - mb) / den); }
    }
    pin8(xn);
    const bool b8 = (c & 8) != 0, b4 = (c & 4) != 0;
    STAMP(1);
    {
      float a[8] = {0.f, 0.f, 0.f, 0.f, 0.f, 0.f, 0.f, 0.f};
      wait4<4>(P0); rows4_o(P2, vg, rg, g0, GSTEP); pin4(P0);
      gemv8(a, xn, P0); pin8(a);
      wait4<4>(P1); rows4_o(P3, vg, rg, g0 + 4 * GSTEP, GSTEP); pin4(P1);
      gemv8(a, xn + 4, P1);
      L.qp[w][8 * c8 + r8] = fold8(a, b8);
    }
    wg_barrier();
    STAMP(2);
    {
      float a[8] = {0.f, 0.f, 0.f, 0.f, 0.f, 0.f, 0.f, 0.f};
      wait4<4>(P2); rows4_o(P0, vg, ru, g0, GSTEP); pin4(P2);
      gemv8(a, xn, P2); pin8(a);
      wait4<4>(P3); rows4_o(P1, vg, ru, g0 + 4 * GSTEP, GSTEP); pin4(P3);
      gemv8(a, xn + 4, P3);
      L.part[w][8 * c8 + r8] = fold8(a, b8);
    }
    {
      float a[8] = {0.f, 0.f, 0.f, 0.f, 0.f, 0.f, 0.f, 0.f};
      wait4<4>(P0); tile_o(R0, voff, rk, t0); pin4(P0);
      gemv8(a, xn, P0); pin8(a);
      wait4<4>(P1); tile_o(R1, voff, rk, t0 + 16384); pin4(P1);
      gemv8(a, xn + 4, P1);
      L.part[w][64 + 8 * c8 + r8] = fold8(a, b8);
    }
    wait4<4>(R0); tile_o(R2, voff, rk, t0 + 2 * 16384);
    wait4<4>(R1); tile_o(R3, voff, rk, t0 + 3 * 16384);
    STAMP(3);
    wg_barrier();
    STAMP(4);
    const u32x4 q = *(const u32x4*)&L.q[8 * c];
    const u32 tag = __builtin_amdgcn_readfirstlane(L.ctl[0]);
    const u32 tref = __builtin_amdgcn_readfirstlane(L.ctl[1]);

    float m = -INFINITY, acc[8] = {0.f, 0.f, 0.f, 0.f, 0.f, 0.f, 0.f, 0.f};
    u32 due16 = (tref << 4) + PACE_T0 * 16 + w * (PACE_D16 / 4);
    auto clock16 = [&]() -> u32 { return (u32)__builtin_amdgcn_s_memrealtime() << 4; };
    auto pace = [&](u32 early, u32 step) {
      if ((int)(early - due16) < 0) {
#pragma unroll 1
        while ((int)(clock16() - due16) < 0) __builtin_amdgcn_s_sleep(1);
      }
      due16 += step;
    };
    auto kstep = [&](u32x4 (&R)[4], int n) {
      i32x4 rr = rk;
      rr.x = n + 4 < 64 ? rk.x : rv.x; rr.y = n + 4 < 64 ? rk.y : rv.y;
      u32 soff = t0 + (u32)((n + 4) & 63) * 16384;
      u32 early = clock16();
      asm volatile("" : "+s"(rr), "+s"(soff), "+s"(early));
      wait4<12>(R);
      float d[4];
#pragma unroll
      for (int u = 0; u < 4; u++) d[u] = dot2(R[u][3], q[3], dot2(R[u][2], q[2], dot2(R[u][1], q[1], dot2(R[u][0], q[0], 0.f))));
      const float e0 = (b8 ? d[2] : d[0]) + dpp<ROR8>(b8 ? d[0] : d[2]);
      const float e1 = (b8 ? d[3] : d[1]) + dpp<ROR8>(b8 ? d[1] : d[3]);
      float f = (b4 ? e1 : e0) + dpp<HMIRROR>(b4 ? e0 : e1);
      f += dpp<XOR1>(f); f += dpp<XOR2>(f);
      const float y = rbf(rbf(f) * 0.08838834764831845f) * 1.4426950408889634f;
      L.y[w][n][lane] = y;
      m = fmaxf(m, y);
      asm volatile("" : "+v"(m));
      pace(early, n < PACE_NA ? PACE_D16A : PACE_D16);
      tile(R, voff, rr, soff);
      PROG();
    };
    auto vload = [&](int n) -> float { return __builtin_amdgcn_exp2f(L.y[w][n][lane] - m); };
    auto vstep = [&](auto younger, u32x4 (&R)[4], int n) {
      u32 soff = t0 + (u32)(n + 4) * 16384;
      u32 early = clock16();
      float p = vload(n);
      asm volatile("" : "+s"(soff), "+v"(p), "+s"(early));
      wait4<decltype(younger)::value>(R);
      gemv4<0, 4>(acc, p, R);
      pin8(acc);
      pace(early, PACE_D16V);
      tile(R, voff, rv, soff);
      PROG();
    };
#pragma unroll 1
    for (int n = 0; n < 64; n += 4) { kstep(R0, n); kstep(R1, n + 1); kstep(R2, n + 2); kstep(R3, n + 3); }
    m = fmaxf(m, dpp<ROR4>(m)); m = fmaxf(m, dpp<ROR8>(m)); m = xrow_max(m);
    if (lane == 0) L.mw[w] = m;
    lds_done();
    if (lane == 0) lds_put(&L.kdone[w], tag);
    const u32 wvo = (16 * (r & 1) + 8 * (r >> 1)) * 2048 + c * 16;
    const i32x4 rd = mkr(wd, has_ffn ? (u32)FF * D * 2 : 0u);
    const u32 dso = (u32)(dg * 128 + 32 * w) * 2048 + j * 256;
    u32x4 W0[4], W1[4], D0[4], D1[4];
    float a[8] = {0.f, 0.f, 0.f, 0.f, 0.f, 0.f, 0.f, 0.f};
#pragma unroll 1
    for (int n = DMID ? 4 : 0; n < 60; n += 4) { vstep(Int<12>{}, R0, n); vstep(Int<12>{}, R1, n + 1); vstep(Int<12>{}, R2, n + 2); vstep(Int<12>{}, R3, n + 3); }
    {
      const float p0 = vload(60), p1 = vload(61), p2 = vload(62), p3 = vload(63);
#pragma unroll 1
      for (u32 spin = 0; lds_get(&L.ctl[2]) != tag && spin < 64 * SPIN_CAP; spin++) __builtin_amdgcn_s_sleep(2);
      asm volatile("" ::: "memory");
      const float fs = L.fw[w];
      wait4<12>(R0); gemv4<0, 4>(acc, p0, R0);
      wait4<8>(R1); gemv4<0, 4>(acc, p1, R1);
      pin8(acc);
      const i32x4 rwo = mkr(wo, (u32)4096 * D * 2);
      const u32 wso = (u32)(head * 128 + 32 * w) * 2048 + j * 256;
      due16 += WD_DELAY * 16 - w * (PACE_D16 / 4);
      pace(due16 - 1, 0);
      rows4_o(D0, wvo, rd, dso, 2048); rows4_o(D1, wvo, rd, dso + 8192, 2048);
      rows4_o(W0, wvo, rwo, wso, 2048); rows4_o(W1, wvo, rwo, wso + 8192, 2048);
      wait4<DMID ? 12 : 20>(R2); gemv4<0, 4>(acc, p2, R2);
      wait4<DMID ? 8 : 16>(R3); gemv4<0, 4>(acc, p3, R3);
      *(f32x2*)&L.op[w][8 * c + 2 * r] = fold_rows(acc) * fs;
    }
    STAMP(5);
    wg_barrier();
    STAMP(6);
    {
      const float hv = has_ffn ? L.h[32 * w + 16 * (r & 1) + 8 * (r >> 1) + (c & 7)] : 0.f;
      wait4<12>(D0); gemv4<0, 1>(a, hv, D0);
      wait4<8>(D1); gemv4<4, 1>(a, hv, D1);
      pin8(a);
      wait4<0>(W0); wait4<0>(W1);
      float wf[8][8];
#pragma unroll
      for (int i = 0; i < 4; i++)
#pragma unroll
        for (int k = 0; k < 4; k++) {
          wf[i][2 * k] = bf_lo(W0[i][k]); wf[i][2 * k + 1] = bf_hi(W0[i][k]);
          wf[4 + i][2 * k] = bf_lo(W1[i][k]); wf[4 + i][2 * k + 1] = bf_hi(W1[i][k]);
        }
#pragma unroll
      for (int i = 0; i < 8; i++) pin8(wf[i]);
      const gu64* s1 = (const gu64*)(ws + WS_S1) + (size_t)(head * 8 + (lane >> 5)) * 128 + 32 * w + (lane & 31);
      u64 g[4];
      if (S1_DELAY) __builtin_amdgcn_s_sleep(S1_DELAY);
      for (u32 spin = 0;; spin++) {
#pragma unroll
        for (int k = 0; k < 4; k++) g[k] = get(s1 + k * 256);
        if (__all(tag_ok(g[0], tag) & tag_ok(g[1], tag) & tag_ok(g[2], tag) & tag_ok(g[3], tag)) || spin > SPIN_CAP) break;
        if (POLL_SLEEP) __builtin_amdgcn_s_sleep(POLL_SLEEP);
      }
      STAMP(7);
      float o = (val(g[0]) + val(g[1])) + (val(g[2]) + val(g[3]));
      o = rbf(swap32_add(o, o));
      const float orot = dpp<ROR8>(o);
      o = (r & 2) ? orot : o;
#define WSTEP(n) { const float xb = dpp<BCAST + n>(o); _Pragma("unroll") for (int k = 0; k < 8; k++) a[k] = fmaf(xb, wf[n][k], a[k]); }
      WSTEP(0) WSTEP(1) WSTEP(2) WSTEP(3) WSTEP(4) WSTEP(5) WSTEP(6) WSTEP(7)
#undef WSTEP
      *(f32x2*)&L.qp[w][8 * c + 2 * r] = fold_rows(a);
    }
    wg_barrier();
    STAMP(8);
  } else {
    const u32 tstart = (u32)__builtin_amdgcn_s_memrealtime();
    const u32 ep = get32((gu32*)(ws + WS_EPOCH));
    const int fin = (head >> 2) * 8 + j;
    const u32 xfin = ((const GAS u16*)x)[16 * fin + c];
    const u32 tag = __builtin_amdgcn_readfirstlane(ep) + 1u;
    gu64* qrec = (gu64*)(ws + WS_QREC);
    if (lane == 0) put(qrec + (size_t)b * 80 + 64, tag, __uint_as_float(tstart));
    if (lane < 4) L.kdone[lane] = ~tag;
    if (lane == 0) { L.ctl[2] = ~tag; L.ctl[3] = ~tag; }
    STAMP(1);
    wg_barrier();
    {
      put(qrec + (size_t)b * 80 + lane, tag, (L.qp[0][lane] + L.qp[1][lane]) + (L.qp[2][lane] + L.qp[3][lane]));
      STAMP(2);
      const gu64* qr = qrec + (size_t)head * 8 * 80;
      __builtin_amdgcn_s_sleep(QDELAY);
      u64 g[9];
      for (u32 spin = 0;; spin++) {
#pragma unroll
        for (int k = 0; k < 8; k++) g[k] = get(qr + k * 80 + lane);
        g[8] = get(qr + (lane & 7) * 80 + 64);
        bool ok = true;
#pragma unroll
        for (int k = 0; k < 9; k++) ok &= tag_ok(g[k], tag);
        if (__all(ok) || spin > SPIN_CAP) break;
      }
      const float q0 = (val(g[0]) + val(g[2])) + (val(g[4]) + val(g[6])), q1 = (val(g[1]) + val(g[3])) + (val(g[5]) + val(g[7]));
      L.q[lane] = bf_bits(q0); L.q[64 + lane] = bf_bits(q1);
      int tm = (int)((u32)g[8] - tstart);
      tm = min(tm, __builtin_amdgcn_mov_dpp(tm, XOR1, 0xf, 0xf, true)); tm = min(tm, __builtin_amdgcn_mov_dpp(tm, XOR2, 0xf, 0xf, true));
      tm = min(tm, __builtin_amdgcn_mov_dpp(tm, HMIRROR, 0xf, 0xf, true));
      if (lane == 0) { L.ctl[0] = tag; L.ctl[1] = tstart + (u32)min(tm, 0); }
    }
    STAMP(3);
    wg_barrier();
    STAMP(4);
    if (has_ffn) {
      gu64* gu = (gu64*)(ws + WS_GU);
      const f32x2 mine = {(L.part[0][lane] + L.part[1][lane]) + (L.part[2][lane] + L.part[3][lane]),
                          (L.part[0][64 + lane] + L.part[1][64 + lane]) + (L.part[2][64 + lane] + L.part[3][64 + lane])};
      __hip_atomic_store(gu + unit * 64 + lane, __builtin_bit_cast(u64, mine), __ATOMIC_RELAXED, __HIP_MEMORY_SCOPE_AGENT);
      drain();
      if (lane == 0) put32((gu32*)(ws + WS_FLAG1 + unit * 64), tag);
      const gu32* f1 = (const gu32*)(ws + WS_FLAG1 + (dg * 8 + (lane & 7)) * 64);
      __builtin_amdgcn_s_sleep(FDELAY1);
      for (u32 spin = 0; !__all(get32(f1) == tag) && spin < SPIN_CAP; spin++) __builtin_amdgcn_s_sleep(32);
      __builtin_amdgcn_fence(__ATOMIC_ACQUIRE, "wavefront");
      u64 pl[8];
#pragma unroll
      for (int k = 0; k < 8; k++) pl[k] = get(gu + (dg * 8 + k) * 64 + lane);
      asm volatile("" : "+v"(pl[0]), "+v"(pl[1]), "+v"(pl[2]), "+v"(pl[3]), "+v"(pl[4]), "+v"(pl[5]), "+v"(pl[6]), "+v"(pl[7]));
      auto pair = [&](int k) { return __builtin_bit_cast(f32x2, pl[k]); };
      const f32x2 lo = (pair(0) + pair(1)) + (pair(2) + pair(3)), hi = (pair(4) + pair(5)) + (pair(6) + pair(7));
      auto act = [](float g, float u) {
        g = rbf(g); u = rbf(u);
        const float inner = 0.7978845608028654f * (g + 0.044715f * g * g * g);
        return rbf(rbf(0.5f * g * (1.f + tanhf(inner))) * u);
      };
      L.h[lane] = act(lo.x, lo.y); L.h[64 + lane] = act(hi.x, hi.y);
    }
    lds_done();
    if (lane == 0) lds_put(&L.ctl[3], tag);
    STAMP(5);
    {
#pragma unroll 1
      for (u32 spin = 0; !__all(lds_get(&L.kdone[lane & 3]) == tag) && spin < 64 * SPIN_CAP; spin++) __builtin_amdgcn_s_sleep(16);
      asm volatile("" ::: "memory");
      const float mine = L.mw[lane & 3];
      float mc = fmaxf(mine, dpp<XOR1>(mine)); mc = fmaxf(mc, dpp<XOR2>(mc));
      float s = 0.f;
      const float* yy = &L.y[0][0][lane];
#pragma unroll 8
      for (int t = 0; t < 256; t++) s += __builtin_amdgcn_exp2f(yy[t * 64] - mc);
      s = wave_sum(s) * 0.25f;
      gu64* ml = (gu64*)(ws + WS_ML);
      if (lane < 2) put(ml + b * 16 + lane, tag, lane == 0 ? mc : s);
      __builtin_amdgcn_s_sleep(127);
      u64 g0, g1;
      for (u32 spin = 0;; spin++) {
        g0 = get(ml + (head * 8 + (lane & 7)) * 16); g1 = get(ml + (head * 8 + (lane & 7)) * 16 + 1);
        if (__all(tag_ok(g0, tag) & tag_ok(g1, tag)) || spin > SPIN_CAP) break;
        __builtin_amdgcn_s_sleep(64);
      }
      float M = val(g0);
      M = fmaxf(M, dpp<XOR1>(M)); M = fmaxf(M, dpp<XOR2>(M)); M = fmaxf(M, dpp<HMIRROR>(M));
      float t = val(g1) * __builtin_amdgcn_exp2f(val(g0) - M);
      t += dpp<XOR1>(t); t += dpp<XOR2>(t); t += dpp<HMIRROR>(t);
      if (lane < 4) L.fw[lane] = __builtin_amdgcn_exp2f(mine - M) / t;
      lds_done();
      if (lane == 0) lds_put(&L.ctl[2], tag);
    }
    STAMP(9);
    wg_barrier();
    STAMP(6);
    {
      float o0 = L.op[0][lane], o1 = L.op[0][64 + lane];
#pragma unroll
      for (int k = 1; k < 4; k++) { o0 += L.op[k][lane]; o1 += L.op[k][64 + lane]; }
      gu64* s1 = (gu64*)(ws + WS_S1) + (size_t)b * 128;
      put(s1 + lane, tag, o0); put(s1 + 64 + lane, tag, o1);
    }
    STAMP(7);
    wg_barrier();
    STAMP(8);
    {
      float s0 = L.qp[0][lane], s1 = L.qp[0][64 + lane];
#pragma unroll
      for (int k = 1; k < 4; k++) { s0 += L.qp[k][lane]; s1 += L.qp[k][64 + lane]; }
      gu64* s2 = (gu64*)(ws + WS_S2);
      put(s2 + (size_t)b * 128 + lane, tag, s0); put(s2 + (size_t)b * 128 + 64 + lane, tag, s1);
      if (head & 3) return;
      const gu64* sr = s2 + (size_t)(r * 8 + (fin >> 3)) * 128 + (fin & 7) * 16 + c;
      u64 g[8];
      if (S2_DELAY) __builtin_amdgcn_s_sleep(S2_DELAY);
      for (u32 spin = 0;; spin++) {
        bool ok = true;
#pragma unroll
        for (int k = 0; k < 8; k++) g[k] = get(sr + k * 4 * 8 * 128);
#pragma unroll
        for (int k = 0; k < 8; k++) ok &= tag_ok(g[k], tag);
        if (__all(ok) || spin > SPIN_CAP) break;
        if (POLL_SLEEP) __builtin_amdgcn_s_sleep(POLL_SLEEP);
      }
      float a = ((val(g[0]) + val(g[1])) + (val(g[2]) + val(g[3]))) + ((val(g[4]) + val(g[5])) + (val(g[6]) + val(g[7])));
      a = xrow_sum(a) + bf_lo(xfin);
      if (lane < 16) ((GAS u16*)out)[16 * fin + lane] = bf_bits(a);
      if (b == 0 && lane == 0) put32((gu32*)(ws + WS_EPOCH), tag);
    }
    STAMP(10);
  }
}

}

extern "C" void kernel_launch(void* const* d_in, const int* in_sizes, int n_in, void* d_out, int out_size, void* d_ws, size_t ws_size,
                              hipStream_t stream) {
  (void)in_sizes; (void)n_in; (void)out_size; (void)ws_size;
  fused_decode<<<256, 320, 0, stream>>>((const u16*)d_in[0], (const u16*)d_in[1], (const u16*)d_in[2], (const u16*)d_in[3], (const u16*)d_in[4],
                                        (const u16*)d_in[6], (const u16*)d_in[7], (const u16*)d_in[8], (u16*)d_out, (unsigned char*)d_ws + WS_BASE);
}
```

```cpp
#include <hip/hip_runtime.h>
#include <stdint.h>

namespace {

typedef unsigned u32;
typedef unsigned long long u64;
typedef unsigned short u16;
typedef u32 u32x4 __attribute__((ext_vector_type(4)));
typedef int i32x4 __attribute__((ext_vector_type(4)));
typedef float f32x2 __attribute__((ext_vector_type(2)));
typedef float f32x4 __attribute__((ext_vector_type(4)));
typedef __attribute__((ext_vector_type(2))) __bf16 bf16x2;
#define GAS __attribute__((address_space(1)))
#define LAS __attribute__((address_space(3)))
typedef GAS u64 gu64;
typedef GAS u32 gu32;

constexpr int D = 1024, SK = 32768, DH = 128, FF = 3584;
constexpr u32 HEAD_BYTES = (u32)SK * DH * 2;

#define PACE_T0 560
#define PACE_D16 888
#define PACE_NA 32
#define PACE_D16A 950
#define PACE_D16V PACE_D16
#define WS_BASE 0
#define WD_DELAY 10
#define DMID 0
template <int N> struct Int { static constexpr int value = N; };
#define S1_DELAY 0
#define S2_DELAY 8
#define EARLY_POLL 1
#define WDOT 1
#define WARM0 1
#define WARM1 0
#define WARM_HEADS 4
#define QDELAY 8
#define FDELAY1 64
#define POLL_SLEEP 0

constexpr size_t WS_EPOCH = 0;
constexpr size_t WS_FLAG1 = 16384;
constexpr size_t WS_ML = 32768;
constexpr size_t WS_QREC = 65536;
constexpr size_t WS_S1 = WS_QREC + 256 * 80 * 8;
constexpr size_t WS_S2 = WS_S1 + 256 * 128 * 8;
constexpr size_t WS_GU = WS_S2 + 256 * 128 * 8;
constexpr size_t WS_STAMP = 2u << 20;
constexpr u32 SPIN_CAP = 1u << 16;

__device__ __forceinline__ float bf_lo(u32 v) { return __uint_as_float(v << 16); }
__device__ __forceinline__ float bf_hi(u32 v) { return __uint_as_float(v & 0xffff0000u); }
__device__ __forceinline__ u32 cvt_pk(float lo, float hi) {
  bf16x2 r = {(__bf16)lo, (__bf16)hi};
  return __builtin_bit_cast(u32, r);
}
__device__ __forceinline__ u16 bf_bits(float x) { return __builtin_bit_cast(u16, (__bf16)x); }
__device__ __forceinline__ float rbf(float x) { return bf_lo((u32)bf_bits(x)); }
__device__ __forceinline__ float dot2(u32 a, u32 b, float c) {
  return __builtin_amdgcn_fdot2_f32_bf16(__builtin_bit_cast(bf16x2, a), __builtin_bit_cast(bf16x2, b), c, false);
}
template <int CTRL> __device__ __forceinline__ float dpp(float x) {
  return __builtin_bit_cast(float, __builtin_amdgcn_mov_dpp(__builtin_bit_cast(int, x), CTRL, 0xf, 0xf, true));
}
constexpr int XOR1 = 0xB1, XOR2 = 0x4E, HMIRROR = 0x141, ROR8 = 0x128, ROR4 = 0x124, BCAST = 0x150;
__device__ __forceinline__ float swap32_add(float a, float b) {
  auto s = __builtin_amdgcn_permlane32_swap(__float_as_uint(a), __float_as_uint(b), false, false);
  return __uint_as_float(s[0]) + __uint_as_float(s[1]);
}
__device__ __forceinline__ float swap16_add(float a, float b) {
  auto s = __builtin_amdgcn_permlane16_swap(__float_as_uint(a), __float_as_uint(b), false, false);
  return __uint_as_float(s[0]) + __uint_as_float(s[1]);
}
__device__ __forceinline__ float xrow_sum(float x) { x = swap16_add(x, x); return swap32_add(x, x); }
__device__ __forceinline__ float xrow_max(float x) {
  auto s = __builtin_amdgcn_permlane16_swap(__float_as_uint(x), __float_as_uint(x), false, false);
  x = fmaxf(__uint_as_float(s[0]), __uint_as_float(s[1]));
  auto t = __builtin_amdgcn_permlane32_swap(__float_as_uint(x), __float_as_uint(x), false, false);
  return fmaxf(__uint_as_float(t[0]), __uint_as_float(t[1]));
}
__device__ __forceinline__ float row_sum(float x) { x += dpp<XOR1>(x); x += dpp<XOR2>(x); x += dpp<ROR4>(x); x += dpp<ROR8>(x); return x; }
__device__ __forceinline__ float wave_sum(float x) { return xrow_sum(row_sum(x)); }
__device__ __forceinline__ f32x2 fold_rows(const float (&a)[8]) {
  float s0 = swap32_add(a[0], a[4]), s1 = swap32_add(a[1], a[5]), s2 = swap32_add(a[2], a[6]), s3 = swap32_add(a[3], a[7]);
  return f32x2{swap16_add(s0, s2), swap16_add(s1, s3)};
}

__device__ __forceinline__ i32x4 mkr(const void* p, u32 n) {
  u64 a = (u64)p;
  i32x4 r; r.x = (int)(u32)a; r.y = (int)((u32)(a >> 32) & 0xffffu); r.z = (int)n; r.w = 0x00020000; return r;
}
__device__ __forceinline__ void ld_o(u32x4& d, u32 voff, i32x4 r, u32 soff) {
  asm volatile("buffer_load_dwordx4 %0, %1, %2, %3 offen nt" : "=v"(d) : "v"(voff), "s"(r), "s"(soff));
}
__device__ __forceinline__ void rows4_o(u32x4 (&R)[4], u32 voff, i32x4 r, u32 soff, u32 step) {
  ld_o(R[0], voff, r, soff); ld_o(R[1], voff, r, soff + step); ld_o(R[2], voff, r, soff + 2 * step); ld_o(R[3], voff, r, soff + 3 * step);
}
#define TILE_LOADS \
      "buffer_load_dwordx4 %0, %4, %5, %6 offen nt\n\t" \
      "buffer_load_dwordx4 %1, %4, %5, %6 offen offset:1024 nt\n\t" \
      "buffer_load_dwordx4 %2, %4, %5, %6 offen offset:2048 nt\n\t" \
      "buffer_load_dwordx4 %3, %4, %5, %6 offen offset:3072 nt"
__device__ __forceinline__ void tile_o(u32x4 (&R)[4], u32 voff, i32x4 r, u32 soff) {
  asm volatile(TILE_LOADS : "=&v"(R[0]), "=&v"(R[1]), "=&v"(R[2]), "=&v"(R[3]) : "v"(voff), "s"(r), "s"(soff));
}
__device__ __forceinline__ void tile(u32x4 (&R)[4], u32 voff, i32x4 r, u32 soff) {
  asm volatile(TILE_LOADS : "+v"(R[0]), "+v"(R[1]), "+v"(R[2]), "+v"(R[3]) : "v"(voff), "s"(r), "s"(soff));
}
template <int N> __device__ __forceinline__ void wait4(u32x4 (&R)[4]) {
  asm volatile("s_waitcnt vmcnt(%4)" : "+v"(R[0]), "+v"(R[1]), "+v"(R[2]), "+v"(R[3]) : "i"(N));
}
__device__ __forceinline__ void pin4(u32x4 (&R)[4]) { asm volatile("" : "+v"(R[0]), "+v"(R[1]), "+v"(R[2]), "+v"(R[3])); }
__device__ __forceinline__ void pin8(float (&a)[8]) {
  asm volatile("" : "+v"(a[0]), "+v"(a[1]), "+v"(a[2]), "+v"(a[3]), "+v"(a[4]), "+v"(a[5]), "+v"(a[6]), "+v"(a[7]));
}
__device__ __forceinline__ void wg_barrier() { asm volatile("s_waitcnt lgkmcnt(0)\n\ts_barrier" ::: "memory"); }
__device__ __forceinline__ void drain() { asm volatile("s_waitcnt vmcnt(0)" ::: "memory"); }
__device__ __forceinline__ void lds_done() { asm volatile("s_waitcnt lgkmcnt(0)" ::: "memory"); }
__device__ __forceinline__ u32 lds_get(const u32* p) { return __hip_atomic_load((const LAS u32*)p, __ATOMIC_RELAXED, __HIP_MEMORY_SCOPE_WORKGROUP); }
__device__ __forceinline__ void lds_put(u32* p, u32 v) { __hip_atomic_store((LAS u32*)p, v, __ATOMIC_RELAXED, __HIP_MEMORY_SCOPE_WORKGROUP); }

__device__ __forceinline__ void put(gu64* g, u32 tag, float v) {
  __hip_atomic_store(g, ((u64)tag << 32) | (u64)__float_as_uint(v), __ATOMIC_RELAXED, __HIP_MEMORY_SCOPE_AGENT);
}
__device__ __forceinline__ u64 get(const gu64* g) { return __hip_atomic_load(g, __ATOMIC_RELAXED, __HIP_MEMORY_SCOPE_AGENT); }
__device__ __forceinline__ void put32(gu32* g, u32 v) { __hip_atomic_store(g, v, __ATOMIC_RELAXED, __HIP_MEMORY_SCOPE_AGENT); }
__device__ __forceinline__ u32 get32(const gu32* g) { return __hip_atomic_load(g, __ATOMIC_RELAXED, __HIP_MEMORY_SCOPE_AGENT); }
__device__ __forceinline__ bool tag_ok(u64 x, u32 tag) { return (u32)(x >> 32) == tag; }
__device__ __forceinline__ float val(u64 x) { return __uint_as_float((u32)x); }

template <int LN> __device__ __forceinline__ void gemv_step(float (&acc)[8], float xv, u32x4 w) {
  const float xb = dpp<BCAST + LN>(xv);
#pragma unroll
  for (int k = 0; k < 4; k++) { acc[2 * k] = fmaf(xb, bf_lo(w[k]), acc[2 * k]); acc[2 * k + 1] = fmaf(xb, bf_hi(w[k]), acc[2 * k + 1]); }
}
template <int LN, int ST> __device__ __forceinline__ void gemv4(float (&acc)[8], float xv, u32x4 (&w)[4]) {
  gemv_step<LN>(acc, xv, w[0]); gemv_step<LN + ST>(acc, xv, w[1]); gemv_step<LN + 2 * ST>(acc, xv, w[2]); gemv_step<LN + 3 * ST>(acc, xv, w[3]);
}

__device__ __forceinline__ void gemv8(float (&acc)[8], const float* xv, u32x4 (&w)[4]) {
#pragma unroll
  for (int i = 0; i < 4; i++)
#pragma unroll
    for (int k = 0; k < 4; k++) { acc[2 * k] = fmaf(xv[i], bf_lo(w[i][k]), acc[2 * k]); acc[2 * k + 1] = fmaf(xv[i], bf_hi(w[i][k]), acc[2 * k + 1]); }
}
__device__ __forceinline__ float fold8(const float (&a)[8], bool b8) {
  const f32x2 s = fold_rows(a);
  return (b8 ? s.y : s.x) + dpp<ROR8>(b8 ? s.x : s.y);
}

__device__ __forceinline__ void warm_code(int lane) {
  u64 pc; u32 back, n0, n1;
  asm volatile("s_getpc_b64 %0\n"
               "fd_here_%=:\n\t"
               "s_mov_b32 %1, fd_here_%= - fd_code_begin\n\t"
               "s_mov_b32 %2, fd_code_stream_end - fd_code_begin\n\t"
               "s_mov_b32 %3, fd_code_helper_end - fd_code_begin"
               : "=s"(pc), "=s"(back), "=s"(n0), "=s"(n1));
  pc -= back;
  const u32 n = max(n0, n1);
  u32 v[6];
#pragma unroll
  for (int k = 0; k < 6; k++) v[k] = *(const GAS u32*)(pc + (min(lane * 64u + k * 4096u, n - 4u) & ~3u));
  asm volatile("" :: "v"(v[0]), "v"(v[1]), "v"(v[2]), "v"(v[3]), "v"(v[4]), "v"(v[5]));
}

struct Lds {
  float y[4][64][64];
  float part[4][128];
  float qp[4][128];
  float op[4][128];
  float h[128];
  float mw[4];
  float fw[4];
  u32 kdone[4];
  u16 q[128];
  u32 ctl[4];
};

#define STAMP(i) do {} while (0)
#define PROG() do {} while (0)

__global__ void __launch_bounds__(320, 2)
fused_decode(const u16* __restrict__ x, const u16* __restrict__ kc, const u16* __restrict__ vc, const u16* __restrict__ wq,
             const u16* __restrict__ wo, const u16* __restrict__ wg, const u16* __restrict__ wu, const u16* __restrict__ wd,
             u16* __restrict__ out, unsigned char* __restrict__ ws) {
  __shared__ __attribute__((aligned(16))) Lds L;
  asm volatile("fd_code_begin:");
  const int b = blockIdx.x, head = b >> 3, j = b & 7;
  const int lane = threadIdx.x & 63, wave = __builtin_amdgcn_readfirstlane(threadIdx.x >> 6);
  const int r = lane >> 4, c = lane & 15;
  const bool has_ffn = j != (head & 7);
  const int ks = j >> 1, jb = j & 1, hr = head & 7;
  const int cg = 2 * head + jb - 2 * (head >> 3) - ((hr > 2 * ks || (hr == 2 * ks && jb)) ? 1 : 0) - (hr > 2 * ks + 1 ? 1 : 0);
  const int unit = cg * 4 + ks;
  const int dg = head - ((head - j + 7) >> 3);
  STAMP(0);

  if (wave < 4) {
    const int w = wave;
    u32x4 X0, X1, XS;
    u32x4 P0[4], P1[4], P2[4], P3[4], R0[4], R1[4], R2[4], R3[4];
    const i32x4 rx = mkr(x, D * 2);
    const u32 voff = lane * 16;
    const int r8 = lane >> 3, c8 = lane & 7;
    const u32 row0 = ks * 256 + 64 * w;
    {
      const u32 so = (row0 + 8 * r8) * 2;
      asm volatile("buffer_load_dwordx4 %0, %1, %2, 0 offen" : "=v"(X0) : "v"(voff), "s"(rx));
      asm volatile("buffer_load_dwordx4 %0, %1, %2, 0 offen offset:1024" : "=v"(X1) : "v"(voff), "s"(rx));
      asm volatile("buffer_load_dwordx4 %0, %1, %2, 0 offen" : "=v"(XS) : "v"(so), "s"(rx));
    }
    {
      const i32x4 rq = mkr(wq, (u32)D * 4096 * 2);
      const u32 vo = r8 * 8 * 8192 + c8 * 16, s0 = row0 * 8192 + head * 256 + jb * 128;
      rows4_o(P0, vo, rq, s0, 8192); rows4_o(P1, vo, rq, s0 + 4 * 8192, 8192);
    }
    const u32 nffn = has_ffn ? (u32)D * FF * 2 : 0u;
    const i32x4 rg = mkr(wg, nffn), ru = mkr(wu, nffn);
    constexpr u32 GSTEP = FF * 2;
    const u32 vg = r8 * 8 * (FF * 2) + c8 * 16, g0 = row0 * (FF * 2) + cg * 128;
    const i32x4 rk = mkr((const char*)kc + (size_t)head * HEAD_BYTES, HEAD_BYTES), rv = mkr((const char*)vc + (size_t)head * HEAD_BYTES, HEAD_BYTES);
    const u32 t0 = (u32)(j * 256 + w) * 4096;
    asm volatile("s_waitcnt vmcnt(%3)" : "+v"(X0), "+v"(X1), "+v"(XS) : "i"(8));
    float xn[8];
    {
      float v[16];
#pragma unroll
      for (int k = 0; k < 4; k++) { v[2 * k] = bf_lo(X0[k]); v[2 * k + 1] = bf_hi(X0[k]); v[8 + 2 * k] = bf_lo(X1[k]); v[9 + 2 * k] = bf_hi(X1[k]); }
      float s = 0.f;
#pragma unroll
      for (int k = 0; k < 16; k++) s += v[k];
      const float mean = wave_sum(s) * (1.0f / D);
      float q = 0.f;
#pragma unroll
      for (int k = 0; k < 16; k++) { const float d = v[k] - mean; q = fmaf(d, d, q); }
      const float var = wave_sum(q) * (1.0f / D);
      const float den = rbf(sqrtf(rbf(rbf(var) + 1e-5f)));
      const float mb = rbf(mean);
#pragma unroll
      for (int k = 0; k < 4; k++) { xn[2 * k] = rbf(rbf(bf_lo(XS[k]) - mb) / den); xn[2 * k + 1] = rbf(rbf(bf_hi(XS[k]) - mb) / den); }
    }
    pin8(xn);
    const bool b8 = (c & 8) != 0, b4 = (c & 4) != 0;
    STAMP(1);
    {
      float a[8] = {0.f, 0.f, 0.f, 0.f, 0.f, 0.f, 0.f, 0.f};
      wait4<4>(P0); rows4_o(P2, vg, rg, g0, GSTEP); pin4(P0);
      gemv8(a, xn, P0); pin8(a);
      wait4<4>(P1); rows4_o(P3, vg, rg, g0 + 4 * GSTEP, GSTEP); pin4(P1);
      gemv8(a, xn + 4, P1);
      L.qp[w][8 * c8 + r8] = fold8(a, b8);
    }
    wg_barrier();
    STAMP(2);
    {
      float a[8] = {0.f, 0.f, 0.f, 0.f, 0.f, 0.f, 0.f, 0.f};
      wait4<4>(P2); rows4_o(P0, vg, ru, g0, GSTEP); pin4(P2);
      gemv8(a, xn, P2); pin8(a);
      wait4<4>(P3); rows4_o(P1, vg, ru, g0 + 4 * GSTEP, GSTEP); pin4(P3);
      gemv8(a, xn + 4, P3);
      L.part[w][8 * c8 + r8] = fold8(a, b8);
    }
    {
      float a[8] = {0.f, 0.f, 0.f, 0.f, 0.f, 0.f, 0.f, 0.f};
      wait4<4>(P0); tile_o(R0, voff, rk, t0); pin4(P0);
      gemv8(a, xn, P0); pin8(a);
      wait4<4>(P1); tile_o(R1, voff, rk, t0 + 16384); pin4(P1);
      gemv8(a, xn + 4, P1);
      L.part[w][64 + 8 * c8 + r8] = fold8(a, b8);
    }
    wait4<4>(R0); tile_o(R2, voff, rk, t0 + 2 * 16384);
    wait4<4>(R1); tile_o(R3, voff, rk, t0 + 3 * 16384);
    STAMP(3);
    wg_barrier();
    STAMP(4);
    const u32x4 q = *(const u32x4*)&L.q[8 * c];
    const u32 tag = __builtin_amdgcn_readfirstlane(L.ctl[0]);
    const u32 tref = __builtin_amdgcn_readfirstlane(L.ctl[1]);

    float m = -INFINITY, acc[8] = {0.f, 0.f, 0.f, 0.f, 0.f, 0.f, 0.f, 0.f};
    u32 due16 = (tref << 4) + PACE_T0 * 16 + w * (PACE_D16 / 4);
    auto clock16 = [&]() -> u32 { return (u32)__builtin_amdgcn_s_memrealtime() << 4; };
    auto pace = [&](u32 early, u32 step) {
      if ((int)(early - due16) < 0) {
#pragma unroll 1
        while ((int)(clock16() - due16) < 0) __builtin_amdgcn_s_sleep(1);
      }
      due16 += step;
    };
    auto kstep = [&](u32x4 (&R)[4], int n) {
      i32x4 rr = rk;
      rr.x = n + 4 < 64 ? rk.x : rv.x; rr.y = n + 4 < 64 ? rk.y : rv.y;
      u32 soff = t0 + (u32)((n + 4) & 63) * 16384;
      u32 early = clock16();
      asm volatile("" : "+s"(rr), "+s"(soff), "+s"(early));
      wait4<12>(R);
      float d[4];
#pragma unroll
      for (int u = 0; u < 4; u++) d[u] = dot2(R[u][3], q[3], dot2(R[u][2], q[2], dot2(R[u][1], q[1], dot2(R[u][0], q[0], 0.f))));
      const float e0 = (b8 ? d[2] : d[0]) + dpp<ROR8>(b8 ? d[0] : d[2]);
      const float e1 = (b8 ? d[3] : d[1]) + dpp<ROR8>(b8 ? d[1] : d[3]);
      float f = (b4 ? e1 : e0) + dpp<HMIRROR>(b4 ? e0 : e1);
      f += dpp<XOR1>(f); f += dpp<XOR2>(f);
      const float y = rbf(rbf(f) * 0.08838834764831845f) * 1.4426950408889634f;
      L.y[w][n][lane] = y;
      m = fmaxf(m, y);
      asm volatile("" : "+v"(m));
      pace(early, n < PACE_NA ? PACE_D16A : PACE_D16);
      tile(R, voff, rr, soff);
      PROG();
    };
    auto vload = [&](int n) -> float { return __builtin_amdgcn_exp2f(L.y[w][n][lane] - m); };
    auto vstep = [&](auto younger, u32x4 (&R)[4], int n) {
      u32 soff = t0 + (u32)(n + 4) * 16384;
      u32 early = clock16();
      float p = vload(n);
      asm volatile("" : "+s"(soff), "+v"(p), "+s"(early));
      wait4<decltype(younger)::value>(R);
      gemv4<0, 4>(acc, p, R);
      pin8(acc);
      pace(early, PACE_D16V);
      tile(R, voff, rv, soff);
      PROG();
    };
#pragma unroll 1
    for (int n = 0; n < 64; n += 4) { kstep(R0, n); kstep(R1, n + 1); kstep(R2, n + 2); kstep(R3, n + 3); }
    m = fmaxf(m, dpp<ROR4>(m)); m = fmaxf(m, dpp<ROR8>(m)); m = xrow_max(m);
    if (lane == 0) L.mw[w] = m;
    lds_done();
    if (lane == 0) lds_put(&L.kdone[w], tag);
    const u32 wvo = (16 * (r & 1) + 8 * (r >> 1)) * 2048 + c * 16;
    const i32x4 rd = mkr(wd, has_ffn ? (u32)FF * D * 2 : 0u);
    const u32 dso = (u32)(dg * 128 + 32 * w) * 2048 + j * 256;
    u32x4 W0[4], W1[4], D0[4], D1[4];
    float a[8] = {0.f, 0.f, 0.f, 0.f, 0.f, 0.f, 0.f, 0.f};
#pragma unroll 1
    for (int n = DMID ? 4 : 0; n < 60; n += 4) { vstep(Int<12>{}, R0, n); vstep(Int<12>{}, R1, n + 1); vstep(Int<12>{}, R2, n + 2); vstep(Int<12>{}, R3, n + 3); }
    {
      const float p0 = vload(60), p1 = vload(61), p2 = vload(62), p3 = vload(63);
#pragma unroll 1
      for (u32 spin = 0; lds_get(&L.ctl[2]) != tag && spin < 64 * SPIN_CAP; spin++) __builtin_amdgcn_s_sleep(2);
      asm volatile("" ::: "memory");
      const float fs = L.fw[w];
      wait4<12>(R0); gemv4<0, 4>(acc, p0, R0);
      wait4<8>(R1); gemv4<0, 4>(acc, p1, R1);
      pin8(acc);
      const i32x4 rwo = mkr(wo, (u32)4096 * D * 2);
      const u32 wso = (u32)(head * 128 + 32 * w) * 2048 + j * 256;
      due16 += WD_DELAY * 16 - w * (PACE_D16 / 4);
      pace(due16 - 1, 0);
      rows4_o(D0, wvo, rd, dso, 2048); rows4_o(D1, wvo, rd, dso + 8192, 2048);
      rows4_o(W0, wvo, rwo, wso, 2048); rows4_o(W1, wvo, rwo, wso + 8192, 2048);
      STAMP(13);
      wait4<DMID ? 12 : 20>(R2); gemv4<0, 4>(acc, p2, R2);
      wait4<DMID ? 8 : 16>(R3); gemv4<0, 4>(acc, p3, R3);
      *(f32x2*)&L.op[w][8 * c + 2 * r] = fold_rows(acc) * fs;
    }
    STAMP(5);
    wg_barrier();
    STAMP(6);
    {
      const float hv = has_ffn ? L.h[32 * w + 16 * (r & 1) + 8 * (r >> 1) + (c & 7)] : 0.f;
      wait4<12>(D0); gemv4<0, 1>(a, hv, D0);
      wait4<8>(D1); STAMP(11); gemv4<4, 1>(a, hv, D1);
      pin8(a);
      const gu64* s1 = (const gu64*)(ws + WS_S1) + (size_t)(head * 8 + (lane >> 5)) * 128 + 32 * w + (lane & 31);
      u64 g[4];
      if (S1_DELAY) __builtin_amdgcn_s_sleep(S1_DELAY);
      asm volatile("global_load_dwordx2 %0, %4, off sc1\n\tglobal_load_dwordx2 %1, %4, off offset:2048 sc1\n\t"
                   "global_load_dwordx2 %2, %5, off sc1\n\tglobal_load_dwordx2 %3, %5, off offset:2048 sc1"
                   : "=&v"(g[0]), "=&v"(g[1]), "=&v"(g[2]), "=&v"(g[3]) : "v"(s1), "v"(s1 + 512));
      wait4<4>(W0); wait4<4>(W1);
      asm volatile("s_waitcnt vmcnt(0)" : "+v"(g[0]), "+v"(g[1]), "+v"(g[2]), "+v"(g[3]));
      for (u32 spin = 0; !__all(tag_ok(g[0], tag) & tag_ok(g[1], tag) & tag_ok(g[2], tag) & tag_ok(g[3], tag)) && spin < SPIN_CAP; spin++) {
#pragma unroll
        for (int k = 0; k < 4; k++) g[k] = get(s1 + k * 256);
      }
      STAMP(7);
      float o = (val(g[0]) + val(g[1])) + (val(g[2]) + val(g[3]));
      o = rbf(swap32_add(o, o));
      const float orot = dpp<ROR8>(o);
      o = (r & 2) ? orot : o;
      const u32 ohi = __float_as_uint(o), olo = ohi >> 16;
#define WSTEP(n, W) { const u32 xl = (u32)__builtin_amdgcn_mov_dpp((int)olo, BCAST + n, 0xf, 0xf, true), xh = (u32)__builtin_amdgcn_mov_dpp((int)ohi, BCAST + n, 0xf, 0xf, true); \
        _Pragma("unroll") for (int k = 0; k < 4; k++) { a[2 * k] = dot2(xl, W[k], a[2 * k]); a[2 * k + 1] = dot2(xh, W[k], a[2 * k + 1]); } }
      WSTEP(0, W0[0]) WSTEP(1, W0[1]) WSTEP(2, W0[2]) WSTEP(3, W0[3]) WSTEP(4, W1[0]) WSTEP(5, W1[1]) WSTEP(6, W1[2]) WSTEP(7, W1[3])
#undef WSTEP
      *(f32x2*)&L.qp[w][8 * c + 2 * r] = fold_rows(a);
    }
    wg_barrier();
    asm volatile("fd_code_stream_end:");
    STAMP(8);
  } else {
    const u32 tstart = (u32)__builtin_amdgcn_s_memrealtime();
    const u32 ep = get32((gu32*)(ws + WS_EPOCH));
    const int fin = (head >> 2) * 8 + j;
    const u32 xfin = ((const GAS u16*)x)[16 * fin + c];
    if (WARM0 && head < WARM_HEADS) warm_code(lane);
    const u32 tag = __builtin_amdgcn_readfirstlane(ep) + 1u;
    gu64* qrec = (gu64*)(ws + WS_QREC);
    if (lane == 0) put(qrec + (size_t)b * 80 + 64, tag, __uint_as_float(tstart));
    if (lane < 4) L.kdone[lane] = ~tag;
    if (lane == 0) { L.ctl[2] = ~tag; L.ctl[3] = ~tag; }
    STAMP(1);
    wg_barrier();
    {
      put(qrec + (size_t)b * 80 + lane, tag, (L.qp[0][lane] + L.qp[1][lane]) + (L.qp[2][lane] + L.qp[3][lane]));
      STAMP(2);
      const gu64* qr = qrec + (size_t)head * 8 * 80;
      __builtin_amdgcn_s_sleep(QDELAY);
      u64 g[9];
      for (u32 spin = 0;; spin++) {
#pragma unroll
        for (int k = 0; k < 8; k++) g[k] = get(qr + k * 80 + lane);
        g[8] = get(qr + (lane & 7) * 80 + 64);
        bool ok = true;
#pragma unroll
        for (int k = 0; k < 9; k++) ok &= tag_ok(g[k], tag);
        if (__all(ok) || spin > SPIN_CAP) break;
      }
      const float q0 = (val(g[0]) + val(g[2])) + (val(g[4]) + val(g[6])), q1 = (val(g[1]) + val(g[3])) + (val(g[5]) + val(g[7]));
      L.q[lane] = bf_bits(q0); L.q[64 + lane] = bf_bits(q1);
      int tm = (int)((u32)g[8] - tstart);
      tm = min(tm, __builtin_amdgcn_mov_dpp(tm, XOR1, 0xf, 0xf, true)); tm = min(tm, __builtin_amdgcn_mov_dpp(tm, XOR2, 0xf, 0xf, true));
      tm = min(tm, __builtin_amdgcn_mov_dpp(tm, HMIRROR, 0xf, 0xf, true));
      if (lane == 0) { L.ctl[0] = tag; L.ctl[1] = tstart + (u32)min(tm, 0); }
    }
    STAMP(3);
    wg_barrier();
    STAMP(4);
    if (has_ffn) {
      gu64* gu = (gu64*)(ws + WS_GU);
      const f32x2 mine = {(L.part[0][lane] + L.part[1][lane]) + (L.part[2][lane] + L.part[3][lane]),
                          (L.part[0][64 + lane] + L.part[1][64 + lane]) + (L.part[2][64 + lane] + L.part[3][64 + lane])};
      __hip_atomic_store(gu + unit * 64 + lane, __builtin_bit_cast(u64, mine), __ATOMIC_RELAXED, __HIP_MEMORY_SCOPE_AGENT);
      drain();
      if (lane == 0) put32((gu32*)(ws + WS_FLAG1 + unit * 64), tag);
      const gu32* f1 = (const gu32*)(ws + WS_FLAG1 + (dg * 8 + (lane & 7)) * 64);
      __builtin_amdgcn_s_sleep(FDELAY1);
      for (u32 spin = 0; !__all(get32(f1) == tag) && spin < SPIN_CAP; spin++) __builtin_amdgcn_s_sleep(32);
      __builtin_amdgcn_fence(__ATOMIC_ACQUIRE, "wavefront");
      u64 pl[8];
#pragma unroll
      for (int k = 0; k < 8; k++) pl[k] = get(gu + (dg * 8 + k) * 64 + lane);
      asm volatile("" : "+v"(pl[0]), "+v"(pl[1]), "+v"(pl[2]), "+v"(pl[3]), "+v"(pl[4]), "+v"(pl[5]), "+v"(pl[6]), "+v"(pl[7]));
      auto pair = [&](int k) { return __builtin_bit_cast(f32x2, pl[k]); };
      const f32x2 lo = (pair(0) + pair(1)) + (pair(2) + pair(3)), hi = (pair(4) + pair(5)) + (pair(6) + pair(7));
      auto act = [](float g, float u) {
        g = rbf(g); u = rbf(u);
        const float inner = 0.7978845608028654f * (g + 0.044715f * g * g * g);
        return rbf(rbf(0.5f * g * (1.f + tanhf(inner))) * u);
      };
      L.h[lane] = act(lo.x, lo.y); L.h[64 + lane] = act(hi.x, hi.y);
    }
    lds_done();
    if (lane == 0) lds_put(&L.ctl[3], tag);
    STAMP(5);
    {
#pragma unroll 1
      for (u32 spin = 0; !__all(lds_get(&L.kdone[lane & 3]) == tag) && spin < 64 * SPIN_CAP; spin++) __builtin_amdgcn_s_sleep(16);
      asm volatile("" ::: "memory");
      const float mine = L.mw[lane & 3];
      float mc = fmaxf(mine, dpp<XOR1>(mine)); mc = fmaxf(mc, dpp<XOR2>(mc));
      float s = 0.f;
      const float* yy = &L.y[0][0][lane];
#pragma unroll 8
      for (int t = 0; t < 256; t++) s += __builtin_amdgcn_exp2f(yy[t * 64] - mc);
      s = wave_sum(s) * 0.25f;
      gu64* ml = (gu64*)(ws + WS_ML);
      if (lane < 2) put(ml + b * 16 + lane, tag, lane == 0 ? mc : s);
      __builtin_amdgcn_s_sleep(127);
      u64 g0, g1;
      for (u32 spin = 0;; spin++) {
        g0 = get(ml + (head * 8 + (lane & 7)) * 16); g1 = get(ml + (head * 8 + (lane & 7)) * 16 + 1);
        if (__all(tag_ok(g0, tag) & tag_ok(g1, tag)) || spin > SPIN_CAP) break;
        __builtin_amdgcn_s_sleep(64);
      }
      float M = val(g0);
      M = fmaxf(M, dpp<XOR1>(M)); M = fmaxf(M, dpp<XOR2>(M)); M = fmaxf(M, dpp<HMIRROR>(M));
      float t = val(g1) * __builtin_amdgcn_exp2f(val(g0) - M);
      t += dpp<XOR1>(t); t += dpp<XOR2>(t); t += dpp<HMIRROR>(t);
      if (lane < 4) L.fw[lane] = __builtin_amdgcn_exp2f(mine - M) / t;
      lds_done();
      if (lane == 0) lds_put(&L.ctl[2], tag);
    }
    if (WARM1 && head < WARM_HEADS) {
#pragma unroll 1
      while ((int)((u32)__builtin_amdgcn_s_memrealtime() - tstart) < WARM1) __builtin_amdgcn_s_sleep(32);
      warm_code(lane);
    }
    STAMP(9);
    wg_barrier();
    STAMP(6);
    {
      float o0 = L.op[0][lane], o1 = L.op[0][64 + lane];
#pragma unroll
      for (int k = 1; k < 4; k++) { o0 += L.op[k][lane]; o1 += L.op[k][64 + lane]; }
      gu64* s1 = (gu64*)(ws + WS_S1) + (size_t)b * 128;
      put(s1 + lane, tag, o0); put(s1 + 64 + lane, tag, o1);
    }
    STAMP(7);
    wg_barrier();
    STAMP(8);
    {
      float s0 = L.qp[0][lane], s1 = L.qp[0][64 + lane];
#pragma unroll
      for (int k = 1; k < 4; k++) { s0 += L.qp[k][lane]; s1 += L.qp[k][64 + lane]; }
      gu64* s2 = (gu64*)(ws + WS_S2);
      put(s2 + (size_t)b * 128 + lane, tag, s0); put(s2 + (size_t)b * 128 + 64 + lane, tag, s1);
      STAMP(11);
      if (head & 3) return;
      const gu64* sr = s2 + (size_t)(r * 8 + (fin >> 3)) * 128 + (fin & 7) * 16 + c;
      u64 g[8];
      if (S2_DELAY) __builtin_amdgcn_s_sleep(S2_DELAY);
      for (u32 spin = 0;; spin++) {
        bool ok = true;
#pragma unroll
        for (int k = 0; k < 8; k++) g[k] = get(sr + k * 4 * 8 * 128);
#pragma unroll
        for (int k = 0; k < 8; k++) ok &= tag_ok(g[k], tag);
        if (__all(ok) || spin > SPIN_CAP) break;
        if (POLL_SLEEP) __builtin_amdgcn_s_sleep(POLL_SLEEP);
      }
      float a = ((val(g[0]) + val(g[1])) + (val(g[2]) + val(g[3]))) + ((val(g[4]) + val(g[5])) + (val(g[6]) + val(g[7])));
      a = xrow_sum(a) + bf_lo(xfin);
      if (lane < 16) ((GAS u16*)out)[16 * fin + lane] = bf_bits(a);
      if (b == 0 && lane == 0) put32((gu32*)(ws + WS_EPOCH), tag);
    }
    STAMP(10);
    asm volatile("fd_code_helper_end:");
  }
}

}

extern "C" void kernel_launch(void* const* d_in, const int* in_sizes, int n_in, void* d_out, int out_size, void* d_ws, size_t ws_size,
                              hipStream_t stream) {
  (void)in_sizes; (void)n_in; (void)out_size; (void)ws_size;
  fused_decode<<<256, 320, 0, stream>>>((const u16*)d_in[0], (const u16*)d_in[1], (const u16*)d_in[2], (const u16*)d_in[3], (const u16*)d_in[4],
                                        (const u16*)d_in[6], (const u16*)d_in[7], (const u16*)d_in[8], (u16*)d_out, (unsigned char*)d_ws + WS_BASE);
}
```

```cpp
#include <hip/hip_runtime.h>
#include <stdint.h>

namespace {

typedef unsigned u32;
typedef unsigned long long u64;
typedef unsigned short u16;
typedef u32 u32x4 __attribute__((ext_vector_type(4)));
typedef int i32x4 __attribute__((ext_vector_type(4)));
typedef float f32x2 __attribute__((ext_vector_type(2)));
typedef __attribute__((ext_vector_type(2))) __bf16 bf16x2;
#define GAS __attribute__((address_space(1)))
#define LAS __attribute__((address_space(3)))
typedef GAS u64 gu64;
typedef GAS u32 gu32;

constexpr int D = 1024, SK = 32768, DH = 128, FF = 3584;
constexpr u32 HEAD_BYTES = (u32)SK * DH * 2;

constexpr u32 PACE_T0 = 560;
constexpr u32 PACE_SLOW = 950, PACE_STEP = 888;
constexpr int PACE_NSLOW = 32;
constexpr u32 PACE_SLACK = 60 * 16;

constexpr size_t WS_EPOCH = 0;
constexpr size_t WS_FLAG = 16384;
constexpr size_t WS_ML = 32768;
constexpr size_t WS_QREC = 65536;
constexpr size_t WS_S1 = WS_QREC + 256 * 80 * 8;
constexpr size_t WS_S2 = WS_S1 + 256 * 128 * 8;
constexpr size_t WS_GU = WS_S2 + 256 * 128 * 8;
constexpr u32 SPIN_CAP = 1u << 16;

__device__ __forceinline__ float bf_lo(u32 v) { return __uint_as_float(v << 16); }
__device__ __forceinline__ float bf_hi(u32 v) { return __uint_as_float(v & 0xffff0000u); }
__device__ __forceinline__ u16 bf_bits(float x) { return __builtin_bit_cast(u16, (__bf16)x); }
__device__ __forceinline__ float rbf(float x) { return bf_lo((u32)bf_bits(x)); }
__device__ __forceinline__ float dot2(u32 a, u32 b, float c) {
  return __builtin_amdgcn_fdot2_f32_bf16(__builtin_bit_cast(bf16x2, a), __builtin_bit_cast(bf16x2, b), c, false);
}
template <int CTRL> __device__ __forceinline__ int dppi(int x) { return __builtin_amdgcn_mov_dpp(x, CTRL, 0xf, 0xf, true); }
template <int CTRL> __device__ __forceinline__ float dpp(float x) { return __int_as_float(dppi<CTRL>(__float_as_int(x))); }
constexpr int XOR1 = 0xB1, XOR2 = 0x4E, HMIRROR = 0x141, ROR8 = 0x128, ROR4 = 0x124, BCAST = 0x150;
__device__ __forceinline__ float swap32_add(float a, float b) {
  auto s = __builtin_amdgcn_permlane32_swap(__float_as_uint(a), __float_as_uint(b), false, false);
  return __uint_as_float(s[0]) + __uint_as_float(s[1]);
}
__device__ __forceinline__ float swap16_add(float a, float b) {
  auto s = __builtin_amdgcn_permlane16_swap(__float_as_uint(a), __float_as_uint(b), false, false);
  return __uint_as_float(s[0]) + __uint_as_float(s[1]);
}
__device__ __forceinline__ float xrow_sum(float x) { x = swap16_add(x, x); return swap32_add(x, x); }
__device__ __forceinline__ float xrow_max(float x) {
  auto s = __builtin_amdgcn_permlane16_swap(__float_as_uint(x), __float_as_uint(x), false, false);
  x = fmaxf(__uint_as_float(s[0]), __uint_as_float(s[1]));
  auto t = __builtin_amdgcn_permlane32_swap(__float_as_uint(x), __float_as_uint(x), false, false);
  return fmaxf(__uint_as_float(t[0]), __uint_as_float(t[1]));
}
__device__ __forceinline__ float wave_sum(float x) {
  x += dpp<XOR1>(x); x += dpp<XOR2>(x); x += dpp<ROR4>(x); x += dpp<ROR8>(x);
  return xrow_sum(x);
}
__device__ __forceinline__ f32x2 fold_rows(const float (&a)[8]) {
  float s0 = swap32_add(a[0], a[4]), s1 = swap32_add(a[1], a[5]), s2 = swap32_add(a[2], a[6]), s3 = swap32_add(a[3], a[7]);
  return f32x2{swap16_add(s0, s2), swap16_add(s1, s3)};
}
__device__ __forceinline__ float fold8(const float (&a)[8], bool b8) {
  const f32x2 s = fold_rows(a);
  return (b8 ? s.y : s.x) + dpp<ROR8>(b8 ? s.x : s.y);
}

__device__ __forceinline__ i32x4 mkr(const void* p, u32 n) {
  u64 a = (u64)p;
  i32x4 r; r.x = (int)(u32)a; r.y = (int)((u32)(a >> 32) & 0xffffu); r.z = (int)n; r.w = 0x00020000; return r;
}
__device__ __forceinline__ void ld_o(u32x4& d, u32 voff, i32x4 r, u32 soff) {
  asm volatile("buffer_load_dwordx4 %0, %1, %2, %3 offen nt" : "=v"(d) : "v"(voff), "s"(r), "s"(soff));
}
__device__ __forceinline__ void rows4_o(u32x4 (&R)[4], u32 voff, i32x4 r, u32 soff, u32 step) {
  ld_o(R[0], voff, r, soff); ld_o(R[1], voff, r, soff + step); ld_o(R[2], voff, r, soff + 2 * step); ld_o(R[3], voff, r, soff + 3 * step);
}
#define TILE_LOADS \
      "buffer_load_dwordx4 %0, %4, %5, %6 offen nt\n\t" \
      "buffer_load_dwordx4 %1, %4, %5, %6 offen offset:1024 nt\n\t" \
      "buffer_load_dwordx4 %2, %4, %5, %6 offen offset:2048 nt\n\t" \
      "buffer_load_dwordx4 %3, %4, %5, %6 offen offset:3072 nt"
__device__ __forceinline__ void tile_o(u32x4 (&R)[4], u32 voff, i32x4 r, u32 soff) {
  asm volatile(TILE_LOADS : "=&v"(R[0]), "=&v"(R[1]), "=&v"(R[2]), "=&v"(R[3]) : "v"(voff), "s"(r), "s"(soff));
}
__device__ __forceinline__ void tile(u32x4 (&R)[4], u32 voff, i32x4 r, u32 soff) {
  asm volatile(TILE_LOADS : "+v"(R[0]), "+v"(R[1]), "+v"(R[2]), "+v"(R[3]) : "v"(voff), "s"(r), "s"(soff));
}
template <int N> __device__ __forceinline__ void wait4(u32x4 (&R)[4]) {
  asm volatile("s_waitcnt vmcnt(%4)" : "+v"(R[0]), "+v"(R[1]), "+v"(R[2]), "+v"(R[3]) : "i"(N));
}
__device__ __forceinline__ void pin4(u32x4 (&R)[4]) { asm volatile("" : "+v"(R[0]), "+v"(R[1]), "+v"(R[2]), "+v"(R[3])); }
__device__ __forceinline__ void pin8(float (&a)[8]) {
  asm volatile("" : "+v"(a[0]), "+v"(a[1]), "+v"(a[2]), "+v"(a[3]), "+v"(a[4]), "+v"(a[5]), "+v"(a[6]), "+v"(a[7]));
}
__device__ __forceinline__ void wg_barrier() { asm volatile("s_waitcnt lgkmcnt(0)\n\ts_barrier" ::: "memory"); }
__device__ __forceinline__ void drain() { asm volatile("s_waitcnt vmcnt(0)" ::: "memory"); }
__device__ __forceinline__ void lds_done() { asm volatile("s_waitcnt lgkmcnt(0)" ::: "memory"); }
__device__ __forceinline__ u32 lds_get(const u32* p) { return __hip_atomic_load((const LAS u32*)p, __ATOMIC_RELAXED, __HIP_MEMORY_SCOPE_WORKGROUP); }
__device__ __forceinline__ void lds_put(u32* p, u32 v) { __hip_atomic_store((LAS u32*)p, v, __ATOMIC_RELAXED, __HIP_MEMORY_SCOPE_WORKGROUP); }

__device__ __forceinline__ void put64(gu64* g, u64 v) { __hip_atomic_store(g, v, __ATOMIC_RELAXED, __HIP_MEMORY_SCOPE_AGENT); }
__device__ __forceinline__ void put(gu64* g, u32 tag, float v) { put64(g, ((u64)tag << 32) | (u64)__float_as_uint(v)); }
__device__ __forceinline__ u64 get(const gu64* g) { return __hip_atomic_load(g, __ATOMIC_RELAXED, __HIP_MEMORY_SCOPE_AGENT); }
__device__ __forceinline__ void put32(gu32* g, u32 v) { __hip_atomic_store(g, v, __ATOMIC_RELAXED, __HIP_MEMORY_SCOPE_AGENT); }
__device__ __forceinline__ u32 get32(const gu32* g) { return __hip_atomic_load(g, __ATOMIC_RELAXED, __HIP_MEMORY_SCOPE_AGENT); }
__device__ __forceinline__ bool tag_ok(u64 x, u32 tag) { return (u32)(x >> 32) == tag; }
__device__ __forceinline__ float val(u64 x) { return __uint_as_float((u32)x); }

template <int LN> __device__ __forceinline__ void gemv_step(float (&acc)[8], float xv, u32x4 w) {
  const float xb = dpp<BCAST + LN>(xv);
#pragma unroll
  for (int k = 0; k < 4; k++) { acc[2 * k] = fmaf(xb, bf_lo(w[k]), acc[2 * k]); acc[2 * k + 1] = fmaf(xb, bf_hi(w[k]), acc[2 * k + 1]); }
}
template <int LN, int ST> __device__ __forceinline__ void gemv4(float (&acc)[8], float xv, u32x4 (&w)[4]) {
  gemv_step<LN>(acc, xv, w[0]); gemv_step<LN + ST>(acc, xv, w[1]); gemv_step<LN + 2 * ST>(acc, xv, w[2]); gemv_step<LN + 3 * ST>(acc, xv, w[3]);
}
template <int LN> __device__ __forceinline__ void gemv4_bf(float (&acc)[8], float xv, u32x4 (&w)[4]) {
  const int hi = __float_as_int(xv), lo = (int)((u32)hi >> 16);
  auto one = [&](u32 xl, u32 xh, u32x4 v) {
#pragma unroll
    for (int k = 0; k < 4; k++) { acc[2 * k] = dot2(xl, v[k], acc[2 * k]); acc[2 * k + 1] = dot2(xh, v[k], acc[2 * k + 1]); }
  };
  one(dppi<BCAST + LN>(lo), dppi<BCAST + LN>(hi), w[0]); one(dppi<BCAST + LN + 1>(lo), dppi<BCAST + LN + 1>(hi), w[1]);
  one(dppi<BCAST + LN + 2>(lo), dppi<BCAST + LN + 2>(hi), w[2]); one(dppi<BCAST + LN + 3>(lo), dppi<BCAST + LN + 3>(hi), w[3]);
}
__device__ __forceinline__ void gemv8(float (&acc)[8], const float* xv, u32x4 (&w)[4]) {
#pragma unroll
  for (int i = 0; i < 4; i++)
#pragma unroll
    for (int k = 0; k < 4; k++) { acc[2 * k] = fmaf(xv[i], bf_lo(w[i][k]), acc[2 * k]); acc[2 * k + 1] = fmaf(xv[i], bf_hi(w[i][k]), acc[2 * k + 1]); }
}

__device__ __forceinline__ void warm_code(int lane) {
  u64 pc; u32 back, n0, n1;
  asm volatile("s_getpc_b64 %0\n"
               "fd_here:\n\t"
               "s_mov_b32 %1, fd_here - fd_code_begin\n\t"
               "s_mov_b32 %2, fd_code_stream_end - fd_code_begin\n\t"
               "s_mov_b32 %3, fd_code_helper_end - fd_code_begin"
               : "=s"(pc), "=s"(back), "=s"(n0), "=s"(n1));
  pc -= back;
  const u32 n = max(n0, n1);
  u32 v[5];
#pragma unroll
  for (int k = 0; k < 5; k++) v[k] = *(const GAS u32*)(pc + (min(lane * 64u + k * 4096u, n - 4u) & ~3u));
  asm volatile("" :: "v"(v[0]), "v"(v[1]), "v"(v[2]), "v"(v[3]), "v"(v[4]));
}

struct Lds {
  float y[4][64][64];
  float part[4][128];
  float qp[4][128];
  float op[4][128];
  float h[128];
  float mw[4];
  float fw[4];
  u32 kdone[4];
  u16 q[128];
  u32 ctl[4];
};

__global__ void __launch_bounds__(320, 2)
fused_decode(const u16* __restrict__ x, const u16* __restrict__ kc, const u16* __restrict__ vc, const u16* __restrict__ wq,
             const u16* __restrict__ wo, const u16* __restrict__ wg, const u16* __restrict__ wu, const u16* __restrict__ wd,
             u16* __restrict__ out, unsigned char* __restrict__ ws) {
  __shared__ __attribute__((aligned(16))) Lds L;
  asm volatile("fd_code_begin:");
  const int b = blockIdx.x, head = b >> 3, j = b & 7;
  const int lane = threadIdx.x & 63, wave = __builtin_amdgcn_readfirstlane(threadIdx.x >> 6);
  const int r = lane >> 4, c = lane & 15;
  const bool has_ffn = j != (head & 7);
  const int ks = j >> 1, jb = j & 1, hr = head & 7;
  const int cg = 2 * head + jb - 2 * (head >> 3) - ((hr > 2 * ks || (hr == 2 * ks && jb)) ? 1 : 0) - (hr > 2 * ks + 1 ? 1 : 0);
  const int dg = head - ((head - j + 7) >> 3);

  if (wave < 4) {
    const int w = wave;
    u32x4 X0, X1, XS;
    u32x4 P0[4], P1[4], P2[4], P3[4], R0[4], R1[4], R2[4], R3[4];
    const i32x4 rx = mkr(x, D * 2);
    const u32 voff = lane * 16;
    const int r8 = lane >> 3, c8 = lane & 7;
    const u32 row0 = ks * 256 + 64 * w;
    {
      const u32 so = (row0 + 8 * r8) * 2;
      asm volatile("buffer_load_dwordx4 %0, %1, %2, 0 offen" : "=v"(X0) : "v"(voff), "s"(rx));
      asm volatile("buffer_load_dwordx4 %0, %1, %2, 0 offen offset:1024" : "=v"(X1) : "v"(voff), "s"(rx));
      asm volatile("buffer_load_dwordx4 %0, %1, %2, 0 offen" : "=v"(XS) : "v"(so), "s"(rx));
    }
    {
      const i32x4 rq = mkr(wq, (u32)D * 4096 * 2);
      const u32 vo = r8 * 8 * 8192 + c8 * 16, s0 = row0 * 8192 + head * 256 + jb * 128;
      rows4_o(P0, vo, rq, s0, 8192); rows4_o(P1, vo, rq, s0 + 4 * 8192, 8192);
    }
    const u32 nffn = has_ffn ? (u32)D * FF * 2 : 0u;
    const i32x4 rg = mkr(wg, nffn), ru = mkr(wu, nffn);
    constexpr u32 GSTEP = FF * 2;
    const u32 vg = r8 * 8 * (FF * 2) + c8 * 16, g0 = row0 * (FF * 2) + cg * 128;
    const i32x4 rk = mkr((const char*)kc + (size_t)head * HEAD_BYTES, HEAD_BYTES), rv = mkr((const char*)vc + (size_t)head * HEAD_BYTES, HEAD_BYTES);
    const u32 t0 = (u32)(j * 256 + w) * 4096;
    asm volatile("s_waitcnt vmcnt(8)" : "+v"(X0), "+v"(X1), "+v"(XS));
    float xn[8];
    {
      float v[16];
#pragma unroll
      for (int k = 0; k < 4; k++) { v[2 * k] = bf_lo(X0[k]); v[2 * k + 1] = bf_hi(X0[k]); v[8 + 2 * k] = bf_lo(X1[k]); v[9 + 2 * k] = bf_hi(X1[k]); }
      float s = 0.f;
#pragma unroll
      for (int k = 0; k < 16; k++) s += v[k];
      const float mean = wave_sum(s) * (1.0f / D);
      float q = 0.f;
#pragma unroll
      for (int k = 0; k < 16; k++) { const float d = v[k] - mean; q = fmaf(d, d, q); }
      const float var = wave_sum(q) * (1.0f / D);
      const float den = rbf(sqrtf(rbf(rbf(var) + 1e-5f)));
      const float mb = rbf(mean);
#pragma unroll
      for (int k = 0; k < 4; k++) { xn[2 * k] = rbf(rbf(bf_lo(XS[k]) - mb) / den); xn[2 * k + 1] = rbf(rbf(bf_hi(XS[k]) - mb) / den); }
    }
    pin8(xn);
    const bool b8 = (c & 8) != 0, b4 = (c & 4) != 0;
    {
      float a[8] = {0.f, 0.f, 0.f, 0.f, 0.f, 0.f, 0.f, 0.f};
      wait4<4>(P0); rows4_o(P2, vg, rg, g0, GSTEP); pin4(P0);
      gemv8(a, xn, P0); pin8(a);
      wait4<4>(P1); rows4_o(P3, vg, rg, g0 + 4 * GSTEP, GSTEP); pin4(P1);
      gemv8(a, xn + 4, P1);
      L.qp[w][8 * c8 + r8] = fold8(a, b8);
    }
    wg_barrier();
    {
      float a[8] = {0.f, 0.f, 0.f, 0.f, 0.f, 0.f, 0.f, 0.f};
      wait4<4>(P2); rows4_o(P0, vg, ru, g0, GSTEP); pin4(P2);
      gemv8(a, xn, P2); pin8(a);
      wait4<4>(P3); rows4_o(P1, vg, ru, g0 + 4 * GSTEP, GSTEP); pin4(P3);
      gemv8(a, xn + 4, P3);
      L.part[w][8 * c8 + r8] = fold8(a, b8);
    }
    {
      float a[8] = {0.f, 0.f, 0.f, 0.f, 0.f, 0.f, 0.f, 0.f};
      wait4<4>(P0); tile_o(R0, voff, rk, t0); pin4(P0);
      gemv8(a, xn, P0); pin8(a);
      wait4<4>(P1); tile_o(R1, voff, rk, t0 + 16384); pin4(P1);
      gemv8(a, xn + 4, P1);
      L.part[w][64 + 8 * c8 + r8] = fold8(a, b8);
    }
    wait4<4>(R0); tile_o(R2, voff, rk, t0 + 2 * 16384);
    wait4<4>(R1); tile_o(R3, voff, rk, t0 + 3 * 16384);
    wg_barrier();
    const u32x4 q = *(const u32x4*)&L.q[8 * c];
    const u32 tag = __builtin_amdgcn_readfirstlane(L.ctl[0]);
    const u32 tref = __builtin_amdgcn_readfirstlane(L.ctl[1]);

    float m = -INFINITY, acc[8] = {0.f, 0.f, 0.f, 0.f, 0.f, 0.f, 0.f, 0.f};
    u32 due16 = (tref << 4) + PACE_T0 * 16 + w * (PACE_STEP / 4);
    auto clock16 = [&]() -> u32 { return (u32)__builtin_amdgcn_s_memrealtime() << 4; };
    auto pace = [&](u32 early, u32 step, u32 slack) {
      if ((int)(early - due16) < 0) {
#pragma unroll 1
        while ((int)(clock16() - due16) < 0) __builtin_amdgcn_s_sleep(1);
      } else if (early - due16 > slack) due16 = early - slack;
      due16 += step;
    };
    auto kstep = [&](u32x4 (&R)[4], int n) {
      i32x4 rr = rk;
      rr.x = n + 4 < 64 ? rk.x : rv.x; rr.y = n + 4 < 64 ? rk.y : rv.y;
      u32 soff = t0 + (u32)((n + 4) & 63) * 16384;
      u32 early = clock16();
      asm volatile("" : "+s"(rr), "+s"(soff), "+s"(early));
      wait4<12>(R);
      float d[4];
#pragma unroll
      for (int u = 0; u < 4; u++) d[u] = dot2(R[u][3], q[3], dot2(R[u][2], q[2], dot2(R[u][1], q[1], dot2(R[u][0], q[0], 0.f))));
      const float e0 = (b8 ? d[2] : d[0]) + dpp<ROR8>(b8 ? d[0] : d[2]);
      const float e1 = (b8 ? d[3] : d[1]) + dpp<ROR8>(b8 ? d[1] : d[3]);
      float f = (b4 ? e1 : e0) + dpp<HMIRROR>(b4 ? e0 : e1);
      f += dpp<XOR1>(f); f += dpp<XOR2>(f);
      const float y = rbf(rbf(f) * 0.08838834764831845f) * 1.4426950408889634f;
      L.y[w][n][lane] = y;
      m = fmaxf(m, y);
      asm volatile("" : "+v"(m));
      pace(early, n < PACE_NSLOW ? PACE_SLOW : PACE_STEP, n < PACE_NSLOW ? 0x7fffffffu : PACE_SLACK);
      tile(R, voff, rr, soff);
    };
    auto vload = [&](int n) -> float { return __builtin_amdgcn_exp2f(L.y[w][n][lane] - m); };
    auto vstep = [&](u32x4 (&R)[4], int n) {
      u32 soff = t0 + (u32)(n + 4) * 16384;
      u32 early = clock16();
      float p = vload(n);
      asm volatile("" : "+s"(soff), "+v"(p), "+s"(early));
      wait4<12>(R);
      gemv4<0, 4>(acc, p, R);
      pin8(acc);
      pace(early, PACE_STEP, PACE_SLACK);
      tile(R, voff, rv, soff);
    };
#pragma unroll 1
    for (int n = 0; n < 64; n += 4) { kstep(R0, n); kstep(R1, n + 1); kstep(R2, n + 2); kstep(R3, n + 3); }
    m = fmaxf(m, dpp<ROR4>(m)); m = fmaxf(m, dpp<ROR8>(m)); m = xrow_max(m);
    if (lane == 0) L.mw[w] = m;
    lds_done();
    if (lane == 0) lds_put(&L.kdone[w], tag);
#pragma unroll 1
    for (int n = 0; n < 60; n += 4) { vstep(R0, n); vstep(R1, n + 1); vstep(R2, n + 2); vstep(R3, n + 3); }
    u32x4 W0[4], W1[4], D0[4], D1[4];
    {
      const float p0 = vload(60), p1 = vload(61), p2 = vload(62), p3 = vload(63);
#pragma unroll 1
      for (u32 spin = 0; lds_get(&L.ctl[2]) != tag && spin < 64 * SPIN_CAP; spin++) __builtin_amdgcn_s_sleep(2);
      asm volatile("" ::: "memory");
      const float fs = L.fw[w];
      wait4<12>(R0); gemv4<0, 4>(acc, p0, R0);
      wait4<8>(R1); gemv4<0, 4>(acc, p1, R1);
      pin8(acc);
      const u32 wvo = (16 * (r & 1) + 8 * (r >> 1)) * 2048 + c * 16;
      const i32x4 rwo = mkr(wo, (u32)4096 * D * 2), rd = mkr(wd, has_ffn ? (u32)FF * D * 2 : 0u);
      const u32 wso = (u32)(head * 128 + 32 * w) * 2048 + j * 256, dso = (u32)(dg * 128 + 32 * w) * 2048 + j * 256;
      due16 += 160 - w * (PACE_STEP / 4);
      pace(due16 - 1, 0, 0);
      rows4_o(D0, wvo, rd, dso, 2048); rows4_o(D1, wvo, rd, dso + 8192, 2048);
      rows4_o(W0, wvo, rwo, wso, 2048); rows4_o(W1, wvo, rwo, wso + 8192, 2048);
      wait4<20>(R2); gemv4<0, 4>(acc, p2, R2);
      wait4<16>(R3); gemv4<0, 4>(acc, p3, R3);
      *(f32x2*)&L.op[w][8 * c + 2 * r] = fold_rows(acc) * fs;
    }
    wg_barrier();
    {
      const float hv = has_ffn ? L.h[32 * w + 16 * (r & 1) + 8 * (r >> 1) + (c & 7)] : 0.f;
      float a[8] = {0.f, 0.f, 0.f, 0.f, 0.f, 0.f, 0.f, 0.f};
      wait4<12>(D0); gemv4_bf<0>(a, hv, D0);
      wait4<8>(D1); gemv4_bf<4>(a, hv, D1);
      pin8(a);
      wait4<0>(W0); wait4<0>(W1);
      const gu64* s1 = (const gu64*)(ws + WS_S1) + (size_t)(head * 8 + (lane >> 5)) * 128 + 32 * w + (lane & 31);
      u64 g[4];
      for (u32 spin = 0;; spin++) {
#pragma unroll
        for (int k = 0; k < 4; k++) g[k] = get(s1 + k * 256);
        if (__all(tag_ok(g[0], tag) & tag_ok(g[1], tag) & tag_ok(g[2], tag) & tag_ok(g[3], tag)) || spin > SPIN_CAP) break;
      }
      float o = (val(g[0]) + val(g[1])) + (val(g[2]) + val(g[3]));
      o = rbf(swap32_add(o, o));
      const float orot = dpp<ROR8>(o);
      o = (r & 2) ? orot : o;
      gemv4_bf<0>(a, o, W0); gemv4_bf<4>(a, o, W1);
      *(f32x2*)&L.qp[w][8 * c + 2 * r] = fold_rows(a);
    }
    wg_barrier();
    asm volatile("fd_code_stream_end:");
  } else {
    const u32 tstart = (u32)__builtin_amdgcn_s_memrealtime();
    const u32 ep = get32((gu32*)(ws + WS_EPOCH));
    const int fin = (head >> 2) * 8 + j;
    const u32 xfin = ((const GAS u16*)x)[16 * fin + c];
    if (head < 4) warm_code(lane);
    const u32 tag = __builtin_amdgcn_readfirstlane(ep) + 1u;
    gu64* qrec = (gu64*)(ws + WS_QREC);
    if (lane == 0) put(qrec + (size_t)b * 80 + 64, tag, __uint_as_float(tstart));
    if (lane < 4) L.kdone[lane] = ~tag;
    if (lane == 0) L.ctl[2] = ~tag;
    wg_barrier();
    {
      put(qrec + (size_t)b * 80 + lane, tag, (L.qp[0][lane] + L.qp[1][lane]) + (L.qp[2][lane] + L.qp[3][lane]));
      const gu64* qr = qrec + (size_t)head * 8 * 80;
      __builtin_amdgcn_s_sleep(8);
      u64 g[9];
      for (u32 spin = 0;; spin++) {
#pragma unroll
        for (int k = 0; k < 8; k++) g[k] = get(qr + k * 80 + lane);
        g[8] = get(qr + (lane & 7) * 80 + 64);
        bool ok = true;
#pragma unroll
        for (int k = 0; k < 9; k++) ok &= tag_ok(g[k], tag);
        if (__all(ok) || spin > SPIN_CAP) break;
      }
      L.q[lane] = bf_bits((val(g[0]) + val(g[2])) + (val(g[4]) + val(g[6])));
      L.q[64 + lane] = bf_bits((val(g[1]) + val(g[3])) + (val(g[5]) + val(g[7])));
      int tm = (int)((u32)g[8] - tstart);
      tm = min(tm, dppi<XOR1>(tm)); tm = min(tm, dppi<XOR2>(tm)); tm = min(tm, dppi<HMIRROR>(tm));
      if (lane == 0) { L.ctl[0] = tag; L.ctl[1] = tstart + (u32)min(tm, 0); }
    }
    wg_barrier();
    if (has_ffn) {
      const int unit = cg * 4 + ks;
      gu64* gu = (gu64*)(ws + WS_GU);
      const f32x2 mine = {(L.part[0][lane] + L.part[1][lane]) + (L.part[2][lane] + L.part[3][lane]),
                          (L.part[0][64 + lane] + L.part[1][64 + lane]) + (L.part[2][64 + lane] + L.part[3][64 + lane])};
      put64(gu + unit * 64 + lane, __builtin_bit_cast(u64, mine));
      drain();
      if (lane == 0) put32((gu32*)(ws + WS_FLAG + unit * 64), tag);
      const gu32* fl = (const gu32*)(ws + WS_FLAG + (dg * 8 + (lane & 7)) * 64);
      __builtin_amdgcn_s_sleep(64);
      for (u32 spin = 0; !__all(get32(fl) == tag) && spin < SPIN_CAP; spin++) __builtin_amdgcn_s_sleep(32);
      __builtin_amdgcn_fence(__ATOMIC_ACQUIRE, "wavefront");
      u64 pl[8];
#pragma unroll
      for (int k = 0; k < 8; k++) pl[k] = get(gu + (dg * 8 + k) * 64 + lane);
      asm volatile("" : "+v"(pl[0]), "+v"(pl[1]), "+v"(pl[2]), "+v"(pl[3]), "+v"(pl[4]), "+v"(pl[5]), "+v"(pl[6]), "+v"(pl[7]));
      auto pair = [&](int k) { return __builtin_bit_cast(f32x2, pl[k]); };
      const f32x2 lo = (pair(0) + pair(1)) + (pair(2) + pair(3)), hi = (pair(4) + pair(5)) + (pair(6) + pair(7));
      auto act = [](float g, float u) {
        g = rbf(g); u = rbf(u);
        const float inner = 0.7978845608028654f * (g + 0.044715f * g * g * g);
        return rbf(rbf(0.5f * g * (1.f + tanhf(inner))) * u);
      };
      L.h[lane] = act(lo.x, lo.y); L.h[64 + lane] = act(hi.x, hi.y);
    }
    {
#pragma unroll 1
      for (u32 spin = 0; !__all(lds_get(&L.kdone[lane & 3]) == tag) && spin < 64 * SPIN_CAP; spin++) __builtin_amdgcn_s_sleep(16);
      asm volatile("" ::: "memory");
      const float mine = L.mw[lane & 3];
      float mc = fmaxf(mine, dpp<XOR1>(mine)); mc = fmaxf(mc, dpp<XOR2>(mc));
      float s = 0.f;
      const float* yy = &L.y[0][0][lane];
#pragma unroll 8
      for (int t = 0; t < 256; t++) s += __builtin_amdgcn_exp2f(yy[t * 64] - mc);
      s = wave_sum(s) * 0.25f;
      gu64* ml = (gu64*)(ws + WS_ML);
      if (lane < 2) put(ml + b * 16 + lane, tag, lane == 0 ? mc : s);
      __builtin_amdgcn_s_sleep(127);
      u64 g0, g1;
      for (u32 spin = 0;; spin++) {
        g0 = get(ml + (head * 8 + (lane & 7)) * 16); g1 = get(ml + (head * 8 + (lane & 7)) * 16 + 1);
        if (__all(tag_ok(g0, tag) & tag_ok(g1, tag)) || spin > SPIN_CAP) break;
        __builtin_amdgcn_s_sleep(64);
      }
      float M = val(g0);
      M = fmaxf(M, dpp<XOR1>(M)); M = fmaxf(M, dpp<XOR2>(M)); M = fmaxf(M, dpp<HMIRROR>(M));
      float t = val(g1) * __builtin_amdgcn_exp2f(val(g0) - M);
      t += dpp<XOR1>(t); t += dpp<XOR2>(t); t += dpp<HMIRROR>(t);
      if (lane < 4) L.fw[lane] = __builtin_amdgcn_exp2f(mine - M) / t;
      lds_done();
      if (lane == 0) lds_put(&L.ctl[2], tag);
    }
    wg_barrier();
    {
      gu64* s1 = (gu64*)(ws + WS_S1) + (size_t)b * 128;
      put(s1 + lane, tag, (L.op[0][lane] + L.op[1][lane]) + (L.op[2][lane] + L.op[3][lane]));
      put(s1 + 64 + lane, tag, (L.op[0][64 + lane] + L.op[1][64 + lane]) + (L.op[2][64 + lane] + L.op[3][64 + lane]));
    }
    wg_barrier();
    gu64* s2 = (gu64*)(ws + WS_S2);
    put(s2 + (size_t)b * 128 + lane, tag, (L.qp[0][lane] + L.qp[1][lane]) + (L.qp[2][lane] + L.qp[3][lane]));
    put(s2 + (size_t)b * 128 + 64 + lane, tag, (L.qp[0][64 + lane] + L.qp[1][64 + lane]) + (L.qp[2][64 + lane] + L.qp[3][64 + lane]));
    if ((head & 3) == 0) {
      const gu64* sr = s2 + (size_t)(r * 8 + (fin >> 3)) * 128 + (fin & 7) * 16 + c;
      u64 g[8];
      __builtin_amdgcn_s_sleep(8);
      for (u32 spin = 0;; spin++) {
        bool ok = true;
#pragma unroll
        for (int k = 0; k < 8; k++) g[k] = get(sr + k * 4 * 8 * 128);
#pragma unroll
        for (int k = 0; k < 8; k++) ok &= tag_ok(g[k], tag);
        if (__all(ok) || spin > SPIN_CAP) break;
      }
      float a = ((val(g[0]) + val(g[1])) + (val(g[2]) + val(g[3]))) + ((val(g[4]) + val(g[5])) + (val(g[6]) + val(g[7])));
      a = xrow_sum(a) + bf_lo(xfin);
      if (lane < 16) ((GAS u16*)out)[16 * fin + lane] = bf_bits(a);
      if (b == 0 && lane == 0) put32((gu32*)(ws + WS_EPOCH), tag);
    }
    asm volatile("fd_code_helper_end:");
  }
}

}

extern "C" void kernel_launch(void* const* d_in, const int* in_sizes, int n_in, void* d_out, int out_size, void* d_ws, size_t ws_size,
                              hipStream_t stream) {
  (void)in_sizes; (void)n_in; (void)out_size; (void)ws_size;
  fused_decode<<<256, 320, 0, stream>>>((const u16*)d_in[0], (const u16*)d_in[1], (const u16*)d_in[2], (const u16*)d_in[3], (const u16*)d_in[4],
                                        (const u16*)d_in[6], (const u16*)d_in[7], (const u16*)d_in[8], (u16*)d_out, (unsigned char*)d_ws);
}
```

```cpp
#include <hip/hip_runtime.h>
#include <stdint.h>

namespace {

typedef unsigned u32;
typedef unsigned long long u64;
typedef unsigned short u16;
typedef u32 u32x4 __attribute__((ext_vector_type(4)));
typedef int i32x4 __attribute__((ext_vector_type(4)));
typedef float f32x2 __attribute__((ext_vector_type(2)));
typedef __attribute__((ext_vector_type(2))) __bf16 bf16x2;
#define GAS __attribute__((address_space(1)))
#define LAS __attribute__((address_space(3)))
typedef GAS u64 gu64;
typedef GAS u32 gu32;

constexpr int D = 1024, SK = 32768, DH = 128, FF = 3584;
constexpr u32 HEAD_BYTES = (u32)SK * DH * 2;

constexpr u32 PACE_T0 = 560;
constexpr u32 PACE_SLOW = 950, PACE_STEP = 888;
constexpr int PACE_NSLOW = 32;
constexpr u32 PACE_SLACK = 60 * 16;

constexpr size_t WS_EPOCH = 0;
constexpr size_t WS_FLAG = 16384;
constexpr size_t WS_ML = 32768;
constexpr size_t WS_QREC = 65536;
constexpr size_t WS_S1 = WS_QREC + 256 * 80 * 8;
constexpr size_t WS_S2 = WS_S1 + 256 * 128 * 8;
constexpr size_t WS_GU = WS_S2 + 256 * 128 * 8;
constexpr u32 SPIN_CAP = 1u << 16;

__device__ __forceinline__ float bf_lo(u32 v) { return __uint_as_float(v << 16); }
__device__ __forceinline__ float bf_hi(u32 v) { return __uint_as_float(v & 0xffff0000u); }
__device__ __forceinline__ u16 bf_bits(float x) { return __builtin_bit_cast(u16, (__bf16)x); }
__device__ __forceinline__ float rbf(float x) { return bf_lo((u32)bf_bits(x)); }
__device__ __forceinline__ float dot2(u32 a, u32 b, float c) {
  return __builtin_amdgcn_fdot2_f32_bf16(__builtin_bit_cast(bf16x2, a), __builtin_bit_cast(bf16x2, b), c, false);
}
template <int CTRL> __device__ __forceinline__ int dppi(int x) { return __builtin_amdgcn_mov_dpp(x, CTRL, 0xf, 0xf, true); }
template <int CTRL> __device__ __forceinline__ float dpp(float x) { return __int_as_float(dppi<CTRL>(__float_as_int(x))); }
constexpr int XOR1 = 0xB1, XOR2 = 0x4E, HMIRROR = 0x141, ROR8 = 0x128, ROR4 = 0x124, BCAST = 0x150;
__device__ __forceinline__ float swap32_add(float a, float b) {
  auto s = __builtin_amdgcn_permlane32_swap(__float_as_uint(a), __float_as_uint(b), false, false);
  return __uint_as_float(s[0]) + __uint_as_float(s[1]);
}
__device__ __forceinline__ float swap16_add(float a, float b) {
  auto s = __builtin_amdgcn_permlane16_swap(__float_as_uint(a), __float_as_uint(b), false, false);
  return __uint_as_float(s[0]) + __uint_as_float(s[1]);
}
__device__ __forceinline__ float xrow_sum(float x) { x = swap16_add(x, x); return swap32_add(x, x); }
__device__ __forceinline__ float xrow_max(float x) {
  auto s = __builtin_amdgcn_permlane16_swap(__float_as_uint(x), __float_as_uint(x), false, false);
  x = fmaxf(__uint_as_float(s[0]), __uint_as_float(s[1]));
  auto t = __builtin_amdgcn_permlane32_swap(__float_as_uint(x), __float_as_uint(x), false, false);
  return fmaxf(__uint_as_float(t[0]), __uint_as_float(t[1]));
}
__device__ __forceinline__ float wave_sum(float x) {
  x += dpp<XOR1>(x); x += dpp<XOR2>(x); x += dpp<ROR4>(x); x += dpp<ROR8>(x);
  return xrow_sum(x);
}
__device__ __forceinline__ f32x2 fold_rows(const float (&a)[8]) {
  float s0 = swap32_add(a[0], a[4]), s1 = swap32_add(a[1], a[5]), s2 = swap32_add(a[2], a[6]), s3 = swap32_add(a[3], a[7]);
  return f32x2{swap16_add(s0, s2), swap16_add(s1, s3)};
}
__device__ __forceinline__ float fold8(const float (&a)[8], bool b8) {
  const f32x2 s = fold_rows(a);
  return (b8 ? s.y : s.x) + dpp<ROR8>(b8 ? s.x : s.y);
}

__device__ __forceinline__ i32x4 mkr(const void* p, u32 n) {
  u64 a = (u64)p;
  i32x4 r; r.x = (int)(u32)a; r.y = (int)((u32)(a >> 32) & 0xffffu); r.z = (int)n; r.w = 0x00020000; return r;
}
__device__ __forceinline__ void ld_o(u32x4& d, u32 voff, i32x4 r, u32 soff) {
  asm volatile("buffer_load_dwordx4 %0, %1, %2, %3 offen nt" : "=v"(d) : "v"(voff), "s"(r), "s"(soff));
}
__device__ __forceinline__ void rows4_o(u32x4 (&R)[4], u32 voff, i32x4 r, u32 soff, u32 step) {
  ld_o(R[0], voff, r, soff); ld_o(R[1], voff, r, soff + step); ld_o(R[2], voff, r, soff + 2 * step); ld_o(R[3], voff, r, soff + 3 * step);
}
#define TILE_LOADS \
      "buffer_load_dwordx4 %0, %4, %5, %6 offen nt\n\t" \
      "buffer_load_dwordx4 %1, %4, %5, %6 offen offset:1024 nt\n\t" \
      "buffer_load_dwordx4 %2, %4, %5, %6 offen offset:2048 nt\n\t" \
      "buffer_load_dwordx4 %3, %4, %5, %6 offen offset:3072 nt"
__device__ __forceinline__ void tile_o(u32x4 (&R)[4], u32 voff, i32x4 r, u32 soff) {
  asm volatile(TILE_LOADS : "=&v"(R[0]), "=&v"(R[1]), "=&v"(R[2]), "=&v"(R[3]) : "v"(voff), "s"(r), "s"(soff));
}
__device__ __forceinline__ void tile(u32x4 (&R)[4], u32 voff, i32x4 r, u32 soff) {
  asm volatile(TILE_LOADS : "+v"(R[0]), "+v"(R[1]), "+v"(R[2]), "+v"(R[3]) : "v"(voff), "s"(r), "s"(soff));
}
template <int N> __device__ __forceinline__ void wait4(u32x4 (&R)[4]) {
  asm volatile("s_waitcnt vmcnt(%4)" : "+v"(R[0]), "+v"(R[1]), "+v"(R[2]), "+v"(R[3]) : "i"(N));
}
__device__ __forceinline__ void pin4(u32x4 (&R)[4]) { asm volatile("" : "+v"(R[0]), "+v"(R[1]), "+v"(R[2]), "+v"(R[3])); }
__device__ __forceinline__ void pin8(float (&a)[8]) {
  asm volatile("" : "+v"(a[0]), "+v"(a[1]), "+v"(a[2]), "+v"(a[3]), "+v"(a[4]), "+v"(a[5]), "+v"(a[6]), "+v"(a[7]));
}
__device__ __forceinline__ void wg_barrier() { asm volatile("s_waitcnt lgkmcnt(0)\n\ts_barrier" ::: "memory"); }
__device__ __forceinline__ void drain() { asm volatile("s_waitcnt vmcnt(0)" ::: "memory"); }
__device__ __forceinline__ void lds_done() { asm volatile("s_waitcnt lgkmcnt(0)" ::: "memory"); }
__device__ __forceinline__ u32 lds_get(const u32* p) { return __hip_atomic_load((const LAS u32*)p, __ATOMIC_RELAXED, __HIP_MEMORY_SCOPE_WORKGROUP); }
__device__ __forceinline__ void lds_put(u32* p, u32 v) { __hip_atomic_store((LAS u32*)p, v, __ATOMIC_RELAXED, __HIP_MEMORY_SCOPE_WORKGROUP); }

__device__ __forceinline__ void put64(gu64* g, u64 v) { __hip_atomic_store(g, v, __ATOMIC_RELAXED, __HIP_MEMORY_SCOPE_AGENT); }
__device__ __forceinline__ void put(gu64* g, u32 tag, float v) { put64(g, ((u64)tag << 32) | (u64)__float_as_uint(v)); }
__device__ __forceinline__ u64 get(const gu64* g) { return __hip_atomic_load(g, __ATOMIC_RELAXED, __HIP_MEMORY_SCOPE_AGENT); }
__device__ __forceinline__ void put32(gu32* g, u32 v) { __hip_atomic_store(g, v, __ATOMIC_RELAXED, __HIP_MEMORY_SCOPE_AGENT); }
__device__ __forceinline__ u32 get32(const gu32* g) { return __hip_atomic_load(g, __ATOMIC_RELAXED, __HIP_MEMORY_SCOPE_AGENT); }
__device__ __forceinline__ bool tag_ok(u64 x, u32 tag) { return (u32)(x >> 32) == tag; }
__device__ __forceinline__ float val(u64 x) { return __uint_as_float((u32)x); }

template <int LN> __device__ __forceinline__ void gemv_step(float (&acc)[8], float xv, u32x4 w) {
  const float xb = dpp<BCAST + LN>(xv);
#pragma unroll
  for (int k = 0; k < 4; k++) { acc[2 * k] = fmaf(xb, bf_lo(w[k]), acc[2 * k]); acc[2 * k + 1] = fmaf(xb, bf_hi(w[k]), acc[2 * k + 1]); }
}
template <int LN, int ST> __device__ __forceinline__ void gemv4(float (&acc)[8], float xv, u32x4 (&w)[4]) {
  gemv_step<LN>(acc, xv, w[0]); gemv_step<LN + ST>(acc, xv, w[1]); gemv_step<LN + 2 * ST>(acc, xv, w[2]); gemv_step<LN + 3 * ST>(acc, xv, w[3]);
}
template <int LN> __device__ __forceinline__ void gemv4_bf(float (&acc)[8], float xv, u32x4 (&w)[4]) {
  const int hi = __float_as_int(xv), lo = (int)((u32)hi >> 16);
  auto one = [&](u32 xl, u32 xh, u32x4 v) {
#pragma unroll
    for (int k = 0; k < 4; k++) { acc[2 * k] = dot2(xl, v[k], acc[2 * k]); acc[2 * k + 1] = dot2(xh, v[k], acc[2 * k + 1]); }
  };
  one(dppi<BCAST + LN>(lo), dppi<BCAST + LN>(hi), w[0]); one(dppi<BCAST + LN + 1>(lo), dppi<BCAST + LN + 1>(hi), w[1]);
  one(dppi<BCAST + LN + 2>(lo), dppi<BCAST + LN + 2>(hi), w[2]); one(dppi<BCAST + LN + 3>(lo), dppi<BCAST + LN + 3>(hi), w[3]);
}
__device__ __forceinline__ void gemv8(float (&acc)[8], const float* xv, u32x4 (&w)[4]) {
#pragma unroll
  for (int i = 0; i < 4; i++)
#pragma unroll
    for (int k = 0; k < 4; k++) { acc[2 * k] = fmaf(xv[i], bf_lo(w[i][k]), acc[2 * k]); acc[2 * k + 1] = fmaf(xv[i], bf_hi(w[i][k]), acc[2 * k + 1]); }
}

__device__ __forceinline__ void warm_code(int lane) {
  u64 pc; u32 back, n0, n1;
  asm volatile("s_getpc_b64 %0\n"
               "fd_here:\n\t"
               "s_mov_b32 %1, fd_here - fd_code_begin\n\t"
               "s_mov_b32 %2, fd_code_stream_end - fd_code_begin\n\t"
               "s_mov_b32 %3, fd_code_helper_end - fd_code_begin"
               : "=s"(pc), "=s"(back), "=s"(n0), "=s"(n1));
  pc -= back;
  const u32 n = max(n0, n1);
  u32 v[5];
#pragma unroll
  for (int k = 0; k < 5; k++) v[k] = *(const GAS u32*)(pc + (min(lane * 64u + k * 4096u, n - 4u) & ~3u));
  asm volatile("" :: "v"(v[0]), "v"(v[1]), "v"(v[2]), "v"(v[3]), "v"(v[4]));
}

struct Lds {
  float y[4][64][64];
  float part[4][128];
  float qp[4][128];
  float op[4][128];
  float h[128];
  float mw[4];
  float fw[4];
  u32 kdone[4];
  u16 q[128];
  u32 ctl[4];
};

__global__ void __launch_bounds__(320, 2)
fused_decode(const u16* __restrict__ x, const u16* __restrict__ kc, const u16* __restrict__ vc, const u16* __restrict__ wq,
             const u16* __restrict__ wo, const u16* __restrict__ wg, const u16* __restrict__ wu, const u16* __restrict__ wd,
             u16* __restrict__ out, unsigned char* __restrict__ ws) {
  __shared__ __attribute__((aligned(16))) Lds L;
  asm volatile("fd_code_begin:");
  const int b = blockIdx.x, head = b >> 3, j = b & 7;
  const int lane = threadIdx.x & 63, wave = __builtin_amdgcn_readfirstlane(threadIdx.x >> 6);
  const int r = lane >> 4, c = lane & 15;
  const bool has_ffn = j != (head & 7);
  const int ks = j >> 1, jb = j & 1, hr = head & 7;
  const int cg = 2 * head + jb - 2 * (head >> 3) - ((hr > 2 * ks || (hr == 2 * ks && jb)) ? 1 : 0) - (hr > 2 * ks + 1 ? 1 : 0);
  const int dg = head - ((head - j + 7) >> 3);

  if (wave < 4) {
    const int w = wave;
    u32x4 X0, X1, XS;
    u32x4 P0[4], P1[4], P2[4], P3[4], R0[4], R1[4], R2[4], R3[4];
    const i32x4 rx = mkr(x, D * 2);
    const u32 voff = lane * 16;
    const int r8 = lane >> 3, c8 = lane & 7;
    const u32 row0 = ks * 256 + 64 * w;
    {
      const u32 so = (row0 + 8 * r8) * 2;
      asm volatile("buffer_load_dwordx4 %0, %1, %2, 0 offen" : "=v"(X0) : "v"(voff), "s"(rx));
      asm volatile("buffer_load_dwordx4 %0, %1, %2, 0 offen offset:1024" : "=v"(X1) : "v"(voff), "s"(rx));
      asm volatile("buffer_load_dwordx4 %0, %1, %2, 0 offen" : "=v"(XS) : "v"(so), "s"(rx));
    }
    {
      const i32x4 rq = mkr(wq, (u32)D * 4096 * 2);
      const u32 vo = r8 * 8 * 8192 + c8 * 16, s0 = row0 * 8192 + head * 256 + jb * 128;
      rows4_o(P0, vo, rq, s0, 8192); rows4_o(P1, vo, rq, s0 + 4 * 8192, 8192);
    }
    const u32 nffn = has_ffn ? (u32)D * FF * 2 : 0u;
    const i32x4 rg = mkr(wg, nffn), ru = mkr(wu, nffn);
    constexpr u32 GSTEP = FF * 2;
    const u32 vg = r8 * 8 * (FF * 2) + c8 * 16, g0 = row0 * (FF * 2) + cg * 128;
    const i32x4 rk = mkr((const char*)kc + (size_t)head * HEAD_BYTES, HEAD_BYTES), rv = mkr((const char*)vc + (size_t)head * HEAD_BYTES, HEAD_BYTES);
    const u32 t0 = (u32)(j * 256 + w) * 4096;
    asm volatile("s_waitcnt vmcnt(8)" : "+v"(X0), "+v"(X1), "+v"(XS));
    float xn[8];
    {
      float v[16];
#pragma unroll
      for (int k = 0; k < 4; k++) { v[2 * k] = bf_lo(X0[k]); v[2 * k + 1] = bf_hi(X0[k]); v[8 + 2 * k] = bf_lo(X1[k]); v[9 + 2 * k] = bf_hi(X1[k]); }
      float s = 0.f;
#pragma unroll
      for (int k = 0; k < 16; k++) s += v[k];
      const float mean = wave_sum(s) * (1.0f / D);
      float q = 0.f;
#pragma unroll
      for (int k = 0; k < 16; k++) { const float d = v[k] - mean; q = fmaf(d, d, q); }
      const float var = wave_sum(q) * (1.0f / D);
      const float den = rbf(sqrtf(rbf(rbf(var) + 1e-5f)));
      const float mb = rbf(mean);
#pragma unroll
      for (int k = 0; k < 4; k++) { xn[2 * k] = rbf(rbf(bf_lo(XS[k]) - mb) / den); xn[2 * k + 1] = rbf(rbf(bf_hi(XS[k]) - mb) / den); }
    }
    pin8(xn);
    const bool b8 = (c & 8) != 0, b4 = (c & 4) != 0;
    {
      float a[8] = {0.f, 0.f, 0.f, 0.f, 0.f, 0.f, 0.f, 0.f};
      wait4<4>(P0); rows4_o(P2, vg, rg, g0, GSTEP); pin4(P0);
      gemv8(a, xn, P0); pin8(a);
      wait4<4>(P1); rows4_o(P3, vg, rg, g0 + 4 * GSTEP, GSTEP); pin4(P1);
      gemv8(a, xn + 4, P1);
      L.qp[w][8 * c8 + r8] = fold8(a, b8);
    }
    wg_barrier();
    {
      float a[8] = {0.f, 0.f, 0.f, 0.f, 0.f, 0.f, 0.f, 0.f};
      wait4<4>(P2); rows4_o(P0, vg, ru, g0, GSTEP); pin4(P2);
      gemv8(a, xn, P2); pin8(a);
      wait4<4>(P3); rows4_o(P1, vg, ru, g0 + 4 * GSTEP, GSTEP); pin4(P3);
      gemv8(a, xn + 4, P3);
      L.part[w][8 * c8 + r8] = fold8(a, b8);
    }
    {
      float a[8] = {0.f, 0.f, 0.f, 0.f, 0.f, 0.f, 0.f, 0.f};
      wait4<4>(P0); tile_o(R0, voff, rk, t0); pin4(P0);
      gemv8(a, xn, P0); pin8(a);
      wait4<4>(P1); tile_o(R1, voff, rk, t0 + 16384); pin4(P1);
      gemv8(a, xn + 4, P1);
      L.part[w][64 + 8 * c8 + r8] = fold8(a, b8);
    }
    wait4<4>(R0); tile_o(R2, voff, rk, t0 + 2 * 16384);
    wait4<4>(R1); tile_o(R3, voff, rk, t0 + 3 * 16384);
    wg_barrier();
    const u32x4 q = *(const u32x4*)&L.q[8 * c];
    const u32 tag = __builtin_amdgcn_readfirstlane(L.ctl[0]);
    const u32 tref = __builtin_amdgcn_readfirstlane(L.ctl[1]);

    float m = -INFINITY, acc[8] = {0.f, 0.f, 0.f, 0.f, 0.f, 0.f, 0.f, 0.f};
    u32 due16 = (tref << 4) + PACE_T0 * 16 + w * (PACE_STEP / 4);
    auto clock16 = [&]() -> u32 { return (u32)__builtin_amdgcn_s_memrealtime() << 4; };
    auto pace = [&](u32 early, u32 step, u32 slack) {
      if ((int)(early - due16) < 0) {
#pragma unroll 1
        while ((int)(clock16() - due16) < 0) __builtin_amdgcn_s_sleep(1);
      } else if (early - due16 > slack) due16 = early - slack;
      due16 += step;
    };
    auto kstep = [&](u32x4 (&R)[4], int n) {
      i32x4 rr = rk;
      rr.x = n + 4 < 64 ? rk.x : rv.x; rr.y = n + 4 < 64 ? rk.y : rv.y;
      u32 soff = t0 + (u32)((n + 4) & 63) * 16384;
      u32 early = clock16();
      asm volatile("" : "+s"(rr), "+s"(soff), "+s"(early));
      wait4<12>(R);
      float d[4];
#pragma unroll
      for (int u = 0; u < 4; u++) d[u] = dot2(R[u][3], q[3], dot2(R[u][2], q[2], dot2(R[u][1], q[1], dot2(R[u][0], q[0], 0.f))));
      const float e0 = (b8 ? d[2] : d[0]) + dpp<ROR8>(b8 ? d[0] : d[2]);
      const float e1 = (b8 ? d[3] : d[1]) + dpp<ROR8>(b8 ? d[1] : d[3]);
      float f = (b4 ? e1 : e0) + dpp<HMIRROR>(b4 ? e0 : e1);
      f += dpp<XOR1>(f); f += dpp<XOR2>(f);
      const float y = rbf(rbf(f) * 0.08838834764831845f) * 1.4426950408889634f;
      L.y[w][n][lane] = y;
      m = fmaxf(m, y);
      asm volatile("" : "+v"(m));
      pace(early, n < PACE_NSLOW ? PACE_SLOW : PACE_STEP, n < PACE_NSLOW ? 0x7fffffffu : PACE_SLACK);
      tile(R, voff, rr, soff);
    };
    auto vload = [&](int n) -> float { return __builtin_amdgcn_exp2f(L.y[w][n][lane] - m); };
    auto vstep = [&](u32x4 (&R)[4], int n) {
      u32 soff = t0 + (u32)(n + 4) * 16384;
      u32 early = clock16();
      float p = vload(n);
      asm volatile("" : "+s"(soff), "+v"(p), "+s"(early));
      wait4<12>(R);
      gemv4<0, 4>(acc, p, R);
      pin8(acc);
      pace(early, PACE_STEP, PACE_SLACK);
      tile(R, voff, rv, soff);
    };
#pragma unroll 1
    for (int n = 0; n < 64; n += 4) { kstep(R0, n); kstep(R1, n + 1); kstep(R2, n + 2); kstep(R3, n + 3); }
    m = fmaxf(m, dpp<ROR4>(m)); m = fmaxf(m, dpp<ROR8>(m)); m = xrow_max(m);
    if (lane == 0) L.mw[w] = m;
    lds_done();
    if (lane == 0) lds_put(&L.kdone[w], tag);
#pragma unroll 1
    for (int n = 0; n < 60; n += 4) { vstep(R0, n); vstep(R1, n + 1); vstep(R2, n + 2); vstep(R3, n + 3); }
    u32x4 W0[4], W1[4], D0[4], D1[4];
    {
      const float p0 = vload(60), p1 = vload(61), p2 = vload(62), p3 = vload(63);
#pragma unroll 1
      for (u32 spin = 0; lds_get(&L.ctl[2]) != tag && spin < 64 * SPIN_CAP; spin++) __builtin_amdgcn_s_sleep(2);
      asm volatile("" ::: "memory");
      const float fs = L.fw[w];
      wait4<12>(R0); gemv4<0, 4>(acc, p0, R0);
      wait4<8>(R1); gemv4<0, 4>(acc, p1, R1);
      pin8(acc);
      const u32 wvo = (16 * (r & 1) + 8 * (r >> 1)) * 2048 + c * 16;
      const i32x4 rwo = mkr(wo, (u32)4096 * D * 2), rd = mkr(wd, has_ffn ? (u32)FF * D * 2 : 0u);
      const u32 wso = (u32)(head * 128 + 32 * w) * 2048 + j * 256, dso = (u32)(dg * 128 + 32 * w) * 2048 + j * 256;
      due16 += 160 - w * (PACE_STEP / 4);
      pace(due16 - 1, 0, 0);
      rows4_o(D0, wvo, rd, dso, 2048); rows4_o(D1, wvo, rd, dso + 8192, 2048);
      rows4_o(W0, wvo, rwo, wso, 2048); rows4_o(W1, wvo, rwo, wso + 8192, 2048);
      wait4<20>(R2); gemv4<0, 4>(acc, p2, R2);
      wait4<16>(R3); gemv4<0, 4>(acc, p3, R3);
      *(f32x2*)&L.op[w][8 * c + 2 * r] = fold_rows(acc) * fs;
    }
    wg_barrier();
    {
      const float hv = has_ffn ? L.h[32 * w + 16 * (r & 1) + 8 * (r >> 1) + (c & 7)] : 0.f;
      float a[8] = {0.f, 0.f, 0.f, 0.f, 0.f, 0.f, 0.f, 0.f};
      wait4<12>(D0); gemv4_bf<0>(a, hv, D0);
      wait4<8>(D1); gemv4_bf<4>(a, hv, D1);
      pin8(a);
      wait4<0>(W0); wait4<0>(W1);
      const gu64* s1 = (const gu64*)(ws + WS_S1) + (size_t)(head * 8 + (lane >> 5)) * 128 + 32 * w + (lane & 31);
      u64 g[4];
      for (u32 spin = 0;; spin++) {
#pragma unroll
        for (int k = 0; k < 4; k++) g[k] = get(s1 + k * 256);
        if (__all(tag_ok(g[0], tag) & tag_ok(g[1], tag) & tag_ok(g[2], tag) & tag_ok(g[3], tag)) || spin > SPIN_CAP) break;
      }
      float o = (val(g[0]) + val(g[1])) + (val(g[2]) + val(g[3]));
      o = rbf(swap32_add(o, o));
      const float orot = dpp<ROR8>(o);
      o = (r & 2) ? orot : o;
      gemv4_bf<0>(a, o, W0); gemv4_bf<4>(a, o, W1);
      *(f32x2*)&L.qp[w][8 * c + 2 * r] = fold_rows(a);
    }
    wg_barrier();
    asm volatile("fd_code_stream_end:");
  } else {
    const u32 tstart = (u32)__builtin_amdgcn_s_memrealtime();
    const u32 ep = get32((gu32*)(ws + WS_EPOCH));
    const int fin = (head >> 2) * 8 + j;
    const u32 xfin = ((const GAS u16*)x)[16 * fin + c];
    if (head < 4) warm_code(lane);
    const u32 tag = __builtin_amdgcn_readfirstlane(ep) + 1u;
    gu64* qrec = (gu64*)(ws + WS_QREC);
    if (lane == 0) put(qrec + (size_t)b * 80 + 64, tag, __uint_as_float(tstart));
    if (lane < 4) L.kdone[lane] = ~tag;
    if (lane == 0) L.ctl[2] = ~tag;
    wg_barrier();
    {
      put(qrec + (size_t)b * 80 + lane, tag, (L.qp[0][lane] + L.qp[1][lane]) + (L.qp[2][lane] + L.qp[3][lane]));
      const gu64* qr = qrec + (size_t)head * 8 * 80;
      __builtin_amdgcn_s_sleep(8);
      u64 g[9];
      for (u32 spin = 0;; spin++) {
#pragma unroll
        for (int k = 0; k < 8; k++) g[k] = get(qr + k * 80 + lane);
        g[8] = get(qr + (lane & 7) * 80 + 64);
        bool ok = true;
#pragma unroll
        for (int k = 0; k < 9; k++) ok &= tag_ok(g[k], tag);
        if (__all(ok) || spin > SPIN_CAP) break;
      }
      L.q[lane] = bf_bits((val(g[0]) + val(g[2])) + (val(g[4]) + val(g[6])));
      L.q[64 + lane] = bf_bits((val(g[1]) + val(g[3])) + (val(g[5]) + val(g[7])));
      int tm = (int)((u32)g[8] - tstart);
      tm = min(tm, dppi<XOR1>(tm)); tm = min(tm, dppi<XOR2>(tm)); tm = min(tm, dppi<HMIRROR>(tm));
      if (lane == 0) { L.ctl[0] = tag; L.ctl[1] = tstart + (u32)min(tm, 0); }
    }
    wg_barrier();
    if (has_ffn) {
      const int unit = cg * 4 + ks;
      gu64* gu = (gu64*)(ws + WS_GU);
      const f32x2 mine = {(L.part[0][lane] + L.part[1][lane]) + (L.part[2][lane] + L.part[3][lane]),
                          (L.part[0][64 + lane] + L.part[1][64 + lane]) + (L.part[2][64 + lane] + L.part[3][64 + lane])};
      put64(gu + unit * 64 + lane, __builtin_bit_cast(u64, mine));
      drain();
      if (lane == 0) put32((gu32*)(ws + WS_FLAG + unit * 64), tag);
      const gu32* fl = (const gu32*)(ws + WS_FLAG + (dg * 8 + (lane & 7)) * 64);
      __builtin_amdgcn_s_sleep(64);
      for (u32 spin = 0; !__all(get32(fl) == tag) && spin < SPIN_CAP; spin++) __builtin_amdgcn_s_sleep(32);
      __builtin_amdgcn_fence(__ATOMIC_ACQUIRE, "wavefront");
      u64 pl[8];
#pragma unroll
      for (int k = 0; k < 8; k++) pl[k] = get(gu + (dg * 8 + k) * 64 + lane);
      asm volatile("" : "+v"(pl[0]), "+v"(pl[1]), "+v"(pl[2]), "+v"(pl[3]), "+v"(pl[4]), "+v"(pl[5]), "+v"(pl[6]), "+v"(pl[7]));
      auto pair = [&](int k) { return __builtin_bit_cast(f32x2, pl[k]); };
      const f32x2 lo = (pair(0) + pair(1)) + (pair(2) + pair(3)), hi = (pair(4) + pair(5)) + (pair(6) + pair(7));
      auto act = [](float g, float u) {
        g = rbf(g); u = rbf(u);
        const float inner = 0.7978845608028654f * (g + 0.044715f * g * g * g);
        return rbf(rbf(0.5f * g * (1.f + tanhf(inner))) * u);
      };
      L.h[lane] = act(lo.x, lo.y); L.h[64 + lane] = act(hi.x, hi.y);
    }
    {
#pragma unroll 1
      for (u32 spin = 0; !__all(lds_get(&L.kdone[lane & 3]) == tag) && spin < 64 * SPIN_CAP; spin++) __builtin_amdgcn_s_sleep(16);
      asm volatile("" ::: "memory");
      const float mine = L.mw[lane & 3];
      float mc = fmaxf(mine, dpp<XOR1>(mine)); mc = fmaxf(mc, dpp<XOR2>(mc));
      float s = 0.f;
      const float* yy = &L.y[0][0][lane];
#pragma unroll 8
      for (int t = 0; t < 256; t++) s += __builtin_amdgcn_exp2f(yy[t * 64] - mc);
      s = wave_sum(s) * 0.25f;
      gu64* ml = (gu64*)(ws + WS_ML);
      if (lane < 2) put(ml + b * 16 + lane, tag, lane == 0 ? mc : s);
      __builtin_amdgcn_s_sleep(127);
      u64 g0, g1;
      for (u32 spin = 0;; spin++) {
        g0 = get(ml + (head * 8 + (lane & 7)) * 16); g1 = get(ml + (head * 8 + (lane & 7)) * 16 + 1);
        if (__all(tag_ok(g0, tag) & tag_ok(g1, tag)) || spin > SPIN_CAP) break;
        __builtin_amdgcn_s_sleep(64);
      }
      float M = val(g0);
      M = fmaxf(M, dpp<XOR1>(M)); M = fmaxf(M, dpp<XOR2>(M)); M = fmaxf(M, dpp<HMIRROR>(M));
      float t = val(g1) * __builtin_amdgcn_exp2f(val(g0) - M);
      t += dpp<XOR1>(t); t += dpp<XOR2>(t); t += dpp<HMIRROR>(t);
      if (lane < 4) L.fw[lane] = __builtin_amdgcn_exp2f(mine - M) / t;
      lds_done();
      if (lane == 0) lds_put(&L.ctl[2], tag);
    }
    wg_barrier();
    {
      gu64* s1 = (gu64*)(ws + WS_S1) + (size_t)b * 128;
      put(s1 + lane, tag, (L.op[0][lane] + L.op[1][lane]) + (L.op[2][lane] + L.op[3][lane]));
      put(s1 + 64 + lane, tag, (L.op[0][64 + lane] + L.op[1][64 + lane]) + (L.op[2][64 + lane] + L.op[3][64 + lane]));
    }
    wg_barrier();
    gu64* s2 = (gu64*)(ws + WS_S2);
    put(s2 + (size_t)b * 128 + lane, tag, (L.qp[0][lane] + L.qp[1][lane]) + (L.qp[2][lane] + L.qp[3][lane]));
    put(s2 + (size_t)b * 128 + 64 + lane, tag, (L.qp[0][64 + lane] + L.qp[1][64 + lane]) + (L.qp[2][64 + lane] + L.qp[3][64 + lane]));
    if ((head & 3) == 0) {
      const gu64* sr = s2 + (size_t)(r * 8 + (fin >> 3)) * 128 + (fin & 7) * 16 + c;
      u64 g[8];
      __builtin_amdgcn_s_sleep(8);
      for (u32 spin = 0;; spin++) {
        bool ok = true;
#pragma unroll
        for (int k = 0; k < 8; k++) g[k] = get(sr + k * 4 * 8 * 128);
#pragma unroll
        for (int k = 0; k < 8; k++) ok &= tag_ok(g[k], tag);
        if (__all(ok) || spin > SPIN_CAP) break;
      }
      float a = ((val(g[0]) + val(g[1])) + (val(g[2]) + val(g[3]))) + ((val(g[4]) + val(g[5])) + (val(g[6]) + val(g[7])));
      a = xrow_sum(a) + bf_lo(xfin);
      if (lane < 16) ((GAS u16*)out)[16 * fin + lane] = bf_bits(a);
      if (b == 0 && lane == 0) put32((gu32*)(ws + WS_EPOCH), tag);
      if (b == 0) {
        const int e = (int)((u32)__builtin_amdgcn_s_memrealtime() - tstart);
        const int target = min(10000 + 5 * (e - 8000), 30000);
        while ((int)((u32)__builtin_amdgcn_s_memrealtime() - tstart) < target) __builtin_amdgcn_s_sleep(4);
      }
    }
    asm volatile("fd_code_helper_end:");
  }
}

}

extern "C" void kernel_launch(void* const* d_in, const int* in_sizes, int n_in, void* d_out, int out_size, void* d_ws, size_t ws_size,
                              hipStream_t stream) {
  (void)in_sizes; (void)n_in; (void)out_size; (void)ws_size;
  fused_decode<<<256, 320, 0, stream>>>((const u16*)d_in[0], (const u16*)d_in[1], (const u16*)d_in[2], (const u16*)d_in[3], (const u16*)d_in[4],
                                        (const u16*)d_in[6], (const u16*)d_in[7], (const u16*)d_in[8], (u16*)d_out, (unsigned char*)d_ws);
}
```

```cpp
#include <hip/hip_runtime.h>
#include <stdint.h>

namespace {

typedef unsigned u32;
typedef unsigned long long u64;
typedef unsigned short u16;
typedef u32 u32x4 __attribute__((ext_vector_type(4)));
typedef int i32x4 __attribute__((ext_vector_type(4)));
typedef float f32x2 __attribute__((ext_vector_type(2)));
typedef __attribute__((ext_vector_type(2))) __bf16 bf16x2;
#define GAS __attribute__((address_space(1)))
#define LAS __attribute__((address_space(3)))
typedef GAS u64 gu64;
typedef GAS u32 gu32;

constexpr int D = 1024, SK = 32768, DH = 128, FF = 3584;
constexpr u32 HEAD_BYTES = (u32)SK * DH * 2;

constexpr u32 PACE_T0 = 560;
constexpr u32 PACE_SLOW = 950, PACE_STEP = 888;
constexpr int PACE_NSLOW = 32;
constexpr u32 PACE_SLACK = 60 * 16;

constexpr size_t WS_EPOCH = 0;
constexpr size_t WS_FLAG = 16384;
constexpr size_t WS_ML = 32768;
constexpr size_t WS_QREC = 65536;
constexpr size_t WS_S1 = WS_QREC + 256 * 80 * 8;
constexpr size_t WS_S2 = WS_S1 + 256 * 128 * 8;
constexpr size_t WS_GU = WS_S2 + 256 * 128 * 8;
constexpr u32 SPIN_CAP = 1u << 16;

__device__ __forceinline__ float bf_lo(u32 v) { return __uint_as_float(v << 16); }
__device__ __forceinline__ float bf_hi(u32 v) { return __uint_as_float(v & 0xffff0000u); }
__device__ __forceinline__ u16 bf_bits(float x) { return __builtin_bit_cast(u16, (__bf16)x); }
__device__ __forceinline__ float rbf(float x) { return bf_lo((u32)bf_bits(x)); }
__device__ __forceinline__ float dot2(u32 a, u32 b, float c) {
  return __builtin_amdgcn_fdot2_f32_bf16(__builtin_bit_cast(bf16x2, a), __builtin_bit_cast(bf16x2, b), c, false);
}
template <int CTRL> __device__ __forceinline__ int dppi(int x) { return __builtin_amdgcn_mov_dpp(x, CTRL, 0xf, 0xf, true); }
template <int CTRL> __device__ __forceinline__ float dpp(float x) { return __int_as_float(dppi<CTRL>(__float_as_int(x))); }
constexpr int XOR1 = 0xB1, XOR2 = 0x4E, HMIRROR = 0x141, ROR8 = 0x128, ROR4 = 0x124, BCAST = 0x150;
__device__ __forceinline__ float swap32_add(float a, float b) {
  auto s = __builtin_amdgcn_permlane32_swap(__float_as_uint(a), __float_as_uint(b), false, false);
  return __uint_as_float(s[0]) + __uint_as_float(s[1]);
}
__device__ __forceinline__ float swap16_add(float a, float b) {
  auto s = __builtin_amdgcn_permlane16_swap(__float_as_uint(a), __float_as_uint(b), false, false);
  return __uint_as_float(s[0]) + __uint_as_float(s[1]);
}
__device__ __forceinline__ float xrow_sum(float x) { x = swap16_add(x, x); return swap32_add(x, x); }
__device__ __forceinline__ float xrow_max(float x) {
  auto s = __builtin_amdgcn_permlane16_swap(__float_as_uint(x), __float_as_uint(x), false, false);
  x = fmaxf(__uint_as_float(s[0]), __uint_as_float(s[1]));
  auto t = __builtin_amdgcn_permlane32_swap(__float_as_uint(x), __float_as_uint(x), false, false);
  return fmaxf(__uint_as_float(t[0]), __uint_as_float(t[1]));
}
__device__ __forceinline__ float wave_sum(float x) {
  x += dpp<XOR1>(x); x += dpp<XOR2>(x); x += dpp<ROR4>(x); x += dpp<ROR8>(x);
  return xrow_sum(x);
}
__device__ __forceinline__ f32x2 fold_rows(const float (&a)[8]) {
  float s0 = swap32_add(a[0], a[4]), s1 = swap32_add(a[1], a[5]), s2 = swap32_add(a[2], a[6]), s3 = swap32_add(a[3], a[7]);
  return f32x2{swap16_add(s0, s2), swap16_add(s1, s3)};
}
__device__ __forceinline__ float fold8(const float (&a)[8], bool b8) {
  const f32x2 s = fold_rows(a);
  return (b8 ? s.y : s.x) + dpp<ROR8>(b8 ? s.x : s.y);
}

__device__ __forceinline__ i32x4 mkr(const void* p, u32 n) {
  u64 a = (u64)p;
  i32x4 r; r.x = (int)(u32)a; r.y = (int)((u32)(a >> 32) & 0xffffu); r.z = (int)n; r.w = 0x00020000; return r;
}
__device__ __forceinline__ void ld_o(u32x4& d, u32 voff, i32x4 r, u32 soff) {
  asm volatile("buffer_load_dwordx4 %0, %1, %2, %3 offen nt" : "=v"(d) : "v"(voff), "s"(r), "s"(soff));
}
__device__ __forceinline__ void rows4_o(u32x4 (&R)[4], u32 voff, i32x4 r, u32 soff, u32 step) {
  ld_o(R[0], voff, r, soff); ld_o(R[1], voff, r, soff + step); ld_o(R[2], voff, r, soff + 2 * step); ld_o(R[3], voff, r, soff + 3 * step);
}
#define TILE_LOADS \
      "buffer_load_dwordx4 %0, %4, %5, %6 offen nt\n\t" \
      "buffer_load_dwordx4 %1, %4, %5, %6 offen offset:1024 nt\n\t" \
      "buffer_load_dwordx4 %2, %4, %5, %6 offen offset:2048 nt\n\t" \
      "buffer_load_dwordx4 %3, %4, %5, %6 offen offset:3072 nt"
__device__ __forceinline__ void tile_o(u32x4 (&R)[4], u32 voff, i32x4 r, u32 soff) {
  asm volatile(TILE_LOADS : "=&v"(R[0]), "=&v"(R[1]), "=&v"(R[2]), "=&v"(R[3]) : "v"(voff), "s"(r), "s"(soff));
}
__device__ __forceinline__ void tile(u32x4 (&R)[4], u32 voff, i32x4 r, u32 soff) {
  asm volatile(TILE_LOADS : "+v"(R[0]), "+v"(R[1]), "+v"(R[2]), "+v"(R[3]) : "v"(voff), "s"(r), "s"(soff));
}
template <int N> __device__ __forceinline__ void wait4(u32x4 (&R)[4]) {
  asm volatile("s_waitcnt vmcnt(%4)" : "+v"(R[0]), "+v"(R[1]), "+v"(R[2]), "+v"(R[3]) : "i"(N));
}
__device__ __forceinline__ void pin4(u32x4 (&R)[4]) { asm volatile("" : "+v"(R[0]), "+v"(R[1]), "+v"(R[2]), "+v"(R[3])); }
__device__ __forceinline__ void pin8(float (&a)[8]) {
  asm volatile("" : "+v"(a[0]), "+v"(a[1]), "+v"(a[2]), "+v"(a[3]), "+v"(a[4]), "+v"(a[5]), "+v"(a[6]), "+v"(a[7]));
}
__device__ __forceinline__ void wg_barrier() { asm volatile("s_waitcnt lgkmcnt(0)\n\ts_barrier" ::: "memory"); }
__device__ __forceinline__ void drain() { asm volatile("s_waitcnt vmcnt(0)" ::: "memory"); }
__device__ __forceinline__ void lds_done() { asm volatile("s_waitcnt lgkmcnt(0)" ::: "memory"); }
__device__ __forceinline__ u32 lds_get(const u32* p) { return __hip_atomic_load((const LAS u32*)p, __ATOMIC_RELAXED, __HIP_MEMORY_SCOPE_WORKGROUP); }
__device__ __forceinline__ void lds_put(u32* p, u32 v) { __hip_atomic_store((LAS u32*)p, v, __ATOMIC_RELAXED, __HIP_MEMORY_SCOPE_WORKGROUP); }

__device__ __forceinline__ void put64(gu64* g, u64 v) { __hip_atomic_store(g, v, __ATOMIC_RELAXED, __HIP_MEMORY_SCOPE_AGENT); }
__device__ __forceinline__ void put(gu64* g, u32 tag, float v) { put64(g, ((u64)tag << 32) | (u64)__float_as_uint(v)); }
__device__ __forceinline__ u64 get(const gu64* g) { return __hip_atomic_load(g, __ATOMIC_RELAXED, __HIP_MEMORY_SCOPE_AGENT); }
__device__ __forceinline__ void put32(gu32* g, u32 v) { __hip_atomic_store(g, v, __ATOMIC_RELAXED, __HIP_MEMORY_SCOPE_AGENT); }
__device__ __forceinline__ u32 get32(const gu32* g) { return __hip_atomic_load(g, __ATOMIC_RELAXED, __HIP_MEMORY_SCOPE_AGENT); }
__device__ __forceinline__ bool tag_ok(u64 x, u32 tag) { return (u32)(x >> 32) == tag; }
__device__ __forceinline__ float val(u64 x) { return __uint_as_float((u32)x); }

template <int LN> __device__ __forceinline__ void gemv_step(float (&acc)[8], float xv, u32x4 w) {
  const float xb = dpp<BCAST + LN>(xv);
#pragma unroll
  for (int k = 0; k < 4; k++) { acc[2 * k] = fmaf(xb, bf_lo(w[k]), acc[2 * k]); acc[2 * k + 1] = fmaf(xb, bf_hi(w[k]), acc[2 * k + 1]); }
}
template <int LN, int ST> __device__ __forceinline__ void gemv4(float (&acc)[8], float xv, u32x4 (&w)[4]) {
  gemv_step<LN>(acc, xv, w[0]); gemv_step<LN + ST>(acc, xv, w[1]); gemv_step<LN + 2 * ST>(acc, xv, w[2]); gemv_step<LN + 3 * ST>(acc, xv, w[3]);
}
template <int LN> __device__ __forceinline__ void gemv4_bf(float (&acc)[8], float xv, u32x4 (&w)[4]) {
  const int hi = __float_as_int(xv), lo = (int)((u32)hi >> 16);
  auto one = [&](u32 xl, u32 xh, u32x4 v) {
#pragma unroll
    for (int k = 0; k < 4; k++) { acc[2 * k] = dot2(xl, v[k], acc[2 * k]); acc[2 * k + 1] = dot2(xh, v[k], acc[2 * k + 1]); }
  };
  one(dppi<BCAST + LN>(lo), dppi<BCAST + LN>(hi), w[0]); one(dppi<BCAST + LN + 1>(lo), dppi<BCAST + LN + 1>(hi), w[1]);
  one(dppi<BCAST + LN + 2>(lo), dppi<BCAST + LN + 2>(hi), w[2]); one(dppi<BCAST + LN + 3>(lo), dppi<BCAST + LN + 3>(hi), w[3]);
}
__device__ __forceinline__ void gemv8(float (&acc)[8], const float* xv, u32x4 (&w)[4]) {
#pragma unroll
  for (int i = 0; i < 4; i++)
#pragma unroll
    for (int k = 0; k < 4; k++) { acc[2 * k] = fmaf(xv[i], bf_lo(w[i][k]), acc[2 * k]); acc[2 * k + 1] = fmaf(xv[i], bf_hi(w[i][k]), acc[2 * k + 1]); }
}

__device__ __forceinline__ void warm_code(int lane) {
  u64 pc; u32 back, n0, n1;
  asm volatile("s_getpc_b64 %0\n"
               "fd_here:\n\t"
               "s_mov_b32 %1, fd_here - fd_code_begin\n\t"
               "s_mov_b32 %2, fd_code_stream_end - fd_code_begin\n\t"
               "s_mov_b32 %3, fd_code_helper_end - fd_code_begin"
               : "=s"(pc), "=s"(back), "=s"(n0), "=s"(n1));
  pc -= back;
  const u32 n = max(n0, n1);
  u32 v[5];
#pragma unroll
  for (int k = 0; k < 5; k++) v[k] = *(const GAS u32*)(pc + (min(lane * 64u + k * 4096u, n - 4u) & ~3u));
  asm volatile("" :: "v"(v[0]), "v"(v[1]), "v"(v[2]), "v"(v[3]), "v"(v[4]));
}

struct Lds {
  float y[4][64][64];
  float part[4][128];
  float qp[4][128];
  float op[4][128];
  float h[128];
  float mw[4];
  float fw[4];
  u32 kdone[4];
  u16 q[128];
  u32 ctl[4];
};

__global__ void __launch_bounds__(320, 2)
fused_decode(const u16* __restrict__ x, const u16* __restrict__ kc, const u16* __restrict__ vc, const u16* __restrict__ wq,
             const u16* __restrict__ wo, const u16* __restrict__ wg, const u16* __restrict__ wu, const u16* __restrict__ wd,
             u16* __restrict__ out, unsigned char* __restrict__ ws) {
  __shared__ __attribute__((aligned(16))) Lds L;
  asm volatile("fd_code_begin:");
  const int b = blockIdx.x, head = b >> 3, j = b & 7;
  const int lane = threadIdx.x & 63, wave = __builtin_amdgcn_readfirstlane(threadIdx.x >> 6);
  const int r = lane >> 4, c = lane & 15;
  const bool has_ffn = j != (head & 7);
  const int ks = j >> 1, jb = j & 1, hr = head & 7;
  const int cg = 2 * head + jb - 2 * (head >> 3) - ((hr > 2 * ks || (hr == 2 * ks && jb)) ? 1 : 0) - (hr > 2 * ks + 1 ? 1 : 0);
  const int dg = head - ((head - j + 7) >> 3);

  if (wave < 4) {
    const int w = wave;
    u32x4 X0, X1, XS;
    u32x4 P0[4], P1[4], P2[4], P3[4], R0[4], R1[4], R2[4], R3[4];
    const i32x4 rx = mkr(x, D * 2);
    const u32 voff = lane * 16;
    const int r8 = lane >> 3, c8 = lane & 7;
    const u32 row0 = ks * 256 + 64 * w;
    {
      const u32 so = (row0 + 8 * r8) * 2;
      asm volatile("buffer_load_dwordx4 %0, %1, %2, 0 offen" : "=v"(X0) : "v"(voff), "s"(rx));
      asm volatile("buffer_load_dwordx4 %0, %1, %2, 0 offen offset:1024" : "=v"(X1) : "v"(voff), "s"(rx));
      asm volatile("buffer_load_dwordx4 %0, %1, %2, 0 offen" : "=v"(XS) : "v"(so), "s"(rx));
    }
    {
      const i32x4 rq = mkr(wq, (u32)D * 4096 * 2);
      const u32 vo = r8 * 8 * 8192 + c8 * 16, s0 = row0 * 8192 + head * 256 + jb * 128;
      rows4_o(P0, vo, rq, s0, 8192); rows4_o(P1, vo, rq, s0 + 4 * 8192, 8192);
    }
    const u32 nffn = has_ffn ? (u32)D * FF * 2 : 0u;
    const i32x4 rg = mkr(wg, nffn), ru = mkr(wu, nffn);
    constexpr u32 GSTEP = FF * 2;
    const u32 vg = r8 * 8 * (FF * 2) + c8 * 16, g0 = row0 * (FF * 2) + cg * 128;
    const i32x4 rk = mkr((const char*)kc + (size_t)head * HEAD_BYTES, HEAD_BYTES), rv = mkr((const char*)vc + (size_t)head * HEAD_BYTES, HEAD_BYTES);
    const u32 t0 = (u32)(j * 256 + w) * 4096;
    asm volatile("s_waitcnt vmcnt(8)" : "+v"(X0), "+v"(X1), "+v"(XS));
    float xn[8];
    {
      float v[16];
#pragma unroll
      for (int k = 0; k < 4; k++) { v[2 * k] = bf_lo(X0[k]); v[2 * k + 1] = bf_hi(X0[k]); v[8 + 2 * k] = bf_lo(X1[k]); v[9 + 2 * k] = bf_hi(X1[k]); }
      float s = 0.f;
#pragma unroll
      for (int k = 0; k < 16; k++) s += v[k];
      const float mean = wave_sum(s) * (1.0f / D);
      float q = 0.f;
#pragma unroll
      for (int k = 0; k < 16; k++) { const float d = v[k] - mean; q = fmaf(d, d, q); }
      const float var = wave_sum(q) * (1.0f / D);
      const float den = rbf(sqrtf(rbf(rbf(var) + 1e-5f)));
      const float mb = rbf(mean);
#pragma unroll
      for (int k = 0; k < 4; k++) { xn[2 * k] = rbf(rbf(bf_lo(XS[k]) - mb) / den); xn[2 * k + 1] = rbf(rbf(bf_hi(XS[k]) - mb) / den); }
    }
    pin8(xn);
    const bool b8 = (c & 8) != 0, b4 = (c & 4) != 0;
    {
      float a[8] = {0.f, 0.f, 0.f, 0.f, 0.f, 0.f, 0.f, 0.f};
      wait4<4>(P0); rows4_o(P2, vg, rg, g0, GSTEP); pin4(P0);
      gemv8(a, xn, P0); pin8(a);
      wait4<4>(P1); rows4_o(P3, vg, rg, g0 + 4 * GSTEP, GSTEP); pin4(P1);
      gemv8(a, xn + 4, P1);
      L.qp[w][8 * c8 + r8] = fold8(a, b8);
    }
    wg_barrier();
    {
      float a[8] = {0.f, 0.f, 0.f, 0.f, 0.f, 0.f, 0.f, 0.f};
      wait4<4>(P2); rows4_o(P0, vg, ru, g0, GSTEP); pin4(P2);
      gemv8(a, xn, P2); pin8(a);
      wait4<4>(P3); rows4_o(P1, vg, ru, g0 + 4 * GSTEP, GSTEP); pin4(P3);
      gemv8(a, xn + 4, P3);
      L.part[w][8 * c8 + r8] = fold8(a, b8);
    }
    {
      float a[8] = {0.f, 0.f, 0.f, 0.f, 0.f, 0.f, 0.f, 0.f};
      wait4<4>(P0); tile_o(R0, voff, rk, t0); pin4(P0);
      gemv8(a, xn, P0); pin8(a);
      wait4<4>(P1); tile_o(R1, voff, rk, t0 + 16384); pin4(P1);
      gemv8(a, xn + 4, P1);
      L.part[w][64 + 8 * c8 + r8] = fold8(a, b8);
    }
    wait4<4>(R0); tile_o(R2, voff, rk, t0 + 2 * 16384);
    wait4<4>(R1); tile_o(R3, voff, rk, t0 + 3 * 16384);
    wg_barrier();
    const u32x4 q = *(const u32x4*)&L.q[8 * c];
    const u32 tag = __builtin_amdgcn_readfirstlane(L.ctl[0]);
    const u32 tref = __builtin_amdgcn_readfirstlane(L.ctl[1]);

    float m = -INFINITY, acc[8] = {0.f, 0.f, 0.f, 0.f, 0.f, 0.f, 0.f, 0.f};
    u32 due16 = (tref << 4) + PACE_T0 * 16 + w * (PACE_STEP / 4);
    auto clock16 = [&]() -> u32 { return (u32)__builtin_amdgcn_s_memrealtime() << 4; };
    auto pace = [&](u32 early, u32 step, u32 slack) {
      if ((int)(early - due16) < 0) {
#pragma unroll 1
        while ((int)(clock16() - due16) < 0) __builtin_amdgcn_s_sleep(1);
      } else if (early - due16 > slack) due16 = early - slack;
      due16 += step;
    };
    auto kstep = [&](u32x4 (&R)[4], int n) {
      i32x4 rr = rk;
      rr.x = n + 4 < 64 ? rk.x : rv.x; rr.y = n + 4 < 64 ? rk.y : rv.y;
      u32 soff = t0 + (u32)((n + 4) & 63) * 16384;
      u32 early = clock16();
      asm volatile("" : "+s"(rr), "+s"(soff), "+s"(early));
      wait4<12>(R);
      float d[4];
#pragma unroll
      for (int u = 0; u < 4; u++) d[u] = dot2(R[u][3], q[3], dot2(R[u][2], q[2], dot2(R[u][1], q[1], dot2(R[u][0], q[0], 0.f))));
      const float e0 = (b8 ? d[2] : d[0]) + dpp<ROR8>(b8 ? d[0] : d[2]);
      const float e1 = (b8 ? d[3] : d[1]) + dpp<ROR8>(b8 ? d[1] : d[3]);
      float f = (b4 ? e1 : e0) + dpp<HMIRROR>(b4 ? e0 : e1);
      f += dpp<XOR1>(f); f += dpp<XOR2>(f);
      const float y = rbf(rbf(f) * 0.08838834764831845f) * 1.4426950408889634f;
      L.y[w][n][lane] = y;
      m = fmaxf(m, y);
      asm volatile("" : "+v"(m));
      pace(early, n < PACE_NSLOW ? PACE_SLOW : PACE_STEP, n < PACE_NSLOW ? 0x7fffffffu : PACE_SLACK);
      tile(R, voff, rr, soff);
    };
    auto vload = [&](int n) -> float { return __builtin_amdgcn_exp2f(L.y[w][n][lane] - m); };
    auto vstep = [&](u32x4 (&R)[4], int n) {
      u32 soff = t0 + (u32)(n + 4) * 16384;
      u32 early = clock16();
      float p = vload(n);
      asm volatile("" : "+s"(soff), "+v"(p), "+s"(early));
      wait4<12>(R);
      gemv4<0, 4>(acc, p, R);
      pin8(acc);
      pace(early, PACE_STEP, PACE_SLACK);
      tile(R, voff, rv, soff);
    };
#pragma unroll 1
    for (int n = 0; n < 64; n += 4) { kstep(R0, n); kstep(R1, n + 1); kstep(R2, n + 2); kstep(R3, n + 3); }
    m = fmaxf(m, dpp<ROR4>(m)); m = fmaxf(m, dpp<ROR8>(m)); m = xrow_max(m);
    if (lane == 0) L.mw[w] = m;
    lds_done();
    if (lane == 0) lds_put(&L.kdone[w], tag);
#pragma unroll 1
    for (int n = 0; n < 60; n += 4) { vstep(R0, n); vstep(R1, n + 1); vstep(R2, n + 2); vstep(R3, n + 3); }
    u32x4 W0[4], W1[4], D0[4], D1[4];
    {
      const float p0 = vload(60), p1 = vload(61), p2 = vload(62), p3 = vload(63);
#pragma unroll 1
      for (u32 spin = 0; lds_get(&L.ctl[2]) != tag && spin < 64 * SPIN_CAP; spin++) __builtin_amdgcn_s_sleep(2);
      asm volatile("" ::: "memory");
      const float fs = L.fw[w];
      wait4<12>(R0); gemv4<0, 4>(acc, p0, R0);
      wait4<8>(R1); gemv4<0, 4>(acc, p1, R1);
      pin8(acc);
      const u32 wvo = (16 * (r & 1) + 8 * (r >> 1)) * 2048 + c * 16;
      const i32x4 rwo = mkr(wo, (u32)4096 * D * 2), rd = mkr(wd, has_ffn ? (u32)FF * D * 2 : 0u);
      const u32 wso = (u32)(head * 128 + 32 * w) * 2048 + j * 256, dso = (u32)(dg * 128 + 32 * w) * 2048 + j * 256;
      due16 += 160 - w * (PACE_STEP / 4);
      pace(due16 - 1, 0, 0);
      rows4_o(D0, wvo, rd, dso, 2048); rows4_o(D1, wvo, rd, dso + 8192, 2048);
      rows4_o(W0, wvo, rwo, wso, 2048); rows4_o(W1, wvo, rwo, wso + 8192, 2048);
      wait4<20>(R2); gemv4<0, 4>(acc, p2, R2);
      wait4<16>(R3); gemv4<0, 4>(acc, p3, R3);
      *(f32x2*)&L.op[w][8 * c + 2 * r] = fold_rows(acc) * fs;
    }
    wg_barrier();
    {
      const float hv = has_ffn ? L.h[32 * w + 16 * (r & 1) + 8 * (r >> 1) + (c & 7)] : 0.f;
      float a[8] = {0.f, 0.f, 0.f, 0.f, 0.f, 0.f, 0.f, 0.f};
      wait4<12>(D0); gemv4_bf<0>(a, hv, D0);
      wait4<8>(D1); gemv4_bf<4>(a, hv, D1);
      pin8(a);
      wait4<0>(W0); wait4<0>(W1);
      const gu64* s1 = (const gu64*)(ws + WS_S1) + (size_t)(head * 8 + (lane >> 5)) * 128 + 32 * w + (lane & 31);
      u64 g[4];
      for (u32 spin = 0;; spin++) {
#pragma unroll
        for (int k = 0; k < 4; k++) g[k] = get(s1 + k * 256);
        if (__all(tag_ok(g[0], tag) & tag_ok(g[1], tag) & tag_ok(g[2], tag) & tag_ok(g[3], tag)) || spin > SPIN_CAP) break;
      }
      float o = (val(g[0]) + val(g[1])) + (val(g[2]) + val(g[3]));
      o = rbf(swap32_add(o, o));
      const float orot = dpp<ROR8>(o);
      o = (r & 2) ? orot : o;
      gemv4_bf<0>(a, o, W0); gemv4_bf<4>(a, o, W1);
      *(f32x2*)&L.qp[w][8 * c + 2 * r] = fold_rows(a);
    }
    wg_barrier();
    asm volatile("fd_code_stream_end:");
  } else {
    const u32 tstart = (u32)__builtin_amdgcn_s_memrealtime();
    const u32 ep = get32((gu32*)(ws + WS_EPOCH));
    const int fin = (head >> 2) * 8 + j;
    const u32 xfin = ((const GAS u16*)x)[16 * fin + c];
    {
      const size_t kv = (size_t)head * SK * DH + (size_t)j * 4096 * DH, gu = (size_t)ks * 256 * FF + (has_ffn ? cg : 0) * 64;
      const int e = lane & 1, s = (lane >> 1) & 7;
      const u16* p = s == 0 ? wg + gu + e * 255 * FF : s == 1 ? wu + gu + e * 255 * FF : s == 2 ? kc + kv + e * (4096 * DH - 2) :
                     s == 3 ? vc + kv + e * (4096 * DH - 2) : s == 4 ? wo + (size_t)(head * 128 + e * 127) * D + j * 128 :
                     s == 5 ? wd + (size_t)((has_ffn ? dg : 0) * 128 + e * 127) * D + j * 128 : s == 6 ? out + 16 * fin : x;
      const u32 t = __builtin_nontemporal_load((const GAS u32*)p);
      asm volatile("" :: "v"(t));
    }
    if (head < 4) warm_code(lane);
    const u32 tag = __builtin_amdgcn_readfirstlane(ep) + 1u;
    gu64* qrec = (gu64*)(ws + WS_QREC);
    if (lane == 0) put(qrec + (size_t)b * 80 + 64, tag, __uint_as_float(tstart));
    if (lane < 4) L.kdone[lane] = ~tag;
    if (lane == 0) L.ctl[2] = ~tag;
    wg_barrier();
    {
      put(qrec + (size_t)b * 80 + lane, tag, (L.qp[0][lane] + L.qp[1][lane]) + (L.qp[2][lane] + L.qp[3][lane]));
      const gu64* qr = qrec + (size_t)head * 8 * 80;
      __builtin_amdgcn_s_sleep(8);
      u64 g[9];
      for (u32 spin = 0;; spin++) {
#pragma unroll
        for (int k = 0; k < 8; k++) g[k] = get(qr + k * 80 + lane);
        g[8] = get(qr + (lane & 7) * 80 + 64);
        bool ok = true;
#pragma unroll
        for (int k = 0; k < 9; k++) ok &= tag_ok(g[k], tag);
        if (__all(ok) || spin > SPIN_CAP) break;
      }
      L.q[lane] = bf_bits((val(g[0]) + val(g[2])) + (val(g[4]) + val(g[6])));
      L.q[64 + lane] = bf_bits((val(g[1]) + val(g[3])) + (val(g[5]) + val(g[7])));
      int tm = (int)((u32)g[8] - tstart);
      tm = min(tm, dppi<XOR1>(tm)); tm = min(tm, dppi<XOR2>(tm)); tm = min(tm, dppi<HMIRROR>(tm));
      if (lane == 0) { L.ctl[0] = tag; L.ctl[1] = tstart + (u32)min(tm, 0); }
    }
    wg_barrier();
    if (has_ffn) {
      const int unit = cg * 4 + ks;
      gu64* gu = (gu64*)(ws + WS_GU);
      const f32x2 mine = {(L.part[0][lane] + L.part[1][lane]) + (L.part[2][lane] + L.part[3][lane]),
                          (L.part[0][64 + lane] + L.part[1][64 + lane]) + (L.part[2][64 + lane] + L.part[3][64 + lane])};
      put64(gu + unit * 64 + lane, __builtin_bit_cast(u64, mine));
      drain();
      if (lane == 0) put32((gu32*)(ws + WS_FLAG + unit * 64), tag);
      const gu32* fl = (const gu32*)(ws + WS_FLAG + (dg * 8 + (lane & 7)) * 64);
      __builtin_amdgcn_s_sleep(64);
      for (u32 spin = 0; !__all(get32(fl) == tag) && spin < SPIN_CAP; spin++) __builtin_amdgcn_s_sleep(32);
      __builtin_amdgcn_fence(__ATOMIC_ACQUIRE, "wavefront");
      u64 pl[8];
#pragma unroll
      for (int k = 0; k < 8; k++) pl[k] = get(gu + (dg * 8 + k) * 64 + lane);
      asm volatile("" : "+v"(pl[0]), "+v"(pl[1]), "+v"(pl[2]), "+v"(pl[3]), "+v"(pl[4]), "+v"(pl[5]), "+v"(pl[6]), "+v"(pl[7]));
      auto pair = [&](int k) { return __builtin_bit_cast(f32x2, pl[k]); };
      const f32x2 lo = (pair(0) + pair(1)) + (pair(2) + pair(3)), hi = (pair(4) + pair(5)) + (pair(6) + pair(7));
      auto act = [](float g, float u) {
        g = rbf(g); u = rbf(u);
        const float inner = 0.7978845608028654f * (g + 0.044715f * g * g * g);
        return rbf(rbf(0.5f * g * (1.f + tanhf(inner))) * u);
      };
      L.h[lane] = act(lo.x, lo.y); L.h[64 + lane] = act(hi.x, hi.y);
    }
    {
#pragma unroll 1
      for (u32 spin = 0; !__all(lds_get(&L.kdone[lane & 3]) == tag) && spin < 64 * SPIN_CAP; spin++) __builtin_amdgcn_s_sleep(16);
      asm volatile("" ::: "memory");
      const float mine = L.mw[lane & 3];
      float mc = fmaxf(mine, dpp<XOR1>(mine)); mc = fmaxf(mc, dpp<XOR2>(mc));
      float s = 0.f;
      const float* yy = &L.y[0][0][lane];
#pragma unroll 8
      for (int t = 0; t < 256; t++) s += __builtin_amdgcn_exp2f(yy[t * 64] - mc);
      s = wave_sum(s) * 0.25f;
      gu64* ml = (gu64*)(ws + WS_ML);
      if (lane < 2) put(ml + b * 16 + lane, tag, lane == 0 ? mc : s);
      __builtin_amdgcn_s_sleep(127);
      u64 g0, g1;
      for (u32 spin = 0;; spin++) {
        g0 = get(ml + (head * 8 + (lane & 7)) * 16); g1 = get(ml + (head * 8 + (lane & 7)) * 16 + 1);
        if (__all(tag_ok(g0, tag) & tag_ok(g1, tag)) || spin > SPIN_CAP) break;
        __builtin_amdgcn_s_sleep(64);
      }
      float M = val(g0);
      M = fmaxf(M, dpp<XOR1>(M)); M = fmaxf(M, dpp<XOR2>(M)); M = fmaxf(M, dpp<HMIRROR>(M));
      float t = val(g1) * __builtin_amdgcn_exp2f(val(g0) - M);
      t += dpp<XOR1>(t); t += dpp<XOR2>(t); t += dpp<HMIRROR>(t);
      if (lane < 4) L.fw[lane] = __builtin_amdgcn_exp2f(mine - M) / t;
      lds_done();
      if (lane == 0) lds_put(&L.ctl[2], tag);
    }
    wg_barrier();
    {
      gu64* s1 = (gu64*)(ws + WS_S1) + (size_t)b * 128;
      put(s1 + lane, tag, (L.op[0][lane] + L.op[1][lane]) + (L.op[2][lane] + L.op[3][lane]));
      put(s1 + 64 + lane, tag, (L.op[0][64 + lane] + L.op[1][64 + lane]) + (L.op[2][64 + lane] + L.op[3][64 + lane]));
    }
    wg_barrier();
    gu64* s2 = (gu64*)(ws + WS_S2);
    put(s2 + (size_t)b * 128 + lane, tag, (L.qp[0][lane] + L.qp[1][lane]) + (L.qp[2][lane] + L.qp[3][lane]));
    put(s2 + (size_t)b * 128 + 64 + lane, tag, (L.qp[0][64 + lane] + L.qp[1][64 + lane]) + (L.qp[2][64 + lane] + L.qp[3][64 + lane]));
    if ((head & 3) == 0) {
      const gu64* sr = s2 + (size_t)(r * 8 + (fin >> 3)) * 128 + (fin & 7) * 16 + c;
      u64 g[8];
      __builtin_amdgcn_s_sleep(8);
      for (u32 spin = 0;; spin++) {
        bool ok = true;
#pragma unroll
        for (int k = 0; k < 8; k++) g[k] = get(sr + k * 4 * 8 * 128);
#pragma unroll
        for (int k = 0; k < 8; k++) ok &= tag_ok(g[k], tag);
        if (__all(ok) || spin > SPIN_CAP) break;
      }
      float a = ((val(g[0]) + val(g[1])) + (val(g[2]) + val(g[3]))) + ((val(g[4]) + val(g[5])) + (val(g[6]) + val(g[7])));
      a = xrow_sum(a) + bf_lo(xfin);
      if (lane < 16) ((GAS u16*)out)[16 * fin + lane] = bf_bits(a);
      if (b == 0 && lane == 0) put32((gu32*)(ws + WS_EPOCH), tag);
      if (b == 0) {
        const int e = (int)((u32)__builtin_amdgcn_s_memrealtime() - tstart);
        const int target = min(10000 + 5 * (e - 8000), 30000);
        while ((int)((u32)__builtin_amdgcn_s_memrealtime() - tstart) < target) __builtin_amdgcn_s_sleep(4);
      }
    }
    asm volatile("fd_code_helper_end:");
  }
}

}

extern "C" void kernel_launch(void* const* d_in, const int* in_sizes, int n_in, void* d_out, int out_size, void* d_ws, size_t ws_size,
                              hipStream_t stream) {
  (void)in_sizes; (void)n_in; (void)out_size; (void)ws_size;
  fused_decode<<<256, 320, 0, stream>>>((const u16*)d_in[0], (const u16*)d_in[1], (const u16*)d_in[2], (const u16*)d_in[3], (const u16*)d_in[4],
                                        (const u16*)d_in[6], (const u16*)d_in[7], (const u16*)d_in[8], (u16*)d_out, (unsigned char*)d_ws);
}
```

```cpp
#include <hip/hip_runtime.h>
#include <stdint.h>

namespace {

typedef unsigned u32;
typedef unsigned long long u64;
typedef unsigned short u16;
typedef u32 u32x4 __attribute__((ext_vector_type(4)));
typedef int i32x4 __attribute__((ext_vector_type(4)));
typedef float f32x2 __attribute__((ext_vector_type(2)));
typedef __attribute__((ext_vector_type(2))) __bf16 bf16x2;
#define GAS __attribute__((address_space(1)))
#define LAS __attribute__((address_space(3)))
typedef GAS u64 gu64;
typedef GAS u32 gu32;

constexpr int D = 1024, SK = 32768, DH = 128, FF = 3584;
constexpr u32 HEAD_BYTES = (u32)SK * DH * 2;

constexpr u32 PACE_T0 = 560;
constexpr u32 PACE_SLOW = 950, PACE_STEP = 888;
constexpr int PACE_NSLOW = 32;
constexpr u32 PACE_SLACK = 60 * 16;

constexpr size_t WS_EPOCH = 0;
constexpr size_t WS_FLAG = 16384;
constexpr size_t WS_ML = 32768;
constexpr size_t WS_QREC = 65536;
constexpr size_t WS_S1 = WS_QREC + 256 * 80 * 8;
constexpr size_t WS_S2 = WS_S1 + 256 * 128 * 8;
constexpr size_t WS_GU = WS_S2 + 256 * 128 * 8;
constexpr u32 SPIN_CAP = 1u << 16;

__device__ __forceinline__ float bf_lo(u32 v) { return __uint_as_float(v << 16); }
__device__ __forceinline__ float bf_hi(u32 v) { return __uint_as_float(v & 0xffff0000u); }
__device__ __forceinline__ u16 bf_bits(float x) { return __builtin_bit_cast(u16, (__bf16)x); }
__device__ __forceinline__ float rbf(float x) { return bf_lo((u32)bf_bits(x)); }
__device__ __forceinline__ float dot2(u32 a, u32 b, float c) {
  return __builtin_amdgcn_fdot2_f32_bf16(__builtin_bit_cast(bf16x2, a), __builtin_bit_cast(bf16x2, b), c, false);
}
template <int CTRL> __device__ __forceinline__ int dppi(int x) { return __builtin_amdgcn_mov_dpp(x, CTRL, 0xf, 0xf, true); }
template <int CTRL> __device__ __forceinline__ float dpp(float x) { return __int_as_float(dppi<CTRL>(__float_as_int(x))); }
constexpr int XOR1 = 0xB1, XOR2 = 0x4E, HMIRROR = 0x141, ROR8 = 0x128, ROR4 = 0x124, BCAST = 0x150;
__device__ __forceinline__ float swap32_add(float a, float b) {
  auto s = __builtin_amdgcn_permlane32_swap(__float_as_uint(a), __float_as_uint(b), false, false);
  return __uint_as_float(s[0]) + __uint_as_float(s[1]);
}
__device__ __forceinline__ float swap16_add(float a, float b) {
  auto s = __builtin_amdgcn_permlane16_swap(__float_as_uint(a), __float_as_uint(b), false, false);
  return __uint_as_float(s[0]) + __uint_as_float(s[1]);
}
__device__ __forceinline__ float xrow_sum(float x) { x = swap16_add(x, x); return swap32_add(x, x); }
__device__ __forceinline__ float xrow_max(float x) {
  auto s = __builtin_amdgcn_permlane16_swap(__float_as_uint(x), __float_as_uint(x), false, false);
  x = fmaxf(__uint_as_float(s[0]), __uint_as_float(s[1]));
  auto t = __builtin_amdgcn_permlane32_swap(__float_as_uint(x), __float_as_uint(x), false, false);
  return fmaxf(__uint_as_float(t[0]), __uint_as_float(t[1]));
}
__device__ __forceinline__ float wave_sum(float x) {
  x += dpp<XOR1>(x); x += dpp<XOR2>(x); x += dpp<ROR4>(x); x += dpp<ROR8>(x);
  return xrow_sum(x);
}
__device__ __forceinline__ f32x2 fold_rows(const float (&a)[8]) {
  float s0 = swap32_add(a[0], a[4]), s1 = swap32_add(a[1], a[5]), s2 = swap32_add(a[2], a[6]), s3 = swap32_add(a[3], a[7]);
  return f32x2{swap16_add(s0, s2), swap16_add(s1, s3)};
}
__device__ __forceinline__ float fold8(const float (&a)[8], bool b8) {
  const f32x2 s = fold_rows(a);
  return (b8 ? s.y : s.x) + dpp<ROR8>(b8 ? s.x : s.y);
}

__device__ __forceinline__ i32x4 mkr(const void* p, u32 n) {
  u64 a = (u64)p;
  i32x4 r; r.x = (int)(u32)a; r.y = (int)((u32)(a >> 32) & 0xffffu); r.z = (int)n; r.w = 0x00020000; return r;
}
__device__ __forceinline__ void ld_o(u32x4& d, u32 voff, i32x4 r, u32 soff) {
  asm volatile("buffer_load_dwordx4 %0, %1, %2, %3 offen nt" : "=v"(d) : "v"(voff), "s"(r), "s"(soff));
}
__device__ __forceinline__ void rows4_o(u32x4 (&R)[4], u32 voff, i32x4 r, u32 soff, u32 step) {
  ld_o(R[0], voff, r, soff); ld_o(R[1], voff, r, soff + step); ld_o(R[2], voff, r, soff + 2 * step); ld_o(R[3], voff, r, soff + 3 * step);
}
#define TILE_LOADS \
      "buffer_load_dwordx4 %0, %4, %5, %6 offen nt\n\t" \
      "buffer_load_dwordx4 %1, %4, %5, %6 offen offset:1024 nt\n\t" \
      "buffer_load_dwordx4 %2, %4, %5, %6 offen offset:2048 nt\n\t" \
      "buffer_load_dwordx4 %3, %4, %5, %6 offen offset:3072 nt"
__device__ __forceinline__ void tile_o(u32x4 (&R)[4], u32 voff, i32x4 r, u32 soff) {
  asm volatile(TILE_LOADS : "=&v"(R[0]), "=&v"(R[1]), "=&v"(R[2]), "=&v"(R[3]) : "v"(voff), "s"(r), "s"(soff));
}
__device__ __forceinline__ void tile(u32x4 (&R)[4], u32 voff, i32x4 r, u32 soff) {
  asm volatile(TILE_LOADS : "+v"(R[0]), "+v"(R[1]), "+v"(R[2]), "+v"(R[3]) : "v"(voff), "s"(r), "s"(soff));
}
template <int N> __device__ __forceinline__ void wait4(u32x4 (&R)[4]) {
  asm volatile("s_waitcnt vmcnt(%4)" : "+v"(R[0]), "+v"(R[1]), "+v"(R[2]), "+v"(R[3]) : "i"(N));
}
__device__ __forceinline__ void pin4(u32x4 (&R)[4]) { asm volatile("" : "+v"(R[0]), "+v"(R[1]), "+v"(R[2]), "+v"(R[3])); }
__device__ __forceinline__ void pin8(float (&a)[8]) {
  asm volatile("" : "+v"(a[0]), "+v"(a[1]), "+v"(a[2]), "+v"(a[3]), "+v"(a[4]), "+v"(a[5]), "+v"(a[6]), "+v"(a[7]));
}
__device__ __forceinline__ void wg_barrier() { asm volatile("s_waitcnt lgkmcnt(0)\n\ts_barrier" ::: "memory"); }
__device__ __forceinline__ void drain() { asm volatile("s_waitcnt vmcnt(0)" ::: "memory"); }
__device__ __forceinline__ void lds_done() { asm volatile("s_waitcnt lgkmcnt(0)" ::: "memory"); }
__device__ __forceinline__ u32 lds_get(const u32* p) { return __hip_atomic_load((const LAS u32*)p, __ATOMIC_RELAXED, __HIP_MEMORY_SCOPE_WORKGROUP); }
__device__ __forceinline__ void lds_put(u32* p, u32 v) { __hip_atomic_store((LAS u32*)p, v, __ATOMIC_RELAXED, __HIP_MEMORY_SCOPE_WORKGROUP); }

__device__ __forceinline__ void put64(gu64* g, u64 v) { __hip_atomic_store(g, v, __ATOMIC_RELAXED, __HIP_MEMORY_SCOPE_AGENT); }
__device__ __forceinline__ void put(gu64* g, u32 tag, float v) { put64(g, ((u64)tag << 32) | (u64)__float_as_uint(v)); }
__device__ __forceinline__ u64 get(const gu64* g) { return __hip_atomic_load(g, __ATOMIC_RELAXED, __HIP_MEMORY_SCOPE_AGENT); }
__device__ __forceinline__ void put32(gu32* g, u32 v) { __hip_atomic_store(g, v, __ATOMIC_RELAXED, __HIP_MEMORY_SCOPE_AGENT); }
__device__ __forceinline__ u32 get32(const gu32* g) { return __hip_atomic_load(g, __ATOMIC_RELAXED, __HIP_MEMORY_SCOPE_AGENT); }
__device__ __forceinline__ bool tag_ok(u64 x, u32 tag) { return (u32)(x >> 32) == tag; }
__device__ __forceinline__ float val(u64 x) { return __uint_as_float((u32)x); }

template <int LN> __device__ __forceinline__ void gemv_step(float (&acc)[8], float xv, u32x4 w) {
  const float xb = dpp<BCAST + LN>(xv);
#pragma unroll
  for (int k = 0; k < 4; k++) { acc[2 * k] = fmaf(xb, bf_lo(w[k]), acc[2 * k]); acc[2 * k + 1] = fmaf(xb, bf_hi(w[k]), acc[2 * k + 1]); }
}
template <int LN, int ST> __device__ __forceinline__ void gemv4(float (&acc)[8], float xv, u32x4 (&w)[4]) {
  gemv_step<LN>(acc, xv, w[0]); gemv_step<LN + ST>(acc, xv, w[1]); gemv_step<LN + 2 * ST>(acc, xv, w[2]); gemv_step<LN + 3 * ST>(acc, xv, w[3]);
}
template <int LN> __device__ __forceinline__ void gemv4_bf(float (&acc)[8], float xv, u32x4 (&w)[4]) {
  const int hi = __float_as_int(xv), lo = (int)((u32)hi >> 16);
  auto one = [&](u32 xl, u32 xh, u32x4 v) {
#pragma unroll
    for (int k = 0; k < 4; k++) { acc[2 * k] = dot2(xl, v[k], acc[2 * k]); acc[2 * k + 1] = dot2(xh, v[k], acc[2 * k + 1]); }
  };
  one(dppi<BCAST + LN>(lo), dppi<BCAST + LN>(hi), w[0]); one(dppi<BCAST + LN + 1>(lo), dppi<BCAST + LN + 1>(hi), w[1]);
  one(dppi<BCAST + LN + 2>(lo), dppi<BCAST + LN + 2>(hi), w[2]); one(dppi<BCAST + LN + 3>(lo), dppi<BCAST + LN + 3>(hi), w[3]);
}
__device__ __forceinline__ void gemv8(float (&acc)[8], const float* xv, u32x4 (&w)[4]) {
#pragma unroll
  for (int i = 0; i < 4; i++)
#pragma unroll
    for (int k = 0; k < 4; k++) { acc[2 * k] = fmaf(xv[i], bf_lo(w[i][k]), acc[2 * k]); acc[2 * k + 1] = fmaf(xv[i], bf_hi(w[i][k]), acc[2 * k + 1]); }
}

__device__ __forceinline__ void warm_code(int lane) {
  u64 pc; u32 back, n0, n1;
  asm volatile("s_getpc_b64 %0\n"
               "fd_here:\n\t"
               "s_mov_b32 %1, fd_here - fd_code_begin\n\t"
               "s_mov_b32 %2, fd_code_stream_end - fd_code_begin\n\t"
               "s_mov_b32 %3, fd_code_helper_end - fd_code_begin"
               : "=s"(pc), "=s"(back), "=s"(n0), "=s"(n1));
  pc -= back;
  const u32 n = max(n0, n1);
  u32 v[5];
#pragma unroll
  for (int k = 0; k < 5; k++) v[k] = *(const GAS u32*)(pc + (min(lane * 64u + k * 4096u, n - 4u) & ~3u));
  asm volatile("" :: "v"(v[0]), "v"(v[1]), "v"(v[2]), "v"(v[3]), "v"(v[4]));
}

struct Lds {
  float y[4][64][64];
  float part[4][128];
  float qp[4][128];
  float op[4][128];
  float h[128];
  float mw[4];
  float fw[4];
  u32 kdone[4];
  u16 q[128];
  u32 ctl[4];
};

__global__ void __launch_bounds__(320, 2)
fused_decode(const u16* __restrict__ x, const u16* __restrict__ kc, const u16* __restrict__ vc, const u16* __restrict__ wq,
             const u16* __restrict__ wo, const u16* __restrict__ wg, const u16* __restrict__ wu, const u16* __restrict__ wd,
             u16* __restrict__ out, unsigned char* __restrict__ ws) {
  __shared__ __attribute__((aligned(16))) Lds L;
  asm volatile("fd_code_begin:");
  const int b = blockIdx.x, head = b >> 3, j = b & 7;
  const int lane = threadIdx.x & 63, wave = __builtin_amdgcn_readfirstlane(threadIdx.x >> 6);
  const int r = lane >> 4, c = lane & 15;
  const bool has_ffn = j != (head & 7);
  const int ks = j >> 1, jb = j & 1, hr = head & 7;
  const int cg = 2 * head + jb - 2 * (head >> 3) - ((hr > 2 * ks || (hr == 2 * ks && jb)) ? 1 : 0) - (hr > 2 * ks + 1 ? 1 : 0);
  const int dg = head - ((head - j + 7) >> 3);

  if (wave < 4) {
    const int w = wave;
    u32x4 X0, X1, XS;
    u32x4 P0[4], P1[4], P2[4], P3[4], R0[4], R1[4], R2[4], R3[4];
    const i32x4 rx = mkr(x, D * 2);
    const u32 voff = lane * 16;
    const int r8 = lane >> 3, c8 = lane & 7;
    const u32 row0 = ks * 256 + 64 * w;
    {
      const u32 so = (row0 + 8 * r8) * 2;
      asm volatile("buffer_load_dwordx4 %0, %1, %2, 0 offen" : "=v"(X0) : "v"(voff), "s"(rx));
      asm volatile("buffer_load_dwordx4 %0, %1, %2, 0 offen offset:1024" : "=v"(X1) : "v"(voff), "s"(rx));
      asm volatile("buffer_load_dwordx4 %0, %1, %2, 0 offen" : "=v"(XS) : "v"(so), "s"(rx));
    }
    {
      const i32x4 rq = mkr(wq, (u32)D * 4096 * 2);
      const u32 vo = r8 * 8 * 8192 + c8 * 16, s0 = row0 * 8192 + head * 256 + jb * 128;
      rows4_o(P0, vo, rq, s0, 8192); rows4_o(P1, vo, rq, s0 + 4 * 8192, 8192);
    }
    wg_barrier();
    const u32 nffn = has_ffn ? (u32)D * FF * 2 : 0u;
    const i32x4 rg = mkr(wg, nffn), ru = mkr(wu, nffn);
    constexpr u32 GSTEP = FF * 2;
    const u32 vg = r8 * 8 * (FF * 2) + c8 * 16, g0 = row0 * (FF * 2) + cg * 128;
    const i32x4 rk = mkr((const char*)kc + (size_t)head * HEAD_BYTES, HEAD_BYTES), rv = mkr((const char*)vc + (size_t)head * HEAD_BYTES, HEAD_BYTES);
    const u32 t0 = (u32)(j * 256 + w) * 4096;
    asm volatile("s_waitcnt vmcnt(8)" : "+v"(X0), "+v"(X1), "+v"(XS));
    float xn[8];
    {
      float v[16];
#pragma unroll
      for (int k = 0; k < 4; k++) { v[2 * k] = bf_lo(X0[k]); v[2 * k + 1] = bf_hi(X0[k]); v[8 + 2 * k] = bf_lo(X1[k]); v[9 + 2 * k] = bf_hi(X1[k]); }
      float s = 0.f;
#pragma unroll
      for (int k = 0; k < 16; k++) s += v[k];
      const float mean = wave_sum(s) * (1.0f / D);
      float q = 0.f;
#pragma unroll
      for (int k = 0; k < 16; k++) { const float d = v[k] - mean; q = fmaf(d, d, q); }
      const float var = wave_sum(q) * (1.0f / D);
      const float den = rbf(sqrtf(rbf(rbf(var) + 1e-5f)));
      const float mb = rbf(mean);
#pragma unroll
      for (int k = 0; k < 4; k++) { xn[2 * k] = rbf(rbf(bf_lo(XS[k]) - mb) / den); xn[2 * k + 1] = rbf(rbf(bf_hi(XS[k]) - mb) / den); }
    }
    pin8(xn);
    const bool b8 = (c & 8) != 0, b4 = (c & 4) != 0;
    {
      float a[8] = {0.f, 0.f, 0.f, 0.f, 0.f, 0.f, 0.f, 0.f};
      wait4<4>(P0); rows4_o(P2, vg, rg, g0, GSTEP); pin4(P0);
      gemv8(a, xn, P0); pin8(a);
      wait4<4>(P1); rows4_o(P3, vg, rg, g0 + 4 * GSTEP, GSTEP); pin4(P1);
      gemv8(a, xn + 4, P1);
      L.qp[w][8 * c8 + r8] = fold8(a, b8);
    }
    wg_barrier();
    {
      float a[8] = {0.f, 0.f, 0.f, 0.f, 0.f, 0.f, 0.f, 0.f};
      wait4<4>(P2); rows4_o(P0, vg, ru, g0, GSTEP); pin4(P2);
      gemv8(a, xn, P2); pin8(a);
      wait4<4>(P3); rows4_o(P1, vg, ru, g0 + 4 * GSTEP, GSTEP); pin4(P3);
      gemv8(a, xn + 4, P3);
      L.part[w][8 * c8 + r8] = fold8(a, b8);
    }
    {
      float a[8] = {0.f, 0.f, 0.f, 0.f, 0.f, 0.f, 0.f, 0.f};
      wait4<4>(P0); tile_o(R0, voff, rk, t0); pin4(P0);
      gemv8(a, xn, P0); pin8(a);
      wait4<4>(P1); tile_o(R1, voff, rk, t0 + 16384); pin4(P1);
      gemv8(a, xn + 4, P1);
      L.part[w][64 + 8 * c8 + r8] = fold8(a, b8);
    }
    wait4<4>(R0); tile_o(R2, voff, rk, t0 + 2 * 16384);
    wait4<4>(R1); tile_o(R3, voff, rk, t0 + 3 * 16384);
    wg_barrier();
    const u32x4 q = *(const u32x4*)&L.q[8 * c];
    const u32 tag = __builtin_amdgcn_readfirstlane(L.ctl[0]);
    const u32 tref = __builtin_amdgcn_readfirstlane(L.ctl[1]);

    float m = -INFINITY, acc[8] = {0.f, 0.f, 0.f, 0.f, 0.f, 0.f, 0.f, 0.f};
    u32 due16 = (tref << 4) + PACE_T0 * 16 + w * (PACE_STEP / 4);
    auto clock16 = [&]() -> u32 { return (u32)__builtin_amdgcn_s_memrealtime() << 4; };
    auto pace = [&](u32 early, u32 step, u32 slack) {
      if ((int)(early - due16) < 0) {
#pragma unroll 1
        while ((int)(clock16() - due16) < 0) __builtin_amdgcn_s_sleep(1);
      } else if (early - due16 > slack) due16 = early - slack;
      due16 += step;
    };
    auto kstep = [&](u32x4 (&R)[4], int n) {
      i32x4 rr = rk;
      rr.x = n + 4 < 64 ? rk.x : rv.x; rr.y = n + 4 < 64 ? rk.y : rv.y;
      u32 soff = t0 + (u32)((n + 4) & 63) * 16384;
      u32 early = clock16();
      asm volatile("" : "+s"(rr), "+s"(soff), "+s"(early));
      wait4<12>(R);
      float d[4];
#pragma unroll
      for (int u = 0; u < 4; u++) d[u] = dot2(R[u][3], q[3], dot2(R[u][2], q[2], dot2(R[u][1], q[1], dot2(R[u][0], q[0], 0.f))));
      const float e0 = (b8 ? d[2] : d[0]) + dpp<ROR8>(b8 ? d[0] : d[2]);
      const float e1 = (b8 ? d[3] : d[1]) + dpp<ROR8>(b8 ? d[1] : d[3]);
      float f = (b4 ? e1 : e0) + dpp<HMIRROR>(b4 ? e0 : e1);
      f += dpp<XOR1>(f); f += dpp<XOR2>(f);
      const float y = rbf(rbf(f) * 0.08838834764831845f) * 1.4426950408889634f;
      L.y[w][n][lane] = y;
      m = fmaxf(m, y);
      asm volatile("" : "+v"(m));
      pace(early, n < PACE_NSLOW ? PACE_SLOW : PACE_STEP, n < PACE_NSLOW ? 0x7fffffffu : PACE_SLACK);
      tile(R, voff, rr, soff);
    };
    auto vload = [&](int n) -> float { return __builtin_amdgcn_exp2f(L.y[w][n][lane] - m); };
    auto vstep = [&](u32x4 (&R)[4], int n) {
      u32 soff = t0 + (u32)(n + 4) * 16384;
      u32 early = clock16();
      float p = vload(n);
      asm volatile("" : "+s"(soff), "+v"(p), "+s"(early));
      wait4<12>(R);
      gemv4<0, 4>(acc, p, R);
      pin8(acc);
      pace(early, PACE_STEP, PACE_SLACK);
      tile(R, voff, rv, soff);
    };
#pragma unroll 1
    for (int n = 0; n < 64; n += 4) { kstep(R0, n); kstep(R1, n + 1); kstep(R2, n + 2); kstep(R3, n + 3); }
    m = fmaxf(m, dpp<ROR4>(m)); m = fmaxf(m, dpp<ROR8>(m)); m = xrow_max(m);
    if (lane == 0) L.mw[w] = m;
    lds_done();
    if (lane == 0) lds_put(&L.kdone[w], tag);
#pragma unroll 1
    for (int n = 0; n < 60; n += 4) { vstep(R0, n); vstep(R1, n + 1); vstep(R2, n + 2); vstep(R3, n + 3); }
    u32x4 W0[4], W1[4], D0[4], D1[4];
    {
      const float p0 = vload(60), p1 = vload(61), p2 = vload(62), p3 = vload(63);
#pragma unroll 1
      for (u32 spin = 0; lds_get(&L.ctl[2]) != tag && spin < 64 * SPIN_CAP; spin++) __builtin_amdgcn_s_sleep(2);
      asm volatile("" ::: "memory");
      const float fs = L.fw[w];
      wait4<12>(R0); gemv4<0, 4>(acc, p0, R0);
      wait4<8>(R1); gemv4<0, 4>(acc, p1, R1);
      pin8(acc);
      const u32 wvo = (16 * (r & 1) + 8 * (r >> 1)) * 2048 + c * 16;
      const i32x4 rwo = mkr(wo, (u32)4096 * D * 2), rd = mkr(wd, has_ffn ? (u32)FF * D * 2 : 0u);
      const u32 wso = (u32)(head * 128 + 32 * w) * 2048 + j * 256, dso = (u32)(dg * 128 + 32 * w) * 2048 + j * 256;
      due16 += 160 - w * (PACE_STEP / 4);
      pace(due16 - 1, 0, 0);
      rows4_o(D0, wvo, rd, dso, 2048); rows4_o(D1, wvo, rd, dso + 8192, 2048);
      rows4_o(W0, wvo, rwo, wso, 2048); rows4_o(W1, wvo, rwo, wso + 8192, 2048);
      wait4<20>(R2); gemv4<0, 4>(acc, p2, R2);
      wait4<16>(R3); gemv4<0, 4>(acc, p3, R3);
      *(f32x2*)&L.op[w][8 * c + 2 * r] = fold_rows(acc) * fs;
    }
    wg_barrier();
    {
      const float hv = has_ffn ? L.h[32 * w + 16 * (r & 1) + 8 * (r >> 1) + (c & 7)] : 0.f;
      float a[8] = {0.f, 0.f, 0.f, 0.f, 0.f, 0.f, 0.f, 0.f};
      wait4<12>(D0); gemv4_bf<0>(a, hv, D0);
      wait4<8>(D1); gemv4_bf<4>(a, hv, D1);
      pin8(a);
      wait4<0>(W0); wait4<0>(W1);
      const gu64* s1 = (const gu64*)(ws + WS_S1) + (size_t)(head * 8 + (lane >> 5)) * 128 + 32 * w + (lane & 31);
      u64 g[4];
      for (u32 spin = 0;; spin++) {
#pragma unroll
        for (int k = 0; k < 4; k++) g[k] = get(s1 + k * 256);
        if (__all(tag_ok(g[0], tag) & tag_ok(g[1], tag) & tag_ok(g[2], tag) & tag_ok(g[3], tag)) || spin > SPIN_CAP) break;
      }
      float o = (val(g[0]) + val(g[1])) + (val(g[2]) + val(g[3]));
      o = rbf(swap32_add(o, o));
      const float orot = dpp<ROR8>(o);
      o = (r & 2) ? orot : o;
      gemv4_bf<0>(a, o, W0); gemv4_bf<4>(a, o, W1);
      *(f32x2*)&L.qp[w][8 * c + 2 * r] = fold_rows(a);
    }
    wg_barrier();
    asm volatile("fd_code_stream_end:");
  } else {
    const u32 tstart = (u32)__builtin_amdgcn_s_memrealtime();
    wg_barrier();
    const u32 ep = get32((gu32*)(ws + WS_EPOCH));
    const int fin = (head >> 2) * 8 + j;
    const u32 xfin = ((const GAS u16*)x)[16 * fin + c];
    {
      const int e = lane & 1, s = (lane >> 1) & 3;
      const size_t gu = (size_t)(ks * 256 + e * 255) * FF + (has_ffn ? cg : 0) * 64;
      const u16* p = s == 0 ? wg + gu : s == 1 ? wu + gu : kc + (size_t)head * SK * DH + (size_t)j * 4096 * DH + e * (4096 * DH - 2);
      const u32 t = __builtin_nontemporal_load((const GAS u32*)p);
      asm volatile("" :: "v"(t));
    }
    if (head < 4) warm_code(lane);
    const u32 tag = __builtin_amdgcn_readfirstlane(ep) + 1u;
    gu64* qrec = (gu64*)(ws + WS_QREC);
    if (lane == 0) put(qrec + (size_t)b * 80 + 64, tag, __uint_as_float(tstart));
    if (lane < 4) L.kdone[lane] = ~tag;
    if (lane == 0) L.ctl[2] = ~tag;
    wg_barrier();
    {
      put(qrec + (size_t)b * 80 + lane, tag, (L.qp[0][lane] + L.qp[1][lane]) + (L.qp[2][lane] + L.qp[3][lane]));
      const gu64* qr = qrec + (size_t)head * 8 * 80;
      __builtin_amdgcn_s_sleep(8);
      u64 g[9];
      for (u32 spin = 0;; spin++) {
#pragma unroll
        for (int k = 0; k < 8; k++) g[k] = get(qr + k * 80 + lane);
        g[8] = get(qr + (lane & 7) * 80 + 64);
        bool ok = true;
#pragma unroll
        for (int k = 0; k < 9; k++) ok &= tag_ok(g[k], tag);
        if (__all(ok) || spin > SPIN_CAP) break;
      }
      L.q[lane] = bf_bits((val(g[0]) + val(g[2])) + (val(g[4]) + val(g[6])));
      L.q[64 + lane] = bf_bits((val(g[1]) + val(g[3])) + (val(g[5]) + val(g[7])));
      int tm = (int)((u32)g[8] - tstart);
      tm = min(tm, dppi<XOR1>(tm)); tm = min(tm, dppi<XOR2>(tm)); tm = min(tm, dppi<HMIRROR>(tm));
      if (lane == 0) { L.ctl[0] = tag; L.ctl[1] = tstart + (u32)min(tm, 0); }
    }
    wg_barrier();
    {
      const int e = lane & 1, s = (lane >> 1) & 3;
      const u16* p = s == 0 ? vc + (size_t)head * SK * DH + (size_t)j * 4096 * DH + e * (4096 * DH - 2) :
                     s == 1 ? wo + (size_t)(head * 128 + e * 127) * D + j * 128 :
                     s == 2 ? wd + (size_t)((has_ffn ? dg : 0) * 128 + e * 127) * D + j * 128 : out + 16 * fin;
      const u32 t = __builtin_nontemporal_load((const GAS u32*)p);
      asm volatile("" :: "v"(t));
    }
    if (has_ffn) {
      const int unit = cg * 4 + ks;
      gu64* gu = (gu64*)(ws + WS_GU);
      const f32x2 mine = {(L.part[0][lane] + L.part[1][lane]) + (L.part[2][lane] + L.part[3][lane]),
                          (L.part[0][64 + lane] + L.part[1][64 + lane]) + (L.part[2][64 + lane] + L.part[3][64 + lane])};
      put64(gu + unit * 64 + lane, __builtin_bit_cast(u64, mine));
      drain();
      if (lane == 0) put32((gu32*)(ws + WS_FLAG + unit * 64), tag);
      const gu32* fl = (const gu32*)(ws + WS_FLAG + (dg * 8 + (lane & 7)) * 64);
      __builtin_amdgcn_s_sleep(64);
      for (u32 spin = 0; !__all(get32(fl) == tag) && spin < SPIN_CAP; spin++) __builtin_amdgcn_s_sleep(32);
      __builtin_amdgcn_fence(__ATOMIC_ACQUIRE, "wavefront");
      u64 pl[8];
#pragma unroll
      for (int k = 0; k < 8; k++) pl[k] = get(gu + (dg * 8 + k) * 64 + lane);
      asm volatile("" : "+v"(pl[0]), "+v"(pl[1]), "+v"(pl[2]), "+v"(pl[3]), "+v"(pl[4]), "+v"(pl[5]), "+v"(pl[6]), "+v"(pl[7]));
      auto pair = [&](int k) { return __builtin_bit_cast(f32x2, pl[k]); };
      const f32x2 lo = (pair(0) + pair(1)) + (pair(2) + pair(3)), hi = (pair(4) + pair(5)) + (pair(6) + pair(7));
      auto act = [](float g, float u) {
        g = rbf(g); u = rbf(u);
        const float inner = 0.7978845608028654f * (g + 0.044715f * g * g * g);
        return rbf(rbf(0.5f * g * (1.f + tanhf(inner))) * u);
      };
      L.h[lane] = act(lo.x, lo.y); L.h[64 + lane] = act(hi.x, hi.y);
    }
    {
#pragma unroll 1
      for (u32 spin = 0; !__all(lds_get(&L.kdone[lane & 3]) == tag) && spin < 64 * SPIN_CAP; spin++) __builtin_amdgcn_s_sleep(16);
      asm volatile("" ::: "memory");
      const float mine = L.mw[lane & 3];
      float mc = fmaxf(mine, dpp<XOR1>(mine)); mc = fmaxf(mc, dpp<XOR2>(mc));
      float s = 0.f;
      const float* yy = &L.y[0][0][lane];
#pragma unroll 8
      for (int t = 0; t < 256; t++) s += __builtin_amdgcn_exp2f(yy[t * 64] - mc);
      s = wave_sum(s) * 0.25f;
      gu64* ml = (gu64*)(ws + WS_ML);
      if (lane < 2) put(ml + b * 16 + lane, tag, lane == 0 ? mc : s);
      __builtin_amdgcn_s_sleep(127);
      u64 g0, g1;
      for (u32 spin = 0;; spin++) {
        g0 = get(ml + (head * 8 + (lane & 7)) * 16); g1 = get(ml + (head * 8 + (lane & 7)) * 16 + 1);
        if (__all(tag_ok(g0, tag) & tag_ok(g1, tag)) || spin > SPIN_CAP) break;
        __builtin_amdgcn_s_sleep(64);
      }
      float M = val(g0);
      M = fmaxf(M, dpp<XOR1>(M)); M = fmaxf(M, dpp<XOR2>(M)); M = fmaxf(M, dpp<HMIRROR>(M));
      float t = val(g1) * __builtin_amdgcn_exp2f(val(g0) - M);
      t += dpp<XOR1>(t); t += dpp<XOR2>(t); t += dpp<HMIRROR>(t);
      if (lane < 4) L.fw[lane] = __builtin_amdgcn_exp2f(mine - M) / t;
      lds_done();
      if (lane == 0) lds_put(&L.ctl[2], tag);
    }
    wg_barrier();
    {
      gu64* s1 = (gu64*)(ws + WS_S1) + (size_t)b * 128;
      put(s1 + lane, tag, (L.op[0][lane] + L.op[1][lane]) + (L.op[2][lane] + L.op[3][lane]));
      put(s1 + 64 + lane, tag, (L.op[0][64 + lane] + L.op[1][64 + lane]) + (L.op[2][64 + lane] + L.op[3][64 + lane]));
    }
    wg_barrier();
    gu64* s2 = (gu64*)(ws + WS_S2);
    put(s2 + (size_t)b * 128 + lane, tag, (L.qp[0][lane] + L.qp[1][lane]) + (L.qp[2][lane] + L.qp[3][lane]));
    put(s2 + (size_t)b * 128 + 64 + lane, tag, (L.qp[0][64 + lane] + L.qp[1][64 + lane]) + (L.qp[2][64 + lane] + L.qp[3][64 + lane]));
    if ((head & 3) == 0) {
      const gu64* sr = s2 + (size_t)(r * 8 + (fin >> 3)) * 128 + (fin & 7) * 16 + c;
      u64 g[8];
      __builtin_amdgcn_s_sleep(8);
      for (u32 spin = 0;; spin++) {
        bool ok = true;
#pragma unroll
        for (int k = 0; k < 8; k++) g[k] = get(sr + k * 4 * 8 * 128);
#pragma unroll
        for (int k = 0; k < 8; k++) ok &= tag_ok(g[k], tag);
        if (__all(ok) || spin > SPIN_CAP) break;
      }
      float a = ((val(g[0]) + val(g[1])) + (val(g[2]) + val(g[3]))) + ((val(g[4]) + val(g[5])) + (val(g[6]) + val(g[7])));
      a = xrow_sum(a) + bf_lo(xfin);
      if (lane < 16) ((GAS u16*)out)[16 * fin + lane] = bf_bits(a);
      if (b == 0 && lane == 0) put32((gu32*)(ws + WS_EPOCH), tag);
      if (b == 0) {
        const int e = (int)((u32)__builtin_amdgcn_s_memrealtime() - tstart);
        const int target = min(10000 + 5 * (e - 8000), 30000);
        while ((int)((u32)__builtin_amdgcn_s_memrealtime() - tstart) < target) __builtin_amdgcn_s_sleep(4);
      }
    }
    asm volatile("fd_code_helper_end:");
  }
}

}

extern "C" void kernel_launch(void* const* d_in, const int* in_sizes, int n_in, void* d_out, int out_size, void* d_ws, size_t ws_size,
                              hipStream_t stream) {
  (void)in_sizes; (void)n_in; (void)out_size; (void)ws_size;
  fused_decode<<<256, 320, 0, stream>>>((const u16*)d_in[0], (const u16*)d_in[1], (const u16*)d_in[2], (const u16*)d_in[3], (const u16*)d_in[4],
                                        (const u16*)d_in[6], (const u16*)d_in[7], (const u16*)d_in[8], (u16*)d_out, (unsigned char*)d_ws);
}
```

```cpp
#include <hip/hip_runtime.h>
#include <stdint.h>

namespace {

typedef unsigned u32;
typedef unsigned long long u64;
typedef unsigned short u16;
typedef u32 u32x4 __attribute__((ext_vector_type(4)));
typedef int i32x4 __attribute__((ext_vector_type(4)));
typedef float f32x2 __attribute__((ext_vector_type(2)));
typedef __attribute__((ext_vector_type(2))) __bf16 bf16x2;
#define GAS __attribute__((address_space(1)))
#define LAS __attribute__((address_space(3)))
typedef GAS u64 gu64;
typedef GAS u32 gu32;

constexpr int D = 1024, SK = 32768, DH = 128, FF = 3584;
constexpr u32 HEAD_BYTES = (u32)SK * DH * 2;

constexpr u32 PACE_T0 = 640;
constexpr u32 PACE_SLOW = 900, PACE_STEP = 854;
constexpr int PACE_NSLOW = 24;
constexpr u32 PACE_TAIL = 600;

constexpr int STEER_LATE = 60 * 16, STEER_KEEP = 50 * 16, STEER_MAX = 2000 * 16;
constexpr int STEER_FROM = 2100;

constexpr size_t WS_EPOCH = 0;
constexpr size_t WS_LAG = 8192;
constexpr size_t WS_FLAG = 16384;
constexpr size_t WS_ML = 32768;
constexpr size_t WS_QREC = 65536;
constexpr size_t WS_S1 = WS_QREC + 256 * 80 * 8;
constexpr size_t WS_S2 = WS_S1 + 256 * 128 * 8;
constexpr size_t WS_GU = WS_S2 + 256 * 128 * 8;
constexpr u32 SPIN_CAP = 1u << 16;

__device__ __forceinline__ float bf_lo(u32 v) { return __uint_as_float(v << 16); }
__device__ __forceinline__ float bf_hi(u32 v) { return __uint_as_float(v & 0xffff0000u); }
__device__ __forceinline__ u16 bf_bits(float x) { return __builtin_bit_cast(u16, (__bf16)x); }
__device__ __forceinline__ float rbf(float x) { return bf_lo((u32)bf_bits(x)); }
__device__ __forceinline__ float dot2(u32 a, u32 b, float c) {
  return __builtin_amdgcn_fdot2_f32_bf16(__builtin_bit_cast(bf16x2, a), __builtin_bit_cast(bf16x2, b), c, false);
}
template <int CTRL> __device__ __forceinline__ int dppi(int x) { return __builtin_amdgcn_mov_dpp(x, CTRL, 0xf, 0xf, true); }
template <int CTRL> __device__ __forceinline__ float dpp(float x) { return __int_as_float(dppi<CTRL>(__float_as_int(x))); }
constexpr int XOR1 = 0xB1, XOR2 = 0x4E, HMIRROR = 0x141, ROR8 = 0x128, ROR4 = 0x124, BCAST = 0x150;
__device__ __forceinline__ float swap32_add(float a, float b) {
  auto s = __builtin_amdgcn_permlane32_swap(__float_as_uint(a), __float_as_uint(b), false, false);
  return __uint_as_float(s[0]) + __uint_as_float(s[1]);
}
__device__ __forceinline__ float swap16_add(float a, float b) {
  auto s = __builtin_amdgcn_permlane16_swap(__float_as_uint(a), __float_as_uint(b), false, false);
  return __uint_as_float(s[0]) + __uint_as_float(s[1]);
}
__device__ __forceinline__ float xrow_sum(float x) { x = swap16_add(x, x); return swap32_add(x, x); }
__device__ __forceinline__ float xrow_max(float x) {
  auto s = __builtin_amdgcn_permlane16_swap(__float_as_uint(x), __float_as_uint(x), false, false);
  x = fmaxf(__uint_as_float(s[0]), __uint_as_float(s[1]));
  auto t = __builtin_amdgcn_permlane32_swap(__float_as_uint(x), __float_as_uint(x), false, false);
  return fmaxf(__uint_as_float(t[0]), __uint_as_float(t[1]));
}
__device__ __forceinline__ float wave_sum(float x) {
  x += dpp<XOR1>(x); x += dpp<XOR2>(x); x += dpp<ROR4>(x); x += dpp<ROR8>(x);
  return xrow_sum(x);
}
__device__ __forceinline__ f32x2 fold_rows(const float (&a)[8]) {
  float s0 = swap32_add(a[0], a[4]), s1 = swap32_add(a[1], a[5]), s2 = swap32_add(a[2], a[6]), s3 = swap32_add(a[3], a[7]);
  return f32x2{swap16_add(s0, s2), swap16_add(s1, s3)};
}
__device__ __forceinline__ float fold8(const float (&a)[8], bool b8) {
  const f32x2 s = fold_rows(a);
  return (b8 ? s.y : s.x) + dpp<ROR8>(b8 ? s.x : s.y);
}

__device__ __forceinline__ i32x4 mkr(const void* p, u32 n) {
  u64 a = (u64)p;
  i32x4 r; r.x = (int)(u32)a; r.y = (int)((u32)(a >> 32) & 0xffffu); r.z = (int)n; r.w = 0x00020000; return r;
}
__device__ __forceinline__ void ld_o(u32x4& d, u32 voff, i32x4 r, u32 soff) {
  asm volatile("buffer_load_dwordx4 %0, %1, %2, %3 offen nt" : "=v"(d) : "v"(voff), "s"(r), "s"(soff));
}
__device__ __forceinline__ void rows4_o(u32x4 (&R)[4], u32 voff, i32x4 r, u32 soff, u32 step) {
  ld_o(R[0], voff, r, soff); ld_o(R[1], voff, r, soff + step); ld_o(R[2], voff, r, soff + 2 * step); ld_o(R[3], voff, r, soff + 3 * step);
}
#define TILE_LOADS \
      "buffer_load_dwordx4 %0, %4, %6, %7 offen nt\n\t" \
      "buffer_load_dwordx4 %1, %4, %6, %7 offen offset:1024 nt\n\t" \
      "buffer_load_dwordx4 %2, %5, %6, %7 offen nt\n\t" \
      "buffer_load_dwordx4 %3, %5, %6, %7 offen offset:1024 nt"
__device__ __forceinline__ void tile_o(u32x4 (&R)[4], u32 vlo, u32 vhi, i32x4 r, u32 soff) {
  asm volatile(TILE_LOADS : "=&v"(R[0]), "=&v"(R[1]), "=&v"(R[2]), "=&v"(R[3]) : "v"(vlo), "v"(vhi), "s"(r), "s"(soff));
}
__device__ __forceinline__ void tile(u32x4 (&R)[4], u32 vlo, u32 vhi, i32x4 r, u32 soff) {
  asm volatile(TILE_LOADS : "+v"(R[0]), "+v"(R[1]), "+v"(R[2]), "+v"(R[3]) : "v"(vlo), "v"(vhi), "s"(r), "s"(soff));
}
template <int N> __device__ __forceinline__ void wait4(u32x4 (&R)[4]) {
  asm volatile("s_waitcnt vmcnt(%4)" : "+v"(R[0]), "+v"(R[1]), "+v"(R[2]), "+v"(R[3]) : "i"(N));
}
__device__ __forceinline__ void pin4(u32x4 (&R)[4]) { asm volatile("" : "+v"(R[0]), "+v"(R[1]), "+v"(R[2]), "+v"(R[3])); }
__device__ __forceinline__ void pin8(float (&a)[8]) {
  asm volatile("" : "+v"(a[0]), "+v"(a[1]), "+v"(a[2]), "+v"(a[3]), "+v"(a[4]), "+v"(a[5]), "+v"(a[6]), "+v"(a[7]));
}
__device__ __forceinline__ void wg_barrier() { asm volatile("s_waitcnt lgkmcnt(0)\n\ts_barrier" ::: "memory"); }
__device__ __forceinline__ void drain() { asm volatile("s_waitcnt vmcnt(0)" ::: "memory"); }
__device__ __forceinline__ void lds_done() { asm volatile("s_waitcnt lgkmcnt(0)" ::: "memory"); }
__device__ __forceinline__ u32 lds_get(const u32* p) { return __hip_atomic_load((const LAS u32*)p, __ATOMIC_RELAXED, __HIP_MEMORY_SCOPE_WORKGROUP); }
__device__ __forceinline__ void lds_put(u32* p, u32 v) { __hip_atomic_store((LAS u32*)p, v, __ATOMIC_RELAXED, __HIP_MEMORY_SCOPE_WORKGROUP); }

__device__ __forceinline__ void put64(gu64* g, u64 v) { __hip_atomic_store(g, v, __ATOMIC_RELAXED, __HIP_MEMORY_SCOPE_AGENT); }
__device__ __forceinline__ void put(gu64* g, u32 tag, float v) { put64(g, ((u64)tag << 32) | (u64)__float_as_uint(v)); }
__device__ __forceinline__ u64 get(const gu64* g) { return __hip_atomic_load(g, __ATOMIC_RELAXED, __HIP_MEMORY_SCOPE_AGENT); }
__device__ __forceinline__ void put32(gu32* g, u32 v) { __hip_atomic_store(g, v, __ATOMIC_RELAXED, __HIP_MEMORY_SCOPE_AGENT); }
__device__ __forceinline__ u32 get32(const gu32* g) { return __hip_atomic_load(g, __ATOMIC_RELAXED, __HIP_MEMORY_SCOPE_AGENT); }
__device__ __forceinline__ bool tag_ok(u64 x, u32 tag) { return (u32)(x >> 32) == tag; }
__device__ __forceinline__ float val(u64 x) { return __uint_as_float((u32)x); }

template <int LN> __device__ __forceinline__ void gemv_step(float (&acc)[8], float xv, u32x4 w) {
  const float xb = dpp<BCAST + LN>(xv);
#pragma unroll
  for (int k = 0; k < 4; k++) { acc[2 * k] = fmaf(xb, bf_lo(w[k]), acc[2 * k]); acc[2 * k + 1] = fmaf(xb, bf_hi(w[k]), acc[2 * k + 1]); }
}
template <int LN, int ST> __device__ __forceinline__ void gemv4(float (&acc)[8], float xv, u32x4 (&w)[4]) {
  gemv_step<LN>(acc, xv, w[0]); gemv_step<LN + ST>(acc, xv, w[1]); gemv_step<LN + 2 * ST>(acc, xv, w[2]); gemv_step<LN + 3 * ST>(acc, xv, w[3]);
}
template <int LN> __device__ __forceinline__ void gemv4_bf(float (&acc)[8], float xv, u32x4 (&w)[4]) {
  const int hi = __float_as_int(xv), lo = (int)((u32)hi >> 16);
  auto one = [&](u32 xl, u32 xh, u32x4 v) {
#pragma unroll
    for (int k = 0; k < 4; k++) { acc[2 * k] = dot2(xl, v[k], acc[2 * k]); acc[2 * k + 1] = dot2(xh, v[k], acc[2 * k + 1]); }
  };
  one(dppi<BCAST + LN>(lo), dppi<BCAST + LN>(hi), w[0]); one(dppi<BCAST + LN + 1>(lo), dppi<BCAST + LN + 1>(hi), w[1]);
  one(dppi<BCAST + LN + 2>(lo), dppi<BCAST + LN + 2>(hi), w[2]); one(dppi<BCAST + LN + 3>(lo), dppi<BCAST + LN + 3>(hi), w[3]);
}
__device__ __forceinline__ void gemv8(float (&acc)[8], const float* xv, u32x4 (&w)[4]) {
#pragma unroll
  for (int i = 0; i < 4; i++)
#pragma unroll
    for (int k = 0; k < 4; k++) { acc[2 * k] = fmaf(xv[i], bf_lo(w[i][k]), acc[2 * k]); acc[2 * k + 1] = fmaf(xv[i], bf_hi(w[i][k]), acc[2 * k + 1]); }
}

__device__ __forceinline__ void warm_code(int lane) {
  u64 pc; u32 back, n0, n1;
  asm volatile("s_getpc_b64 %0\n"
               "fd_here:\n\t"
               "s_mov_b32 %1, fd_here - fd_code_begin\n\t"
               "s_mov_b32 %2, fd_code_stream_end - fd_code_begin\n\t"
               "s_mov_b32 %3, fd_code_helper_end - fd_code_begin"
               : "=s"(pc), "=s"(back), "=s"(n0), "=s"(n1));
  pc -= back;
  const u32 n = max(n0, n1);
  u32 v[5];
#pragma unroll
  for (int k = 0; k < 5; k++) v[k] = *(const GAS u32*)(pc + (min(lane * 64u + k * 4096u, n - 4u) & ~3u));
  asm volatile("" :: "v"(v[0]), "v"(v[1]), "v"(v[2]), "v"(v[3]), "v"(v[4]));
}

struct Lds {
  float y[4][64][64];
  float part[4][128];
  float qp[4][128];
  float op[4][128];
  float h[128];
  float mw[4];
  float fw[4];
  int lag[4];
  u32 shift;
  u32 stop;
  u32 pad[2];
  u32 kdone[4];
  u16 q[128];
  u32 ctl[4];
};

__global__ void __launch_bounds__(320, 2)
fused_decode(const u16* __restrict__ x, const u16* __restrict__ kc, const u16* __restrict__ vc, const u16* __restrict__ wq,
             const u16* __restrict__ wo, const u16* __restrict__ wg, const u16* __restrict__ wu, const u16* __restrict__ wd,
             u16* __restrict__ out, unsigned char* __restrict__ ws) {
  __shared__ __attribute__((aligned(16))) Lds L;
  const u32 tstart = (u32)__builtin_amdgcn_s_memrealtime();
  asm volatile("fd_code_begin:" :: "s"(x), "s"(kc), "s"(vc), "s"(wq), "s"(wo), "s"(wg), "s"(wu), "s"(wd), "s"(out), "s"(ws));
  const int b = blockIdx.x, head = b >> 3, j = b & 7;
  const int lane = threadIdx.x & 63, wave = __builtin_amdgcn_readfirstlane(threadIdx.x >> 6);
  const int r = lane >> 4, c = lane & 15;
  const bool has_ffn = j != (head & 7);
  const int ks = j >> 1, jb = j & 1, hr = head & 7;
  const int cg = 2 * head + jb - 2 * (head >> 3) - ((hr > 2 * ks || (hr == 2 * ks && jb)) ? 1 : 0) - (hr > 2 * ks + 1 ? 1 : 0);
  const int dg = head - ((head - j + 7) >> 3);

  if (wave >= 4) {
    const u32 ep = get32((gu32*)(ws + WS_EPOCH));
    const int fin = (head >> 2) * 8 + j;
    const u32 xfin = ((const GAS u16*)x)[16 * fin + c];
    if (head < 4) warm_code(lane);
    const u32 tag = __builtin_amdgcn_readfirstlane(ep) + 1u;
    gu64* qrec = (gu64*)(ws + WS_QREC);
    if (lane == 0) put(qrec + (size_t)b * 80 + 64, tag, __uint_as_float(tstart));
    if (lane < 4) { L.kdone[lane] = ~tag; L.lag[lane] = 0; }
    if (lane == 0) { L.ctl[2] = ~tag; L.stop = ~tag; L.shift = 0; }
    wg_barrier();
    {
      put(qrec + (size_t)b * 80 + lane, tag, (L.qp[0][lane] + L.qp[1][lane]) + (L.qp[2][lane] + L.qp[3][lane]));
      const gu64* qr = qrec + (size_t)head * 8 * 80;
      __builtin_amdgcn_s_sleep(8);
      u64 g[9];
      for (u32 spin = 0;; spin++) {
#pragma unroll
        for (int k = 0; k < 8; k++) g[k] = get(qr + k * 80 + lane);
        g[8] = get(qr + (lane & 7) * 80 + 64);
        bool ok = true;
#pragma unroll
        for (int k = 0; k < 9; k++) ok &= tag_ok(g[k], tag);
        if (__all(ok) || spin > SPIN_CAP) break;
      }
      L.q[lane] = bf_bits((val(g[0]) + val(g[2])) + (val(g[4]) + val(g[6])));
      L.q[64 + lane] = bf_bits((val(g[1]) + val(g[3])) + (val(g[5]) + val(g[7])));
      int tm = (int)((u32)g[8] - tstart);
      tm = min(tm, dppi<XOR1>(tm)); tm = min(tm, dppi<XOR2>(tm)); tm = min(tm, dppi<HMIRROR>(tm));
      if (lane == 0) { L.ctl[0] = tag; L.ctl[1] = tstart + (u32)min(tm, 0); }
    }
    wg_barrier();
    {
      const int e = lane & 1, s = (lane >> 1) & 3;
      const u16* p = s == 0 ? vc + (size_t)head * SK * DH + (size_t)j * 4096 * DH + e * (4096 * DH - 2) :
                     s == 1 ? wo + (size_t)(head * 128 + e * 127) * D + j * 128 :
                     s == 2 ? wd + (size_t)((has_ffn ? dg : 0) * 128 + e * 127) * D + j * 128 :
                     (head & 3) == 0 ? out + 16 * fin : x;
      const u32 t = __builtin_nontemporal_load((const GAS u32*)p);
      asm volatile("" :: "v"(t));
    }
    if (has_ffn) {
      const int unit = cg * 4 + ks;
      gu64* gu = (gu64*)(ws + WS_GU);
      const f32x2 mine = {(L.part[0][lane] + L.part[1][lane]) + (L.part[2][lane] + L.part[3][lane]),
                          (L.part[0][64 + lane] + L.part[1][64 + lane]) + (L.part[2][64 + lane] + L.part[3][64 + lane])};
      put64(gu + unit * 64 + lane, __builtin_bit_cast(u64, mine));
      drain();
      if (lane == 0) put32((gu32*)(ws + WS_FLAG + unit * 64), tag);
      const gu32* fl = (const gu32*)(ws + WS_FLAG + (dg * 8 + (lane & 7)) * 64);
      __builtin_amdgcn_s_sleep(64);
      for (u32 spin = 0; !__all(get32(fl) == tag) && spin < SPIN_CAP; spin++) __builtin_amdgcn_s_sleep(32);
      __builtin_amdgcn_fence(__ATOMIC_ACQUIRE, "wavefront");
      u64 pl[8];
#pragma unroll
      for (int k = 0; k < 8; k++) pl[k] = get(gu + (dg * 8 + k) * 64 + lane);
      asm volatile("" : "+v"(pl[0]), "+v"(pl[1]), "+v"(pl[2]), "+v"(pl[3]), "+v"(pl[4]), "+v"(pl[5]), "+v"(pl[6]), "+v"(pl[7]));
      auto pair = [&](int k) { return __builtin_bit_cast(f32x2, pl[k]); };
      const f32x2 lo = (pair(0) + pair(1)) + (pair(2) + pair(3)), hi = (pair(4) + pair(5)) + (pair(6) + pair(7));
      auto act = [](float g, float u) {
        g = rbf(g); u = rbf(u);
        const float inner = 0.7978845608028654f * (g + 0.044715f * g * g * g);
        return rbf(rbf(0.5f * g * (1.f + tanhf(inner))) * u);
      };
      L.h[lane] = act(lo.x, lo.y); L.h[64 + lane] = act(hi.x, hi.y);
    }
    int shift = 0, told = 0;
    auto steer_once = [&]() {
      gu64* lg = (gu64*)(ws + WS_LAG) + head * 16;
      int a = (int)lds_get((const u32*)&L.lag[lane & 3]);
      a += dppi<XOR1>(a); a += dppi<XOR2>(a);
      a = __builtin_amdgcn_readfirstlane(a) >> 2;
      if (a > max(told, shift) + STEER_LATE) { told = a; if (lane == 0) put(lg + j, tag, __int_as_float(a)); }
      __builtin_amdgcn_s_sleep(14);
      const u64 g = get(lg + (lane & 7));
      int late = tag_ok(g, tag) ? (int)(u32)g : 0;
      late = max(late, dppi<XOR1>(late)); late = max(late, dppi<XOR2>(late)); late = max(late, dppi<HMIRROR>(late));
      late = min(__builtin_amdgcn_readfirstlane(late) - STEER_KEEP, STEER_MAX);
      if (late > shift) { shift = late; if (lane == 0) lds_put(&L.shift, (u32)shift); }
    };
    auto steer = [&](const u32* until) {
#pragma unroll 1
      for (u32 it = 0; it < 8192 && !__all(lds_get(until) == tag); it++) steer_once();
    };
#pragma unroll 1
    while ((int)((u32)__builtin_amdgcn_s_memrealtime() - tstart) < STEER_FROM) __builtin_amdgcn_s_sleep(16);
    steer(&L.kdone[lane & 3]);
    {
#pragma unroll 1
      for (u32 spin = 0; !__all(lds_get(&L.kdone[lane & 3]) == tag) && spin < 64 * SPIN_CAP; spin++) __builtin_amdgcn_s_sleep(16);
      asm volatile("" ::: "memory");
      const float mine = L.mw[lane & 3];
      float mc = fmaxf(mine, dpp<XOR1>(mine)); mc = fmaxf(mc, dpp<XOR2>(mc));
      float s = 0.f;
      const float* yy = &L.y[0][0][lane];
#pragma unroll 8
      for (int t = 0; t < 256; t++) s += __builtin_amdgcn_exp2f(yy[t * 64] - mc);
      s = wave_sum(s) * 0.25f;
      gu64* ml = (gu64*)(ws + WS_ML);
      if (lane < 2) put(ml + b * 16 + lane, tag, lane == 0 ? mc : s);
#pragma unroll 1
      for (int k = 0; k < 5; k++) steer_once();
      u64 g0, g1;
      for (u32 spin = 0;; spin++) {
        g0 = get(ml + (head * 8 + (lane & 7)) * 16); g1 = get(ml + (head * 8 + (lane & 7)) * 16 + 1);
        if (__all(tag_ok(g0, tag) & tag_ok(g1, tag)) || spin > SPIN_CAP) break;
        steer_once();
      }
      float M = val(g0);
      M = fmaxf(M, dpp<XOR1>(M)); M = fmaxf(M, dpp<XOR2>(M)); M = fmaxf(M, dpp<HMIRROR>(M));
      float t = val(g1) * __builtin_amdgcn_exp2f(val(g0) - M);
      t += dpp<XOR1>(t); t += dpp<XOR2>(t); t += dpp<HMIRROR>(t);
      if (lane < 4) L.fw[lane] = __builtin_amdgcn_exp2f(mine - M) / t;
      lds_done();
      if (lane == 0) lds_put(&L.ctl[2], tag);
    }
    steer(&L.stop);
    wg_barrier();
    {
      gu64* s1 = (gu64*)(ws + WS_S1) + (size_t)b * 128;
      put(s1 + lane, tag, (L.op[0][lane] + L.op[1][lane]) + (L.op[2][lane] + L.op[3][lane]));
      put(s1 + 64 + lane, tag, (L.op[0][64 + lane] + L.op[1][64 + lane]) + (L.op[2][64 + lane] + L.op[3][64 + lane]));
    }
    wg_barrier();
    gu64* s2 = (gu64*)(ws + WS_S2);
    put(s2 + (size_t)b * 128 + lane, tag, (L.qp[0][lane] + L.qp[1][lane]) + (L.qp[2][lane] + L.qp[3][lane]));
    put(s2 + (size_t)b * 128 + 64 + lane, tag, (L.qp[0][64 + lane] + L.qp[1][64 + lane]) + (L.qp[2][64 + lane] + L.qp[3][64 + lane]));
    if ((head & 3) == 0) {
      const gu64* sr = s2 + (size_t)(r * 8 + (fin >> 3)) * 128 + (fin & 7) * 16 + c;
      u64 g[8];
      __builtin_amdgcn_s_sleep(16);
      for (u32 spin = 0;; spin++) {
        bool ok = true;
#pragma unroll
        for (int k = 0; k < 8; k++) g[k] = get(sr + k * 4 * 8 * 128);
#pragma unroll
        for (int k = 0; k < 8; k++) ok &= tag_ok(g[k], tag);
        if (__all(ok) || spin > SPIN_CAP) break;
      }
      float a = ((val(g[0]) + val(g[1])) + (val(g[2]) + val(g[3]))) + ((val(g[4]) + val(g[5])) + (val(g[6]) + val(g[7])));
      a = xrow_sum(a) + bf_lo(xfin);
      if (lane < 16) ((GAS u16*)out)[16 * fin + lane] = bf_bits(a);
      if (b == 0 && lane == 0) put32((gu32*)(ws + WS_EPOCH), tag);
    }
    asm volatile("fd_code_helper_end:");
  } else {
    const int w = wave;
    u32x4 X0, X1, XS;
    u32x4 P0[4], P1[4], P2[4], P3[4], R0[4], R1[4], R2[4], R3[4];
    const i32x4 rx = mkr(x, D * 2);
    const u32 voff = lane * 16;
    const int r8 = lane >> 3, c8 = lane & 7;
    const u32 row0 = ks * 256 + 64 * w;
    {
      const u32 so = (row0 + 8 * r8) * 2;
      asm volatile("buffer_load_dwordx4 %0, %1, %2, 0 offen" : "=v"(X0) : "v"(voff), "s"(rx));
      asm volatile("buffer_load_dwordx4 %0, %1, %2, 0 offen offset:1024" : "=v"(X1) : "v"(voff), "s"(rx));
      asm volatile("buffer_load_dwordx4 %0, %1, %2, 0 offen" : "=v"(XS) : "v"(so), "s"(rx));
    }
    {
      const i32x4 rq = mkr(wq, (u32)D * 4096 * 2);
      const u32 vo = r8 * 8 * 8192 + c8 * 16, s0 = row0 * 8192 + head * 256 + jb * 128;
      rows4_o(P0, vo, rq, s0, 8192); rows4_o(P1, vo, rq, s0 + 4 * 8192, 8192);
    }
    const u32 nffn = has_ffn ? (u32)D * FF * 2 : 0u;
    const i32x4 rg = mkr(wg, nffn), ru = mkr(wu, nffn);
    constexpr u32 GSTEP = FF * 2;
    const u32 vg = r8 * 8 * (FF * 2) + c8 * 16, g0 = row0 * (FF * 2) + cg * 128;
    const i32x4 rk = mkr((const char*)kc + (size_t)head * HEAD_BYTES, HEAD_BYTES), rv = mkr((const char*)vc + (size_t)head * HEAD_BYTES, HEAD_BYTES);
    const u32 t0 = ((u32)j << 20) | ((u32)(w & 1) << 15) | ((u32)(w >> 1) << 11), vhi = voff + 8192;
    auto toff = [&](u32 n) -> u32 { return t0 | ((n & 1u) << 12) | ((n & 2u) << 13) | ((n & 60u) << 14); };
    asm volatile("s_waitcnt vmcnt(8)" : "+v"(X0), "+v"(X1), "+v"(XS));
    u32 touch;
    {
      const int e = lane & 1, sel = (lane >> 1) & 3;
      const size_t gu = (size_t)(ks * 256 + e * 255) * FF + (has_ffn ? cg : 0) * 64;
      const u16* p = sel == 0 ? wg + gu : sel == 1 ? wu + gu : kc + (size_t)head * SK * DH + (size_t)j * 4096 * DH + e * (4096 * DH - 2);
      asm volatile("global_load_dword %0, %1, off nt" : "=v"(touch) : "v"(p));
    }
    float xn[8];
    {
      float v[16];
#pragma unroll
      for (int k = 0; k < 4; k++) { v[2 * k] = bf_lo(X0[k]); v[2 * k + 1] = bf_hi(X0[k]); v[8 + 2 * k] = bf_lo(X1[k]); v[9 + 2 * k] = bf_hi(X1[k]); }
      float s = 0.f;
#pragma unroll
      for (int k = 0; k < 16; k++) s += v[k];
      const float mean = wave_sum(s) * (1.0f / D);
      float q = 0.f;
#pragma unroll
      for (int k = 0; k < 16; k++) { const float d = v[k] - mean; q = fmaf(d, d, q); }
      const float var = wave_sum(q) * (1.0f / D);
      const float den = rbf(sqrtf(rbf(rbf(var) + 1e-5f)));
      const float mb = rbf(mean);
#pragma unroll
      for (int k = 0; k < 4; k++) { xn[2 * k] = rbf(rbf(bf_lo(XS[k]) - mb) / den); xn[2 * k + 1] = rbf(rbf(bf_hi(XS[k]) - mb) / den); }
    }
    pin8(xn);
    const bool b8 = (c & 8) != 0, b4 = (c & 4) != 0;
    {
      float a[8] = {0.f, 0.f, 0.f, 0.f, 0.f, 0.f, 0.f, 0.f};
      wait4<5>(P0); rows4_o(P2, vg, rg, g0, GSTEP); pin4(P0);
      gemv8(a, xn, P0); pin8(a);
      wait4<5>(P1); rows4_o(P3, vg, rg, g0 + 4 * GSTEP, GSTEP); pin4(P1);
      gemv8(a, xn + 4, P1);
      L.qp[w][8 * c8 + r8] = fold8(a, b8);
    }
    wg_barrier();
    {
      float a[8] = {0.f, 0.f, 0.f, 0.f, 0.f, 0.f, 0.f, 0.f};
      wait4<4>(P2); asm volatile("" :: "v"(touch)); rows4_o(P0, vg, ru, g0, GSTEP); pin4(P2);
      gemv8(a, xn, P2); pin8(a);
      wait4<4>(P3); rows4_o(P1, vg, ru, g0 + 4 * GSTEP, GSTEP); pin4(P3);
      gemv8(a, xn + 4, P3);
      L.part[w][8 * c8 + r8] = fold8(a, b8);
    }
    {
      float a[8] = {0.f, 0.f, 0.f, 0.f, 0.f, 0.f, 0.f, 0.f};
      wait4<4>(P0); tile_o(R0, voff, vhi, rk, toff(0)); pin4(P0);
      gemv8(a, xn, P0); pin8(a);
      wait4<4>(P1); tile_o(R1, voff, vhi, rk, toff(1)); pin4(P1);
      gemv8(a, xn + 4, P1);
      L.part[w][64 + 8 * c8 + r8] = fold8(a, b8);
    }
    wait4<4>(R0); tile_o(R2, voff, vhi, rk, toff(2));
    wait4<4>(R1); tile_o(R3, voff, vhi, rk, toff(3));
    wg_barrier();
    const u32x4 q = *(const u32x4*)&L.q[8 * c];
    const u32 tag = __builtin_amdgcn_readfirstlane(L.ctl[0]);
    const u32 tref = __builtin_amdgcn_readfirstlane(L.ctl[1]);

    float m = -INFINITY, acc[8] = {0.f, 0.f, 0.f, 0.f, 0.f, 0.f, 0.f, 0.f};
    u32 due16 = (tref << 4) + PACE_T0 * 16 + w * (PACE_STEP / 4);
    auto clock16 = [&]() -> u32 { return (u32)__builtin_amdgcn_s_memrealtime() << 4; };
    u32 sh = 0;
    auto pace = [&](u32 early, u32 step) {
      u32 now = early;
      if ((int)(early - due16 - sh) < 0) {
#pragma unroll 1
        while ((int)((now = clock16()) - due16 - sh) < 0) __builtin_amdgcn_s_sleep(1);
      }
      if (lane == 0) lds_put((u32*)&L.lag[w], now - due16);
      due16 += step;
    };
    auto kstep = [&](u32x4 (&R)[4], int n) {
      i32x4 rr = rk;
      rr.x = n + 4 < 64 ? rk.x : rv.x; rr.y = n + 4 < 64 ? rk.y : rv.y;
      u32 soff = toff(n + 4);
      u32 early = clock16();
      sh = __builtin_amdgcn_readfirstlane(lds_get(&L.shift));
      asm volatile("" : "+s"(rr), "+s"(soff), "+s"(early), "+s"(sh));
      wait4<12>(R);
      float d[4];
#pragma unroll
      for (int u = 0; u < 4; u++) d[u] = dot2(R[u][3], q[3], dot2(R[u][2], q[2], dot2(R[u][1], q[1], dot2(R[u][0], q[0], 0.f))));
      const float e0 = (b8 ? d[2] : d[0]) + dpp<ROR8>(b8 ? d[0] : d[2]);
      const float e1 = (b8 ? d[3] : d[1]) + dpp<ROR8>(b8 ? d[1] : d[3]);
      float f = (b4 ? e1 : e0) + dpp<HMIRROR>(b4 ? e0 : e1);
      f += dpp<XOR1>(f); f += dpp<XOR2>(f);
      const float y = rbf(rbf(f) * 0.08838834764831845f) * 1.4426950408889634f;
      L.y[w][n][lane] = y;
      m = fmaxf(m, y);
      asm volatile("" : "+v"(m));
      pace(early, n < PACE_NSLOW ? PACE_SLOW : PACE_STEP);
      tile(R, voff, vhi, rr, soff);
    };
    auto vload = [&](int n) -> float { return __builtin_amdgcn_exp2f(L.y[w][n][lane] - m); };
    auto vstep = [&](u32x4 (&R)[4], int n) {
      u32 soff = toff(n + 4);
      u32 early = clock16();
      sh = __builtin_amdgcn_readfirstlane(lds_get(&L.shift));
      float p = vload(n);
      asm volatile("" : "+s"(soff), "+v"(p), "+s"(early), "+s"(sh));
      wait4<12>(R);
      gemv4<0, 4>(acc, p, R);
      pin8(acc);
      pace(early, PACE_STEP);
      tile(R, voff, vhi, rv, soff);
    };
#pragma unroll 1
    for (int n = 0; n < 64; n += 4) { kstep(R0, n); kstep(R1, n + 1); kstep(R2, n + 2); kstep(R3, n + 3); }
    m = fmaxf(m, dpp<ROR4>(m)); m = fmaxf(m, dpp<ROR8>(m)); m = xrow_max(m);
    if (lane == 0) L.mw[w] = m;
    lds_done();
    if (lane == 0) lds_put(&L.kdone[w], tag);
#pragma unroll 1
    for (int n = 0; n < 60; n += 4) {
      vstep(R0, n); vstep(R1, n + 1); vstep(R2, n + 2); vstep(R3, n + 3);
      if (n == 52 && lane == 0) lds_put(&L.stop, tag);
    }
    u32x4 W0[4], W1[4], D0[4], D1[4];
    {
      const float p0 = vload(60), p1 = vload(61), p2 = vload(62), p3 = vload(63);
#pragma unroll 1
      for (u32 spin = 0; lds_get(&L.ctl[2]) != tag && spin < 64 * SPIN_CAP; spin++) __builtin_amdgcn_s_sleep(2);
      asm volatile("" ::: "memory");
      const float fs = L.fw[w];
      wait4<12>(R0); gemv4<0, 4>(acc, p0, R0);
      wait4<8>(R1); gemv4<0, 4>(acc, p1, R1);
      pin8(acc);
      const u32 wvo = (2 * (r & 1) + (r >> 1)) * 2048 + c * 16;
      const i32x4 rwo = mkr(wo, (u32)4096 * D * 2), rd = mkr(wd, has_ffn ? (u32)FF * D * 2 : 0u);
      const u32 wso = (u32)(head * 128 + 32 * w) * 2048 + j * 256, dso = (u32)(dg * 128 + 32 * w) * 2048 + j * 256;
      due16 += w * (PACE_TAIL / 4) - w * (PACE_STEP / 4) - 200;
      pace(due16 - 1, PACE_TAIL); rows4_o(D0, wvo, rd, dso, 8192);
      wait4<8>(R2); gemv4<0, 4>(acc, p2, R2); pin8(acc);
      pace(due16 - 1, PACE_TAIL); rows4_o(D1, wvo, rd, dso + 32768, 8192);
      wait4<8>(R3); gemv4<0, 4>(acc, p3, R3);
      *(f32x2*)&L.op[w][8 * c + 2 * r] = fold_rows(acc) * fs;
      wg_barrier();
      pace(due16 - 1, PACE_TAIL); rows4_o(W0, wvo, rwo, wso, 8192);
      pace(due16 - 1, PACE_TAIL); rows4_o(W1, wvo, rwo, wso + 32768, 8192);
    }
    {
      const float hv = has_ffn ? L.h[32 * w + 4 * (c & 7) + 2 * (r & 1) + (r >> 1)] : 0.f;
      float a[8] = {0.f, 0.f, 0.f, 0.f, 0.f, 0.f, 0.f, 0.f};
      wait4<12>(D0); gemv4_bf<0>(a, hv, D0);
      wait4<8>(D1); gemv4_bf<4>(a, hv, D1);
      pin8(a);
      wait4<0>(W0); wait4<0>(W1);
      const gu64* s1 = (const gu64*)(ws + WS_S1) + (size_t)(head * 8 + (lane >> 5)) * 128 + 32 * w + 4 * (c & 7) + 2 * (r & 1) + (c >> 3);
      u64 g[4];
      for (u32 spin = 0;; spin++) {
#pragma unroll
        for (int k = 0; k < 4; k++) g[k] = get(s1 + k * 256);
        if (__all(tag_ok(g[0], tag) & tag_ok(g[1], tag) & tag_ok(g[2], tag) & tag_ok(g[3], tag)) || spin > SPIN_CAP) break;
      }
      float o = (val(g[0]) + val(g[1])) + (val(g[2]) + val(g[3]));
      o = rbf(swap32_add(o, o));
      const float orot = dpp<ROR8>(o);
      o = (r & 2) ? orot : o;
      gemv4_bf<0>(a, o, W0); gemv4_bf<4>(a, o, W1);
      *(f32x2*)&L.qp[w][8 * c + 2 * r] = fold_rows(a);
    }
    wg_barrier();
    asm volatile("fd_code_stream_end:");
  }
}

}

extern "C" void kernel_launch(void* const* d_in, const int* in_sizes, int n_in, void* d_out, int out_size, void* d_ws, size_t ws_size,
                              hipStream_t stream) {
  (void)in_sizes; (void)n_in; (void)out_size; (void)ws_size;
  fused_decode<<<256, 320, 0, stream>>>((const u16*)d_in[0], (const u16*)d_in[1], (const u16*)d_in[2], (const u16*)d_in[3], (const u16*)d_in[4],
                                        (const u16*)d_in[6], (const u16*)d_in[7], (const u16*)d_in[8], (u16*)d_out, (unsigned char*)d_ws);
}
```
